```python
import torch

def _softmax(x, axis=-1):
    return torch.softmax(x, dim=axis)
def _gelu(x):
    return torch.nn.functional.gelu(x, approximate="tanh")
def _silu(x):
    return torch.nn.functional.silu(x)
def _sigmoid(x):
    return torch.sigmoid(x)
def _layernorm(x, axis=-1, eps=1e-5):
    m = x.mean(dim=axis, keepdim=True)
    v = x.var(dim=axis, keepdim=True, unbiased=False)
    return (x - m) / torch.sqrt(v + eps)
def _scatter_add(tgt, idx, src, axis=0):
    return tgt.clone().index_add_(axis, idx, src)
def _segment_sum(src, seg, n):
    out = torch.zeros((n,) + src.shape[1:], dtype=src.dtype)
    return out.index_add_(0, seg, src)
def _pad(x, pw):
    flat = tuple(v for pair in reversed(pw) for v in pair)
    return torch.nn.functional.pad(x, flat)

kernel_name = 'sequential_preLN__mha_block__mixer_s_mlp__prompt'

def setup_inputs():

    _w = (lambda a: (lambda t: t.to(torch.bfloat16) if t.is_floating_point() else t)(torch.as_tensor(a)))
    def _A_setup():
        B, S, D, H = 1, 2048, 512, 32
        return {
            "x": _w((lambda *s: torch.randn(*s, dtype=torch.bfloat16) * 0.05)(B, S, D)),
            "wqkv": _w((lambda *s: torch.randn(*s, dtype=torch.bfloat16) * 0.05)(D, 3 * D)),
            "wo": _w((lambda *s: torch.randn(*s, dtype=torch.bfloat16) * 0.05)(D, D)),
        }
    _A_in = _A_setup()

    def _B_setup():
        B, S, D, F = 1, 2048, 512, 1792
        return {
            "x":  _w((lambda *s: torch.randn(*s, dtype=torch.bfloat16) * 0.05)(B, S, D)),
            "w1": _w((lambda *s: torch.randn(*s, dtype=torch.bfloat16) * 3.2)(D, D) * (D ** -0.5)),
            "w2": _w((lambda *s: torch.randn(*s, dtype=torch.bfloat16) * 3.2)(D, D) * (D ** -0.5)),
            "w3": _w((lambda *s: torch.randn(*s, dtype=torch.bfloat16) * 3.2)(D, F) * (D ** -0.5)),
            "w4": _w((lambda *s: torch.randn(*s, dtype=torch.bfloat16) * 3.2)(F, D) * (F ** -0.5)),
        }
    _B_in = _B_setup()

    return {
        "A_x": _A_in["x"],
        "A_wqkv": _A_in["wqkv"],
        "A_wo": _A_in["wo"],
        "B_x": _B_in["x"],
        "B_w1": _B_in["w1"],
        "B_w2": _B_in["w2"],
        "B_w3": _B_in["w3"],
        "B_w4": _B_in["w4"],
    }

def reference(A_x, A_wqkv, A_wo, B_x, B_w1, B_w2, B_w3, B_w4):
    def _A_ref(x, wqkv, wo):
        B, S, D = x.shape
        H = 32
        Dh = D // H
        qkv = (lambda x, *d: x.permute(*d))((x @ wqkv).reshape(B, S, 3, H, Dh), 2, 0, 3, 1, 4)
        q, k, v = qkv[0], qkv[1], qkv[2]
        scores = torch.einsum("bhqd,bhkd->bhqk", q, k) * (Dh ** -0.5)
        p = _softmax(scores, axis=-1)
        o = torch.einsum("bhqk,bhkd->bhqd", p, v)
        return (lambda x, *d: x.permute(*d))(o, 0, 2, 1, 3).reshape(B, S, D) @ wo

    def _B_ref(x, w1, w2, w3, w4):
        B, S, D = x.shape
        h = _gelu(_layernorm(x, axis=-1) @ w1)
        c = D // 4
        g0 = _pad(h[:, :, 0*c:1*c], ((0, 0), (1, 0), (0, 0)))[:, :S, :]
        g1 = _pad(h[:, :, 1*c:2*c], ((0, 0), (0, 1), (0, 0)))[:, 1:, :]
        g2 = _pad(h[:, :, 2*c:3*c], ((0, 0), (2, 0), (0, 0)))[:, :S, :]
        g3 = _pad(h[:, :, 3*c:   ], ((0, 0), (0, 2), (0, 0)))[:, 2:, :]
        hs = torch.concatenate([g0, g1, g2, g3], axis=-1) @ w2
        x1 = x + hs
        h2 = _gelu(_layernorm(x1, axis=-1) @ w3) @ w4
        return x1 + h2

    x = A_x
    h = x + _A_ref(_layernorm(x, axis=-1), A_wqkv, A_wo)
    h2 = h + _B_ref(_layernorm(h, axis=-1), B_w1, B_w2, B_w3, B_w4)
    return h2
```

```cpp
#include <hip/hip_runtime.h>
#include <hip/hip_bf16.h>
#include <stdint.h>
typedef unsigned short u16;
__device__ __forceinline__ float bf2f(u16 b){ return __uint_as_float(((unsigned)b)<<16); }
__device__ __forceinline__ u16 f2bf(float f){ unsigned u=__float_as_uint(f); u += 0x7fffu + ((u>>16)&1u); return (u16)(u>>16); }
constexpr int S=2048, D=512, H=32, DH=16, F=1792;

__global__ void ln_kernel(const u16* x, u16* y){
  int row=blockIdx.x; int t=threadIdx.x;
  float v[8]; float s=0;
  for(int i=0;i<8;i++){ v[i]=bf2f(x[row*D+t*8+i]); s+=v[i]; }
  for(int o=32;o>0;o>>=1) s+=__shfl_xor(s,o);
  float m=s/D; float q=0;
  for(int i=0;i<8;i++){ float d=v[i]-m; q+=d*d; }
  for(int o=32;o>0;o>>=1) q+=__shfl_xor(q,o);
  float r=rsqrtf(q/D+1e-5f);
  for(int i=0;i<8;i++) y[row*D+t*8+i]=f2bf((v[i]-m)*r);
}
__device__ __forceinline__ float gelu(float x){ float u=0.7978845608028654f*(x+0.044715f*x*x*x); return 0.5f*x*(1.f+tanhf(u)); }
__global__ void gemm_kernel(const u16* A, const u16* B, u16* C, int N, int K, int mode, const u16* r1, const u16* r2, int shiftA){
  int n=blockIdx.x*64+threadIdx.x; int m=blockIdx.y;
  float acc=0;
  for(int k=0;k<K;k++){
    int src=m;
    if(shiftA){ int g=k>>7; src = g==0? m-1 : g==1? m+1 : g==2? m-2 : m+2; }
    float a = (src>=0 && src<S)? bf2f(A[src*K+k]) : 0.f;
    acc += a*bf2f(B[k*N+n]);
  }
  if(mode==1) acc=gelu(bf2f(f2bf(acc)));
  if(mode>=2) acc = bf2f(f2bf(acc)) + bf2f(r1[m*N+n]);
  if(mode==3) acc = bf2f(f2bf(acc)) + bf2f(r2[m*N+n]);
  C[m*N+n]=f2bf(acc);
}
__global__ void attn_kernel(const u16* qkv, u16* o){
  int h=blockIdx.y; int q=blockIdx.x*64+threadIdx.x;
  float qv[16]; for(int d=0;d<16;d++) qv[d]=bf2f(qkv[q*1536+h*16+d])*0.25f;
  float m=-1e30f,l=0,acc[16]; for(int d=0;d<16;d++) acc[d]=0;
  for(int k=0;k<S;k++){
    float s=0; for(int d=0;d<16;d++) s+=qv[d]*bf2f(qkv[k*1536+512+h*16+d]);
    float mn=fmaxf(m,s); float a=__expf(m-mn), p=__expf(s-mn); l=l*a+p;
    for(int d=0;d<16;d++) acc[d]=acc[d]*a+p*bf2f(qkv[k*1536+1024+h*16+d]);
    m=mn;
  }
  for(int d=0;d<16;d++) o[q*D+h*16+d]=f2bf(acc[d]/l);
}
extern "C" void kernel_launch(void* const* d_in, const int* in_sizes, int n_in, void* d_out, int out_size, void* d_ws, size_t ws_size, hipStream_t stream){
  const u16* x=(const u16*)d_in[0]; const u16* wqkv=(const u16*)d_in[1]; const u16* wo=(const u16*)d_in[2];
  const u16* w1=(const u16*)d_in[4]; const u16* w2=(const u16*)d_in[5]; const u16* w3=(const u16*)d_in[6]; const u16* w4=(const u16*)d_in[7];
  u16* ws=(u16*)d_ws; u16* xn=ws; u16* qkv=xn+S*D; u16* o=qkv+S*1536; u16* h=o+S*D; u16* y=h+S*D; u16* yn=y+S*D; u16* g=yn+S*D; u16* x1=g+S*D; u16* x1n=x1+S*D; u16* hid=x1n+S*D;
  ln_kernel<<<S,64,0,stream>>>(x,xn);
  gemm_kernel<<<dim3(1536/64,S),64,0,stream>>>(xn,wqkv,qkv,1536,512,0,nullptr,nullptr,0);
  attn_kernel<<<dim3(S/64,H),64,0,stream>>>(qkv,o);
  gemm_kernel<<<dim3(512/64,S),64,0,stream>>>(o,wo,h,512,512,2,x,nullptr,0);
  ln_kernel<<<S,64,0,stream>>>(h,y);
  ln_kernel<<<S,64,0,stream>>>(y,yn);
  gemm_kernel<<<dim3(512/64,S),64,0,stream>>>(yn,w1,g,512,512,1,nullptr,nullptr,0);
  gemm_kernel<<<dim3(512/64,S),64,0,stream>>>(g,w2,x1,512,512,2,y,nullptr,1);
  ln_kernel<<<S,64,0,stream>>>(x1,x1n);
  gemm_kernel<<<dim3(F/64,S),64,0,stream>>>(x1n,w3,hid,F,512,1,nullptr,nullptr,0);
  gemm_kernel<<<dim3(512/64,S),64,0,stream>>>(hid,w4,(u16*)d_out,512,F,3,x1,h,0);
}
```

```cpp
#include <hip/hip_runtime.h>
#include <stdint.h>

typedef unsigned short u16;
typedef __attribute__((ext_vector_type(8))) short bf16x8;
typedef __attribute__((ext_vector_type(4))) short s16x4;
typedef __attribute__((ext_vector_type(8))) __bf16 bf8_t;
typedef __attribute__((ext_vector_type(16))) __bf16 bf16_t;
typedef __attribute__((ext_vector_type(2))) __bf16 bf2_t;
typedef __attribute__((ext_vector_type(16))) float f32x16;
typedef __attribute__((ext_vector_type(8))) float f32x8;
typedef __attribute__((ext_vector_type(4))) float f32x4;
typedef __attribute__((ext_vector_type(2))) float f32x2;
typedef __attribute__((ext_vector_type(4))) unsigned u32x4;
typedef __attribute__((ext_vector_type(2))) unsigned u32x2;
typedef __amdgpu_buffer_rsrc_t rsrc_t;
#define LAS __attribute__((address_space(3)))
#define DI __device__ __forceinline__
#define RLX_AGENT __ATOMIC_RELAXED, __HIP_MEMORY_SCOPE_AGENT

constexpr int S = 2048, D = 512, F = 1792;
constexpr float EPS = 1e-5f;
constexpr size_t WS_FLAG = 0;
constexpr size_t WS_KMAX = 65536;
constexpr size_t WS_CS   = WS_KMAX + 8192;
constexpr size_t WS_STH  = WS_CS + 8 * 2304 * 4;
constexpr size_t WS_STX  = WS_STH + 262144;
constexpr size_t WS_Q    = 1u << 20;
constexpr size_t WS_K    = WS_Q + (2u << 20);
constexpr size_t WS_V    = WS_K + (2u << 20);
constexpr size_t WS_O    = WS_V + (2u << 20);
constexpr size_t WS_H    = WS_O + (2u << 20);
constexpr size_t WS_G    = WS_H + (2u << 20);
constexpr size_t WS_X1   = WS_G + (2u << 20);
constexpr size_t WS_U    = WS_X1 + (2u << 20);
constexpr size_t WS_END  = WS_U + 32u * 224 * 1024;
constexpr int FLAG_O = 0, FLAG_H = 2048, FLAG_G = 4096, FLAG_X = 6144, FLAG_U = 8192, FLAG_WORDS = 10240;

constexpr int LX = 0, LY = 65536, LZ = 131072, LDS_BYTES = 163840;
constexpr int LZ_ONES = LZ, LZ_CS1 = LZ + 4608, LZ_CS3 = LZ_CS1 + 256, LZ_END = LZ_CS3 + 896;

DI rsrc_t mkrsrc(const void* p, unsigned bytes) { return __builtin_amdgcn_make_buffer_rsrc((void*)p, 0, bytes, 0x00020000); }
DI unsigned rfl(unsigned v) { return __builtin_amdgcn_readfirstlane(v); }
#define MFMA32(a, b, c) __builtin_amdgcn_mfma_f32_32x32x16_bf16((a), (b), (c), 0, 0, 0)
#define WAITV(n) asm volatile("s_waitcnt vmcnt(" #n ")" ::: "memory")
#define BAR() asm volatile("s_waitcnt lgkmcnt(0)\n\ts_barrier" ::: "memory")
template <int N> DI void waitv() { asm volatile("s_waitcnt vmcnt(%0)" ::"i"(N) : "memory"); }

template <int OFF, bool SC1> DI void dma16(rsrc_t rs, unsigned voff, unsigned soff, unsigned lds) {
  if constexpr (SC1) asm volatile("s_mov_b32 m0, %0\n\ts_nop 0\n\tbuffer_load_dwordx4 %1, %2, %3 offen offset:%4 sc1 lds" ::"s"(lds), "v"(voff), "s"(rs), "s"(soff), "i"(OFF) : "memory");
  else asm volatile("s_mov_b32 m0, %0\n\ts_nop 0\n\tbuffer_load_dwordx4 %1, %2, %3 offen offset:%4 lds" ::"s"(lds), "v"(voff), "s"(rs), "s"(soff), "i"(OFF) : "memory");
}
DI bf16x8 lds128(unsigned a) { return *(const LAS bf16x8*)(uintptr_t)a; }
DI f32x4 lds128f(unsigned a) { return *(const LAS f32x4*)(uintptr_t)a; }
DI s16x4 ldstr(unsigned a) { return __builtin_amdgcn_ds_read_tr16_b64_v4i16((LAS s16x4*)(uintptr_t)a); }
DI bf16x8 trfrag(unsigned a0, unsigned a1) { s16x4 lo = ldstr(a0), hi = ldstr(a1); return __builtin_shufflevector(lo, hi, 0, 1, 2, 3, 4, 5, 6, 7); }
DI unsigned pk2(float lo, float hi) { f32x2 v = {lo, hi}; bf2_t r = __builtin_convertvector(v, bf2_t); return __builtin_bit_cast(unsigned, r); }
DI float bflo(unsigned u) { return __uint_as_float(u << 16); }
DI float bfhi(unsigned u) { return __uint_as_float(u & 0xffff0000u); }
DI float swapadd(float v) { auto r = __builtin_amdgcn_permlane32_swap(__float_as_uint(v), __float_as_uint(v), false, false); return __uint_as_float(r[0]) + __uint_as_float(r[1]); }
DI float swapmax(float v) { auto r = __builtin_amdgcn_permlane32_swap(__float_as_uint(v), __float_as_uint(v), false, false); return fmaxf(__uint_as_float(r[0]), __uint_as_float(r[1])); }
DI float dot2(unsigned a, unsigned b, float c) { return __builtin_amdgcn_fdot2_f32_bf16(__builtin_bit_cast(bf2_t, a), __builtin_bit_cast(bf2_t, b), c, false); }
DI float gelu(float x) {
  const float t = x * __builtin_fmaf(x * x, -0.10294324f, -2.3022082f);
  return x * __builtin_amdgcn_rcpf(1.0f + __builtin_amdgcn_exp2f(t));
}
DI void st128_sc1(rsrc_t rs, unsigned off, u32x4 v) { __builtin_amdgcn_raw_buffer_store_b128(v, rs, off, 0, 16); }
DI u32x4 pack8(const f32x16& a, int o) { u32x4 p; p[0] = pk2(a[o], a[o + 1]); p[1] = pk2(a[o + 2], a[o + 3]); p[2] = pk2(a[o + 4], a[o + 5]); p[3] = pk2(a[o + 6], a[o + 7]); return p; }

__global__ void __launch_bounds__(512, 2) k1_qkv(const u16* __restrict__ x, const u16* __restrict__ wqkv, const u16* __restrict__ w1,
                                                 const u16* __restrict__ w3, char* __restrict__ ws) {
  extern __shared__ __attribute__((aligned(16))) char smem[];
  const unsigned lds0 = (unsigned)(uintptr_t)smem;
  const int b = blockIdx.x, tid = threadIdx.x, l = tid & 63;
  const unsigned w = rfl(tid >> 6);
  const int xcd = b & 7, r = b >> 3;
  const int rb = (xcd >> 1) * 8 + (r >> 2), cb = (xcd & 1) * 4 + (r & 3);
  const int h = l >> 5, l31 = l & 31;
  constexpr int SLOT = 32768, NS = 5;
  if (w >= 4) {
    const unsigned iw = w - 4;
    const rsrc_t rx = mkrsrc(x, S * D * 2), rw = mkrsrc(wqkv, D * 1536 * 2);
    const unsigned vx0 = (l >> 3) * 1024 + (((l & 7) ^ (l >> 4)) << 4), vx1 = (l >> 3) * 1024 + (((l & 7) ^ (4 + (l >> 4))) << 4);
    const unsigned vw = (l >> 3) * 3072 + (((l & 7) ^ (((l >> 4) & 1) << 2)) << 4);
    auto issue = [&](int c, int slot) {
      const unsigned lb = lds0 + slot * SLOT;
      dma16<0, false>(rx, vx0, (rb * 64 + iw * 16) * 1024 + c * 128, lb + iw * 2048);
      dma16<0, false>(rx, vx1, (rb * 64 + iw * 16 + 8) * 1024 + c * 128, lb + iw * 2048 + 1024);
#pragma unroll
      for (int i = 0; i < 6; ++i) {
        const unsigned pid = iw * 6 + i, p = pid >> 3, g = pid & 7;
        dma16<0, false>(rw, vw, (c * 64 + g * 8) * 3072 + p * 1024 + cb * 128, lb + 8192 + pid * 1024);
      }
    };
#pragma unroll
    for (int c = 0; c < NS; ++c) issue(c, c);
    const int unit = b + 256 * (int)iw;
    u32x4 sv[8];
    const bool side = unit < 288;
    int scol = 0, skp = 0;
    if (side) {
      skp = unit & 7; const int cg = unit >> 3;
      const u16* src = cg < 8 ? w1 + cg * 64 : w3 + (cg - 8) * 64; const int pitch = cg < 8 ? D : F;
      scol = cg * 64;
#pragma unroll
      for (int i = 0; i < 8; ++i) sv[i] = *(const u32x4*)(src + (size_t)(skp * 64 + i * 8 + (l >> 3)) * pitch + (l & 7) * 8);
    }
    if (iw == 3 && l < 40) ((unsigned*)(ws + WS_FLAG))[b * 40 + l] = 0u;
#pragma unroll
    for (int c = 0; c < 8; ++c) {
      if (c == 0) waitv<32>(); else if (c <= 4) waitv<24>(); else if (c == 5) waitv<16>(); else if (c == 6) waitv<8>(); else waitv<0>();
      BAR();
      if (c >= 1 && c + NS - 1 < 8) issue(c + NS - 1, (c - 1) % NS);
    }
    if (side) {
      float s[8];
#pragma unroll
      for (int j = 0; j < 8; ++j) s[j] = 0.f;
#pragma unroll
      for (int i = 0; i < 8; ++i)
#pragma unroll
        for (int j = 0; j < 4; ++j) { s[2 * j] += bflo(sv[i][j]); s[2 * j + 1] += bfhi(sv[i][j]); }
#pragma unroll
      for (int j = 0; j < 8; ++j) { s[j] += __shfl_xor(s[j], 8); s[j] += __shfl_xor(s[j], 16); s[j] += __shfl_xor(s[j], 32); }
      if (l < 8) { float* dst = (float*)(ws + WS_CS) + skp * 2304 + scol + l * 8; *(f32x4*)dst = (f32x4){s[0], s[1], s[2], s[3]}; *(f32x4*)(dst + 4) = (f32x4){s[4], s[5], s[6], s[7]}; }
    }
    BAR(); BAR();
    return;
  }
  const unsigned tt = w & 1, hp = w >> 1;
  const int t = tt * 32 + l31;
  unsigned xa[4];
#pragma unroll
  for (int ks = 0; ks < 4; ++ks) xa[ks] = lds0 + t * 128 + (((2 * ks + h) ^ ((t >> 1) & 7)) << 4);
  const int G = (l >> 4) & 1, q = (l >> 2) & 3, p4 = l & 3;
  const unsigned wa = lds0 + 8192 + (8 * h + q) * 128 + (((4 * hp + 2 * G + (p4 >> 1)) ^ ((q >> 1) << 2)) << 4) + 8 * (p4 & 1);
  f32x16 aq = {}, ak = {}, av = {};
  float sx = 0.f, sxx = 0.f, c0 = 0.f, c1 = 0.f, c2 = 0.f;
  const unsigned ONE2 = 0x3f803f80u;
#pragma unroll
  for (int c = 0; c < 8; ++c) {
    BAR();
    const unsigned so = (c % NS) * SLOT;
#pragma unroll
    for (int ks = 0; ks < 4; ++ks) {
      const bf16x8 xf = lds128(xa[ks] + so);
      const bf16x8 wq = trfrag(wa + so + ks * 2048, wa + so + ks * 2048 + 512);
      const bf16x8 wk = trfrag(wa + so + 8192 + ks * 2048, wa + so + 8192 + ks * 2048 + 512);
      const bf16x8 wv = trfrag(wa + so + 16384 + ks * 2048, wa + so + 16384 + ks * 2048 + 512);
      aq = MFMA32(wq, xf, aq); ak = MFMA32(wk, xf, ak); av = MFMA32(xf, wv, av);
      if ((ks & 1) == (int)hp) { const u32x4 u = __builtin_bit_cast(u32x4, xf);
#pragma unroll
        for (int j = 0; j < 4; ++j) { sx = dot2(u[j], ONE2, sx); sxx = dot2(u[j], u[j], sxx); } }
      if ((ks & 1) == (int)tt) { const u32x4 uq = __builtin_bit_cast(u32x4, wq), uk = __builtin_bit_cast(u32x4, wk), uv = __builtin_bit_cast(u32x4, wv);
#pragma unroll
        for (int j = 0; j < 4; ++j) { c0 = dot2(uq[j], ONE2, c0); c1 = dot2(uk[j], ONE2, c1); c2 = dot2(uv[j], ONE2, c2); } }
    }
  }
  sx = swapadd(sx); sxx = swapadd(sxx); c0 = swapadd(c0); c1 = swapadd(c1); c2 = swapadd(c2);
  BAR();
  LAS float* T = (LAS float*)(uintptr_t)lds0;
  if (h == 0) { T[hp * 64 + t] = sx; T[128 + hp * 64 + t] = sxx;
    T[256 + tt * 192 + 0 * 64 + hp * 32 + l31] = c0; T[256 + tt * 192 + 64 + hp * 32 + l31] = c1; T[256 + tt * 192 + 128 + hp * 32 + l31] = c2; }
  BAR();
  const int tok = rb * 64 + t, headA = cb * 4 + hp * 2;
  {
    const float mu = (T[t] + T[64 + t]) * (1.f / D), var = fmaxf((T[128 + t] + T[192 + t]) * (1.f / D) - mu * mu, 0.f), rs = __builtin_amdgcn_rsqf(var + EPS);
    f32x16 vq, vk;
#pragma unroll
    for (int g = 0; g < 4; ++g) {
      const unsigned o = lds0 + (256 + hp * 32 + 8 * g + 4 * h) * 4;
      const f32x4 cq = lds128f(o) + lds128f(o + 768), ck = lds128f(o + 256) + lds128f(o + 256 + 768);
#pragma unroll
      for (int j = 0; j < 4; ++j) { vq[4 * g + j] = (aq[4 * g + j] - mu * cq[j]) * (rs * 0.36067376f); vk[4 * g + j] = (ak[4 * g + j] - mu * ck[j]) * rs; }
    }
    u32x4* Qb = (u32x4*)(ws + WS_Q); u32x4* Kb = (u32x4*)(ws + WS_K);
    Qb[(headA * 2 + h) * 2048 + tok] = pack8(vq, 0); Qb[(headA * 2 + 2 + h) * 2048 + tok] = pack8(vq, 8);
    const int tp = (tok & ~12) | ((tok & 4) << 1) | ((tok & 8) >> 1);
    Kb[(headA * 2 + h) * 2048 + tp] = pack8(vk, 0); Kb[(headA * 2 + 2 + h) * 2048 + tp] = pack8(vk, 8);
    float na = 0.f, nb = 0.f;
#pragma unroll
    for (int j = 0; j < 8; ++j) { na += vk[j] * vk[j]; nb += vk[8 + j] * vk[8 + j]; }
    na = swapadd(na); nb = swapadd(nb);
#pragma unroll
    for (int o = 1; o < 32; o <<= 1) { na = fmaxf(na, __shfl_xor(na, o)); nb = fmaxf(nb, __shfl_xor(nb, o)); }
    if (l == 0) { float* km = (float*)(ws + WS_KMAX); km[headA * 64 + rb * 2 + tt] = na; km[(headA + 1) * 64 + rb * 2 + tt] = nb; }
  }
  {
    const float cv = T[256 + 128 + hp * 32 + l31] + T[256 + 192 + 128 + hp * 32 + l31];
    f32x16 vv;
#pragma unroll
    for (int g = 0; g < 4; ++g) {
      const unsigned o = lds0 + (tt * 32 + 8 * g + 4 * h) * 4;
      const f32x4 s1 = lds128f(o) + lds128f(o + 256), s2 = lds128f(o + 512) + lds128f(o + 768);
#pragma unroll
      for (int j = 0; j < 4; ++j) { const float mu = s1[j] * (1.f / D), var = fmaxf(s2[j] * (1.f / D) - mu * mu, 0.f); vv[4 * g + j] = (av[4 * g + j] - mu * cv) * __builtin_amdgcn_rsqf(var + EPS); }
    }
    u32x4* Vb = (u32x4*)(ws + WS_V);
    const int head = headA + (l31 >> 4), tile = rb * 4 + tt * 2;
    Vb[(head * 128 + tile) * 32 + h * 16 + (l31 & 15)] = pack8(vv, 0);
    Vb[(head * 128 + tile + 1) * 32 + h * 16 + (l31 & 15)] = pack8(vv, 8);
  }
}

DI void poll_flags(const unsigned* p, bool need) {
  for (unsigned sp = 0; sp < (1u << 20); ++sp) {
    const unsigned v = need ? __hip_atomic_load(p, RLX_AGENT) : 1u;
    if (__all(v != 0u)) break;
    __builtin_amdgcn_s_sleep(1);
  }
  asm volatile("" ::: "memory");
}
DI void set_flag(unsigned* p, int l) { WAITV(0); if (l == 0) __hip_atomic_store(p, 1u, RLX_AGENT); }

template <int NP> DI void stage64(rsrc_t rw, unsigned vw, unsigned pitchB, unsigned colB, unsigned k0, unsigned lds, unsigned w) {
#pragma unroll
  for (int i = 0; i < NP; ++i) { const unsigned pid = w * NP + i; dma16<0, false>(rw, vw, (k0 + pid * 8) * pitchB + colB, lds + pid * 1024); }
}
DI void issue_atile(rsrc_t ra, const unsigned (&v)[4], unsigned sbase, unsigned lds, unsigned iw) {
#pragma unroll
  for (int j = 0; j < 4; ++j) {
    const unsigned o = (j * 16 + iw * 4) * 1024;
    dma16<0, true>(ra, v[j], sbase + o, lds + o); dma16<1024, true>(ra, v[j], sbase + o, lds + o);
    dma16<2048, true>(ra, v[j], sbase + o, lds + o); dma16<3072, true>(ra, v[j], sbase + o, lds + o);
  }
}
DI void io_waits4() { waitv<12>(); BAR(); waitv<8>(); BAR(); waitv<4>(); BAR(); waitv<0>(); BAR(); }
DI void row_stats(rsrc_t rs, unsigned off, int h, float& mu, float& var) {
  float s = 0.f, q = 0.f;
#pragma unroll
  for (int i = 0; i < 4; ++i) { const f32x4 v = __builtin_bit_cast(f32x4, __builtin_amdgcn_raw_buffer_load_b128(rs, off + (4 * h + i) * 1024, 0, 16)); s += v[0] + v[2]; q += v[1] + v[3]; }
  s = swapadd(s); q = swapadd(q);
  mu = s * (1.f / D); var = fmaxf(q * (1.f / D) - mu * mu, 0.f);
}
DI void put_stats(rsrc_t rs, unsigned off, const f32x16& v, int h) {
  float s = 0.f, q = 0.f;
#pragma unroll
  for (int i = 0; i < 16; ++i) { s += v[i]; q += v[i] * v[i]; }
  s = swapadd(s); q = swapadd(q);
  if (h == 0) __builtin_amdgcn_raw_buffer_store_b64((u32x2){__float_as_uint(s), __float_as_uint(q)}, rs, off, 0, 16);
}
DI f32x16 round16(const f32x16& v, u32x4& p0, u32x4& p1) {
  p0 = pack8(v, 0); p1 = pack8(v, 8); f32x16 o;
#pragma unroll
  for (int j = 0; j < 4; ++j) { o[2 * j] = bflo(p0[j]); o[2 * j + 1] = bfhi(p0[j]); o[8 + 2 * j] = bflo(p1[j]); o[8 + 2 * j + 1] = bfhi(p1[j]); }
  return o;
}
DI f32x16 gemm64(const bf16x8 (&wf)[32], unsigned ba) {
  f32x16 acc = {};
#pragma unroll
  for (int j = 0; j < 4; ++j) {
    BAR();
#pragma unroll
    for (int k = 0; k < 8; ++k) acc = MFMA32(wf[j * 8 + k], lds128(ba + (j * 8 + k) * 2048), acc);
  }
  return acc;
}

__global__ void __launch_bounds__(512, 2) k2_main(const u16* __restrict__ x, const u16* __restrict__ wo, const u16* __restrict__ w1, const u16* __restrict__ w2,
                                                  const u16* __restrict__ w3, const u16* __restrict__ w4, u16* __restrict__ out, char* __restrict__ ws) {
  extern __shared__ __attribute__((aligned(16))) char smem[];
  const unsigned lds0 = (unsigned)(uintptr_t)smem;
  const int b = blockIdx.x, tid = threadIdx.x, l = tid & 63;
  const unsigned w = rfl(tid >> 6);
  const int xcd = b & 7, r = b >> 3;
  const int h = l >> 5, l31 = l & 31;
  unsigned* flags = (unsigned*)(ws + WS_FLAG);
  const rsrc_t rws = mkrsrc(ws, (unsigned)WS_END);
  const unsigned rb = xcd * 4 + (r >> 3), cb = r & 7;
  {
    const unsigned head = xcd * 4 + (r & 3), qb = r >> 2;
    const unsigned vl = l * 16;
#pragma unroll
    for (int st = 0; st < 8; ++st) {
#pragma unroll
      for (int i = 0; i < 2; ++i) {
        const unsigned p = 2 * w + i;
        const unsigned base = lds0 + (st >> 2) * 65536;
        const unsigned so = p < 8 ? (unsigned)WS_K + ((head * 2 + (p >> 2)) * 2048 + st * 256 + (p & 3) * 64) * 16 : (unsigned)WS_V + head * 65536 + (st * 8 + p - 8) * 1024;
        const unsigned ld = p < 8 ? base + (p >> 2) * 16384 + ((st & 3) * 256 + (p & 3) * 64) * 16 : base + 32768 + ((st & 3) * 8 + p - 8) * 1024;
        dma16<0, false>(rws, vl, so, ld);
      }
    }
    const int tok = qb * 256 + w * 32 + l31;
    const bf16x8 qf = *(const bf16x8*)(ws + WS_Q + ((head * 2 + h) * 2048 + tok) * 16);
    float km = ((const float*)(ws + WS_KMAX))[head * 64 + l];
    if (tid < 288) *(LAS u32x4*)(uintptr_t)(lds0 + LZ_ONES + tid * 16) = (u32x4){0x3f803f80u, 0x3f803f80u, 0x3f803f80u, 0x3f803f80u};
    if (tid < 288) {
      const float* cs = (const float*)(ws + WS_CS) + (tid < 64 ? cb * 64 + tid : 512 + cb * 224 + (tid - 64));
      float s = 0.f;
#pragma unroll
      for (int i = 0; i < 8; ++i) s += cs[i * 2304];
      *(LAS float*)(uintptr_t)(lds0 + LZ_CS1 + tid * 4) = s;
    }
#pragma unroll
    for (int o = 1; o < 64; o <<= 1) km = fmaxf(km, __shfl_xor(km, o));
    float qn = 0.f;
    { const u32x4 u = __builtin_bit_cast(u32x4, qf);
#pragma unroll
      for (int j = 0; j < 4; ++j) qn = dot2(u[j], u[j], qn); }
    qn = swapadd(qn);
    const float bound = __builtin_amdgcn_sqrtf(qn * km) * 1.02f;
    const bool fast = __all(bound <= 48.f);
    f32x16 oacc = {};
    const int idx = 0xDDDD;
    unsigned ka = lds0 + h * 16384 + l31 * 16;
    unsigned va = l31 < 16 ? lds0 + 32768 + h * 256 + l31 * 16 : lds0 + LZ_ONES + (l & 15) * 16;
    const unsigned vinc = l31 < 16 ? 4096u : 0u, vjump = l31 < 16 ? 32768u : 0u;
#define SMF(A, P, o) oacc = __builtin_amdgcn_smfmac_f32_32x32x32_bf16(__builtin_bit_cast(bf8_t, A), __builtin_bit_cast(bf16_t, __builtin_shufflevector(P, P, o, o + 1, o + 2, o + 3, o + 4, o + 5, o + 6, o + 7)), oacc, idx, 0, 0)
#define EXP16(P) _Pragma("unroll") for (int i_ = 0; i_ < 16; ++i_) P[i_] = __builtin_amdgcn_exp2f(P[i_])
    waitv<14>(); BAR();
    if (fast) {
      f32x16 negm;
#pragma unroll
      for (int i = 0; i < 16; ++i) negm[i] = -bound;
      for (int it = 0; it < 16; ++it) {
        if (it == 2) { waitv<0>(); BAR(); }
#pragma unroll
        for (int u = 0; u < 4; ++u) {
          const bf16x8 kf = lds128(ka + u * 512), v0 = lds128(va + u * 1024), v1 = lds128(va + u * 1024 + 512);
          f32x16 s = MFMA32(kf, qf, negm);
          EXP16(s);
          SMF(v0, s, 0); SMF(v1, s, 8);
        }
        ka += 2048; va += vinc;
        if (it == 7) { ka += 65536 - 16384; va += vjump; }
      }
    } else {
      float m = -3.0e38f;
      const f32x16 zero = {};
      for (int it = 0; it < 16; ++it) {
        if (it == 2) { waitv<0>(); BAR(); }
#pragma unroll 1
        for (int u = 0; u < 4; ++u) {
          const bf16x8 kf = lds128(ka + u * 512), v0 = lds128(va + u * 1024), v1 = lds128(va + u * 1024 + 512);
          f32x16 s = MFMA32(kf, qf, zero);
          float tm = s[0];
#pragma unroll
          for (int i = 1; i < 16; ++i) tm = fmaxf(tm, s[i]);
          const float mn = fmaxf(m, swapmax(tm)), al = __builtin_amdgcn_exp2f(m - mn);
          m = mn;
#pragma unroll
          for (int i = 0; i < 16; ++i) { s[i] = __builtin_amdgcn_exp2f(s[i] - mn); oacc[i] *= al; }
          SMF(v0, s, 0); SMF(v1, s, 8);
        }
        ka += 2048; va += vinc;
        if (it == 7) { ka += 65536 - 16384; va += vjump; }
      }
    }
    const float inv = 1.0f / oacc[8];
#pragma unroll
    for (int i = 0; i < 8; ++i) oacc[i] *= inv;
    st128_sc1(rws, (unsigned)WS_O + (tok >> 6) * 65536 + (head * 2 + h) * 1024 + (tok & 63) * 16, pack8(oacc, 0));
    set_flag(flags + FLAG_O + (qb * 4 + (w >> 1)) * 64 + head * 2 + (w & 1), l);
  }
  const int G = (l >> 4) & 1, q = (l >> 2) & 3, p4 = l & 3;
  const unsigned vw512 = (l >> 3) * 1024 + (((l & 7) ^ (((l >> 4) & 1) << 2)) << 4);
  const unsigned nt = w & 1, tt = (w >> 1) & 1;
  const unsigned wa64 = lds0 + LX + (4 * h + q) * 128 + (((4 * nt + 2 * G + (p4 >> 1)) ^ ((q >> 1) << 2)) << 4) + 8 * (p4 & 1);
  const unsigned ba = lds0 + LY + h * 1024 + (tt * 32 + l31) * 16;
  const unsigned tl = tt * 32 + l31, tok = rb * 64 + tl;
  const unsigned vplain[4] = {(unsigned)l * 16, (unsigned)l * 16, (unsigned)l * 16, (unsigned)l * 16};
  const unsigned tileo = rb * 65536 + (cb * 8 + nt * 4 + h) * 1024 + tl * 16;
  const unsigned stato = rb * 8192 + tl * 16;
  bf16x8 wf[32];
  f32x16 hres, xres; float mu_h = 0.f, rs_h = 0.f;
  auto load_w64 = [&](const u16* W) {
    BAR();
    stage64<8>(mkrsrc(W, D * D * 2), vw512, 1024, cb * 128, 0, lds0 + LX, w);
    waitv<0>(); BAR();
    if (w < 4) {
#pragma unroll
      for (int k = 0; k < 32; ++k) wf[k] = trfrag(wa64 + k * 2048, wa64 + k * 2048 + 1024);
    }
  };
  load_w64(wo);
  if (w >= 4) {
    if (w == 4) poll_flags(flags + FLAG_O + rb * 64 + l, true);
    BAR();
    issue_atile(rws, vplain, (unsigned)WS_O + rb * 65536, lds0 + LY, w - 4);
    io_waits4();
  } else {
    u32x2 xr[4];
#pragma unroll
    for (int g = 0; g < 4; ++g) xr[g] = *(const u32x2*)(x + tok * D + cb * 64 + nt * 32 + 8 * g + 4 * h);
    BAR();
    f32x16 acc = gemm64(wf, ba);
#pragma unroll
    for (int g = 0; g < 4; ++g) { acc[4 * g] += bflo(xr[g][0]); acc[4 * g + 1] += bfhi(xr[g][0]); acc[4 * g + 2] += bflo(xr[g][1]); acc[4 * g + 3] += bfhi(xr[g][1]); }
    u32x4 p0, p1; hres = round16(acc, p0, p1);
    st128_sc1(rws, (unsigned)WS_H + tileo, p0); st128_sc1(rws, (unsigned)WS_H + tileo + 2048, p1);
    put_stats(rws, (unsigned)WS_STH + stato + cb * 1024 + nt * 8, hres, h);
    set_flag(flags + FLAG_H + rb * 64 + cb * 4 + w, l);
  }
  load_w64(w1);
  if (w >= 4) {
    if (w == 4) poll_flags(flags + FLAG_H + rb * 64 + (l & 31), true);
    BAR();
    issue_atile(rws, vplain, (unsigned)WS_H + rb * 65536, lds0 + LY, w - 4);
    io_waits4();
  } else {
    BAR();
    float var; row_stats(rws, (unsigned)WS_STH + stato, h, mu_h, var);
    rs_h = __builtin_amdgcn_rsqf(var + EPS);
    const float cz = rs_h * __builtin_amdgcn_rsqf(var * rs_h * rs_h + EPS);
    f32x16 acc = gemm64(wf, ba);
#pragma unroll
    for (int g = 0; g < 4; ++g) { const f32x4 cs = lds128f(lds0 + LZ_CS1 + (nt * 32 + 8 * g + 4 * h) * 4);
#pragma unroll
      for (int j = 0; j < 4; ++j) acc[4 * g + j] = gelu((acc[4 * g + j] - mu_h * cs[j]) * cz); }
    st128_sc1(rws, (unsigned)WS_G + tileo, pack8(acc, 0)); st128_sc1(rws, (unsigned)WS_G + tileo + 2048, pack8(acc, 8));
    set_flag(flags + FLAG_G + rb * 64 + cb * 4 + w, l);
  }
  load_w64(w2);
  if (w >= 4) {
    if (w == 4) {
      const int k = l & 15, cbn = (k >> 3) * 4 + ((k >> 2) & 1) + (l >= 48 ? 2 : 0);
      const int rn = l < 32 ? rb : (l < 48 ? (int)rb - 1 : (int)rb + 1);
      poll_flags(flags + FLAG_G + rn * 64 + (l < 32 ? l : cbn * 4 + (k & 3)), rn >= 0 && rn < 32);
    }
    BAR();
    unsigned vs[4];
#pragma unroll
    for (int j = 0; j < 4; ++j) { const int src = (int)rb * 64 + l + (j == 0 ? -1 : j == 1 ? 1 : j == 2 ? -2 : 2);
      vs[j] = (src >= 0 && src < S) ? (unsigned)((src >> 6) * 65536 + (src & 63) * 16) : 0x7ff00000u; }
    issue_atile(mkrsrc(ws + WS_G, 2u << 20), vs, 0, lds0 + LY, w - 4);
    io_waits4();
  } else {
    BAR();
    f32x16 acc = gemm64(wf, ba);
#pragma unroll
    for (int i = 0; i < 16; ++i) acc[i] += (hres[i] - mu_h) * rs_h;
    u32x4 p0, p1; xres = round16(acc, p0, p1);
    st128_sc1(rws, (unsigned)WS_X1 + tileo, p0); st128_sc1(rws, (unsigned)WS_X1 + tileo + 2048, p1);
    put_stats(rws, (unsigned)WS_STX + stato + cb * 1024 + nt * 8, xres, h);
    set_flag(flags + FLAG_X + rb * 64 + cb * 4 + w, l);
#pragma unroll
    for (int i = 0; i < 16; ++i) hres[i] += xres[i];
  }
  {
    const rsrc_t r3 = mkrsrc(w3, D * F * 2);
    unsigned v3[7];
#pragma unroll
    for (int i = 0; i < 7; ++i) { const unsigned e = i * 64 + l; v3[i] = (e / 28) * (F * 2) + (e % 28) * 16; }
    const unsigned wa3 = lds0 + LX + (4 * h + q) * 448 + w * 64 + G * 32 + p4 * 8;
#pragma unroll
    for (int pc = 0; pc < 4; ++pc) {
      BAR();
#pragma unroll
      for (int i = 0; i < 7; ++i) dma16<0, false>(r3, v3[i], (pc * 128 + w * 16) * (F * 2) + cb * 448, lds0 + LX + w * 7168 + i * 1024);
      waitv<0>(); BAR();
      if (w < 7) {
#pragma unroll
        for (int k = 0; k < 8; ++k) wf[pc * 8 + k] = trfrag(wa3 + k * 16 * 448, wa3 + (k * 16 + 8) * 448);
      }
    }
    if (w == 7) {
      poll_flags(flags + FLAG_X + rb * 64 + (l & 31), true);
      BAR();
#pragma unroll
      for (int j = 0; j < 4; ++j)
#pragma unroll
        for (int i = 0; i < 4; ++i) { const unsigned o = (j * 16 + i * 4) * 1024, sb = (unsigned)WS_X1 + rb * 65536 + o;
          dma16<0, true>(rws, vplain[0], sb, lds0 + LY + o); dma16<1024, true>(rws, vplain[0], sb, lds0 + LY + o);
          dma16<2048, true>(rws, vplain[0], sb, lds0 + LY + o); if (j * 4 + i < 15) dma16<3072, true>(rws, vplain[0], sb, lds0 + LY + o); else { waitv<47>(); dma16<3072, true>(rws, vplain[0], sb, lds0 + LY + o); } }
      waitv<48>(); BAR(); waitv<32>(); BAR(); waitv<16>(); BAR(); waitv<0>(); BAR();
    } else {
      BAR();
      float mu0, var0, mu1, var1;
      row_stats(rws, (unsigned)WS_STX + rb * 8192 + l31 * 16, h, mu0, var0);
      row_stats(rws, (unsigned)WS_STX + rb * 8192 + (32 + l31) * 16, h, mu1, var1);
      const float rs0 = __builtin_amdgcn_rsqf(var0 + EPS), rs1 = __builtin_amdgcn_rsqf(var1 + EPS);
      f32x16 a0 = {}, a1 = {};
      const unsigned bb = lds0 + LY + h * 1024 + l31 * 16;
#pragma unroll
      for (int j = 0; j < 4; ++j) {
        BAR();
#pragma unroll
        for (int k = 0; k < 8; ++k) { a0 = MFMA32(wf[j * 8 + k], lds128(bb + (j * 8 + k) * 2048), a0); a1 = MFMA32(wf[j * 8 + k], lds128(bb + (j * 8 + k) * 2048 + 512), a1); }
      }
#pragma unroll
      for (int g = 0; g < 4; ++g) { const f32x4 cs = lds128f(lds0 + LZ_CS3 + (w * 32 + 8 * g + 4 * h) * 4);
#pragma unroll
        for (int j = 0; j < 4; ++j) { a0[4 * g + j] = gelu((a0[4 * g + j] - mu0 * cs[j]) * rs0); a1[4 * g + j] = gelu((a1[4 * g + j] - mu1 * cs[j]) * rs1); } }
      const unsigned uo = (unsigned)WS_U + rb * (224 * 1024) + (cb * 28 + w * 4 + h) * 1024 + l31 * 16;
      st128_sc1(rws, uo, pack8(a0, 0)); st128_sc1(rws, uo + 2048, pack8(a0, 8));
      st128_sc1(rws, uo + 512, pack8(a1, 0)); st128_sc1(rws, uo + 2048 + 512, pack8(a1, 8));
      set_flag(flags + FLAG_U + rb * 64 + cb * 8 + w, l);
    }
  }
  {
    const unsigned kq = w >> 1;
    const rsrc_t r4 = mkrsrc(w4, F * D * 2);
#pragma unroll
    for (int pc = 0; pc < 4; ++pc) {
      BAR();
      if (pc < 3) stage64<8>(r4, vw512, 1024, cb * 128, pc * 512, lds0 + LX, w); else stage64<4>(r4, vw512, 1024, cb * 128, pc * 512, lds0 + LX, w);
      waitv<0>(); BAR();
#pragma unroll
      for (int c = 0; c < (pc < 3 ? 4 : 2); ++c)
#pragma unroll
        for (int k = 0; k < 2; ++k) wf[(pc * 4 + c) * 2 + k] = trfrag(wa64 + kq * 4096 + (c * 8 + k) * 2048, wa64 + kq * 4096 + (c * 8 + k) * 2048 + 1024);
    }
    if (w == 7) poll_flags(flags + FLAG_U + rb * 64 + l, (l & 7) != 7);
    BAR();
    const unsigned ub = (unsigned)WS_U + rb * (224 * 1024) + w * 2048;
    auto issue = [&](int c) { dma16<0, true>(rws, vplain[0], ub + c * 16384, lds0 + (c & 7) * 16384 + w * 2048); dma16<1024, true>(rws, vplain[0], ub + c * 16384, lds0 + (c & 7) * 16384 + w * 2048); };
#pragma unroll
    for (int c = 0; c < 8; ++c) issue(c);
    f32x16 a0 = {}, a1 = {};
    const unsigned bb = lds0 + kq * 4096 + h * 1024 + l31 * 16;
#pragma unroll
    for (int c = 0; c < 14; ++c) {
      if (c == 0) waitv<14>(); else if (c <= 7) waitv<12>(); else if (c == 8) waitv<10>(); else if (c == 9) waitv<8>(); else if (c == 10) waitv<6>(); else if (c == 11) waitv<4>(); else if (c == 12) waitv<2>(); else waitv<0>();
      BAR();
      if (c >= 1 && c + 7 < 14) issue(c + 7);
#pragma unroll
      for (int k = 0; k < 2; ++k) { a0 = MFMA32(wf[c * 2 + k], lds128(bb + (c & 7) * 16384 + k * 2048), a0); a1 = MFMA32(wf[c * 2 + k], lds128(bb + (c & 7) * 16384 + k * 2048 + 512), a1); }
    }
    BAR();
    auto slot = [&](unsigned tt_, unsigned j) { return lds0 + ((nt * 2 + tt_) * 3 + j) * 4096 + l * 16; };
    auto put = [&](unsigned a, const f32x16& v) {
#pragma unroll
      for (int g = 0; g < 4; ++g) *(LAS f32x4*)(uintptr_t)(a + g * 1024) = (f32x4){v[4 * g], v[4 * g + 1], v[4 * g + 2], v[4 * g + 3]}; };
    if (kq != 0) put(slot(0, kq - 1), a0);
    if (kq != 1) put(slot(1, kq == 0 ? 0 : kq - 1), a1);
    BAR();
    if (w < 4) {
      f32x16 acc = kq == 0 ? a0 : a1;
#pragma unroll
      for (int j = 0; j < 3; ++j)
#pragma unroll
        for (int g = 0; g < 4; ++g) { const f32x4 v = lds128f(slot(kq, j) + g * 1024);
#pragma unroll
          for (int i = 0; i < 4; ++i) acc[4 * g + i] += v[i]; }
#pragma unroll
      for (int g = 0; g < 4; ++g)
        *(u32x2*)(out + tok * D + cb * 64 + nt * 32 + 8 * g + 4 * h) = (u32x2){pk2(acc[4 * g] + hres[4 * g], acc[4 * g + 1] + hres[4 * g + 1]), pk2(acc[4 * g + 2] + hres[4 * g + 2], acc[4 * g + 3] + hres[4 * g + 3])};
    }
  }
}

extern "C" void kernel_launch(void* const* d_in, const int* in_sizes, int n_in, void* d_out, int out_size, void* d_ws, size_t ws_size, hipStream_t stream) {
  const u16* x = (const u16*)d_in[0]; const u16* wqkv = (const u16*)d_in[1]; const u16* wo = (const u16*)d_in[2];
  const u16* w1 = (const u16*)d_in[4]; const u16* w2 = (const u16*)d_in[5]; const u16* w3 = (const u16*)d_in[6]; const u16* w4 = (const u16*)d_in[7];
  (void)hipFuncSetAttribute((const void*)k1_qkv, hipFuncAttributeMaxDynamicSharedMemorySize, LDS_BYTES);
  (void)hipFuncSetAttribute((const void*)k2_main, hipFuncAttributeMaxDynamicSharedMemorySize, LDS_BYTES);
  k1_qkv<<<256, 512, LDS_BYTES, stream>>>(x, wqkv, w1, w3, (char*)d_ws);
  k2_main<<<256, 512, LDS_BYTES, stream>>>(x, wo, w1, w2, w3, w4, (u16*)d_out, (char*)d_ws);
}
```

```cpp
#include <hip/hip_runtime.h>
#include <stdint.h>

typedef unsigned short u16;
typedef __attribute__((ext_vector_type(8))) short bf16x8;
typedef __attribute__((ext_vector_type(4))) short s16x4;
typedef __attribute__((ext_vector_type(8))) __bf16 bf8_t;
typedef __attribute__((ext_vector_type(16))) __bf16 bf16_t;
typedef __attribute__((ext_vector_type(2))) __bf16 bf2_t;
typedef __attribute__((ext_vector_type(16))) float f32x16;
typedef __attribute__((ext_vector_type(8))) float f32x8;
typedef __attribute__((ext_vector_type(4))) float f32x4;
typedef __attribute__((ext_vector_type(2))) float f32x2;
typedef __attribute__((ext_vector_type(16))) unsigned u32x16;
typedef __attribute__((ext_vector_type(4))) unsigned u32x4;
typedef __attribute__((ext_vector_type(2))) unsigned u32x2;
typedef __amdgpu_buffer_rsrc_t rsrc_t;
#define LAS __attribute__((address_space(3)))
#define DI __device__ __forceinline__
#define RLX_AGENT __ATOMIC_RELAXED, __HIP_MEMORY_SCOPE_AGENT

constexpr int S = 2048, D = 512, F = 1792;
constexpr float EPS = 1e-5f;
constexpr unsigned WS_FLAG = 0;
constexpr unsigned WS_KMAX = 196608;
constexpr unsigned WS_STH  = 262144;
constexpr unsigned WS_STX  = WS_STH + 262144;
constexpr unsigned WS_Q    = 1u << 20;
constexpr unsigned WS_KV   = WS_Q + (2u << 20);
constexpr unsigned WS_O    = WS_KV + (4u << 20);
constexpr unsigned WS_H    = WS_O + (2u << 20);
constexpr unsigned WS_G    = WS_H + (2u << 20);
constexpr unsigned WS_X1   = WS_G + (2u << 20);
constexpr unsigned WS_U    = WS_X1 + (2u << 20);
constexpr unsigned WS_TW   = WS_U + 32u * 224 * 1024;
constexpr unsigned TW_O = WS_TW, TW_1 = TW_O + 524288, TW_2 = TW_1 + 524288, TW_3 = TW_2 + 524288, TW_4 = TW_3 + 1835008;
constexpr unsigned WS_END  = TW_4 + 1835008;
constexpr int FLAG_O = 0, FLAG_H = 8192, FLAG_G = 16384, FLAG_X = 24576, FLAG_U = 32768, FLAG_W = 40960;

constexpr int LDS_BYTES = 163840;
constexpr unsigned L_P = 96 * 1024, L_Z = 128 * 1024, L_ST = 156 * 1024;

#define STAMP(i)
#define STAMPC(i)
#define STDECL
#define STDUMP(k)

DI rsrc_t mkrsrc(const void* p, unsigned bytes) { return __builtin_amdgcn_make_buffer_rsrc((void*)p, 0, bytes, 0x00020000); }
DI unsigned rfl(unsigned v) { return __builtin_amdgcn_readfirstlane(v); }
DI int lane() { int v; asm volatile("v_mbcnt_lo_u32_b32 %0, -1, 0\n\tv_mbcnt_hi_u32_b32 %0, -1, %0" : "=v"(v)); return v; }
#define MFMA32(a, b, c) __builtin_amdgcn_mfma_f32_32x32x16_bf16((a), (b), (c), 0, 0, 0)
#define MFMA16(a, b, c) __builtin_amdgcn_mfma_f32_16x16x32_bf16((a), (b), (c), 0, 0, 0)
#define BAR() asm volatile("s_waitcnt lgkmcnt(0)\n\ts_barrier" ::: "memory")
template <int N> DI void waitv() { asm volatile("s_waitcnt vmcnt(%0)" ::"i"(N) : "memory"); }
#define TOUCH(x) asm volatile("" : "+v"(x))
#define SB() __builtin_amdgcn_sched_barrier(0)
DI void drain() { waitv<0>(); __builtin_amdgcn_s_waitcnt(0x0F70); }

template <int OFF, bool SC1> DI void dma16(rsrc_t rs, unsigned voff, unsigned soff, unsigned lds) {
  if constexpr (SC1) asm volatile("s_mov_b32 m0, %0\n\ts_nop 0\n\tbuffer_load_dwordx4 %1, %2, %3 offen offset:%4 sc1 lds" ::"s"(lds), "v"(voff), "s"(rs), "s"(soff), "i"(OFF) : "memory");
  else asm volatile("s_mov_b32 m0, %0\n\ts_nop 0\n\tbuffer_load_dwordx4 %1, %2, %3 offen offset:%4 lds" ::"s"(lds), "v"(voff), "s"(rs), "s"(soff), "i"(OFF) : "memory");
}
template <int N> DI void dmaN(rsrc_t rs, unsigned voff, unsigned soff, unsigned lds) {
  if constexpr (N == 1) dma16<0, true>(rs, voff, soff, lds);
  else if constexpr (N == 2) asm volatile("s_mov_b32 m0, %0\n\ts_nop 0\n\tbuffer_load_dwordx4 %1, %2, %3 offen sc1 lds\n\tbuffer_load_dwordx4 %1, %2, %3 offen offset:1024 sc1 lds" ::"s"(lds), "v"(voff), "s"(rs), "s"(soff) : "memory");
  else if constexpr (N == 3) asm volatile("s_mov_b32 m0, %0\n\ts_nop 0\n\tbuffer_load_dwordx4 %1, %2, %3 offen sc1 lds\n\tbuffer_load_dwordx4 %1, %2, %3 offen offset:1024 sc1 lds\n\tbuffer_load_dwordx4 %1, %2, %3 offen offset:2048 sc1 lds" ::"s"(lds), "v"(voff), "s"(rs), "s"(soff) : "memory");
  else asm volatile("s_mov_b32 m0, %0\n\ts_nop 0\n\tbuffer_load_dwordx4 %1, %2, %3 offen sc1 lds\n\tbuffer_load_dwordx4 %1, %2, %3 offen offset:1024 sc1 lds\n\tbuffer_load_dwordx4 %1, %2, %3 offen offset:2048 sc1 lds\n\tbuffer_load_dwordx4 %1, %2, %3 offen offset:3072 sc1 lds" ::"s"(lds), "v"(voff), "s"(rs), "s"(soff) : "memory");
}
DI bf16x8 lds128(unsigned a) { return *(const LAS bf16x8*)(uintptr_t)a; }
DI f32x4 lds128f(unsigned a) { return *(const LAS f32x4*)(uintptr_t)a; }
DI s16x4 ldstr(unsigned a) { return __builtin_amdgcn_ds_read_tr16_b64_v4i16((LAS s16x4*)(uintptr_t)a); }
DI bf16x8 trfrag(unsigned a0, unsigned a1) { s16x4 lo = ldstr(a0), hi = ldstr(a1); return __builtin_shufflevector(lo, hi, 0, 1, 2, 3, 4, 5, 6, 7); }
DI unsigned pk2(float lo, float hi) { f32x2 v = {lo, hi}; bf2_t r = __builtin_convertvector(v, bf2_t); return __builtin_bit_cast(unsigned, r); }
DI float bflo(unsigned u) { return __uint_as_float(u << 16); }
DI float bfhi(unsigned u) { return __uint_as_float(u & 0xffff0000u); }
DI float swapadd(float v) { auto r = __builtin_amdgcn_permlane32_swap(__float_as_uint(v), __float_as_uint(v), false, false); return __uint_as_float(r[0]) + __uint_as_float(r[1]); }
DI float swapmax(float v) { auto r = __builtin_amdgcn_permlane32_swap(__float_as_uint(v), __float_as_uint(v), false, false); return fmaxf(__uint_as_float(r[0]), __uint_as_float(r[1])); }
DI float swap16add(float v) { auto r = __builtin_amdgcn_permlane16_swap(__float_as_uint(v), __float_as_uint(v), false, false); return __uint_as_float(r[0]) + __uint_as_float(r[1]); }
DI float swap16max(float v) { auto r = __builtin_amdgcn_permlane16_swap(__float_as_uint(v), __float_as_uint(v), false, false); return fmaxf(__uint_as_float(r[0]), __uint_as_float(r[1])); }
template <int CTRL> DI float dppf(float v) { return __uint_as_float(__builtin_amdgcn_update_dpp(0, __float_as_uint(v), CTRL, 0xf, 0xf, true)); }
DI float max16(float v) { v = fmaxf(v, dppf<0xB1>(v)); v = fmaxf(v, dppf<0x4E>(v)); v = fmaxf(v, dppf<0x141>(v)); return fmaxf(v, dppf<0x140>(v)); }
DI float sum8(float v) { v += dppf<0xB1>(v); v += dppf<0x4E>(v); return v + dppf<0x141>(v); }
DI float dot2(unsigned a, unsigned b, float c) { return __builtin_amdgcn_fdot2_f32_bf16(__builtin_bit_cast(bf2_t, a), __builtin_bit_cast(bf2_t, b), c, false); }
DI float gelu(float x) {
  const float t = x * __builtin_fmaf(x * x, -0.10294324f, -2.3022082f);
  return x * __builtin_amdgcn_rcpf(1.0f + __builtin_amdgcn_exp2f(t));
}
DI void st128(rsrc_t rs, unsigned off, u32x4 v) { __builtin_amdgcn_raw_buffer_store_b128(v, rs, off, 0, 16); }
DI void st128s(rsrc_t rs, unsigned voff, unsigned soff, u32x4 v) { __builtin_amdgcn_raw_buffer_store_b128(v, rs, voff, soff, 16); }
DI bf16x8 ld128(rsrc_t rs, unsigned voff, unsigned soff) { return __builtin_bit_cast(bf16x8, __builtin_amdgcn_raw_buffer_load_b128(rs, voff, soff, 16)); }
template <int OFF> DI void ldw(bf16x8& r, rsrc_t rs, unsigned voff, unsigned soff) {
  asm volatile("buffer_load_dwordx4 %0, %1, %2, %3 offen offset:%4 sc1" : "+v"(r) : "v"(voff), "s"(rs), "s"(soff), "i"(OFF) : "memory");
}
template <int OFF> DI void ldn(bf16x8& r, rsrc_t rs, unsigned voff, unsigned soff) {
  asm volatile("buffer_load_dwordx4 %0, %1, %2, %3 offen offset:%4 sc1" : "=v"(r) : "v"(voff), "s"(rs), "s"(soff), "i"(OFF) : "memory");
}
template <int N> DI void got4(bf16x8 (&f)[4]) { asm volatile("s_waitcnt vmcnt(%4)" : "+v"(f[0]), "+v"(f[1]), "+v"(f[2]), "+v"(f[3]) : "i"(N) : "memory"); }
template <int OFF> DI void ldsw(bf16x8& r, unsigned a) { asm volatile("ds_read_b128 %0, %1 offset:%2" : "+v"(r) : "v"(a), "i"(OFF) : "memory"); }
template <int V> struct IC { static constexpr int v = V; };
template <int B, int E, class Fn> DI void sfor(Fn&& fn) { if constexpr (B < E) { fn(IC<B>{}); sfor<B + 1, E>(fn); } }
DI u32x4 pack8(const f32x16& a, int o) { u32x4 p; p[0] = pk2(a[o], a[o + 1]); p[1] = pk2(a[o + 2], a[o + 3]); p[2] = pk2(a[o + 4], a[o + 5]); p[3] = pk2(a[o + 6], a[o + 7]); return p; }

DI bf16x8 split_op(float v, bool first, bool on) {
  const unsigned hi = __float_as_uint(v) & 0xffff0000u, lo = pk2(v - __uint_as_float(hi), 0.f);
  u32x4 o = {0u, 0u, 0u, 0u};
  if (first) { o[0] = hi | (hi >> 16); o[1] = lo | (lo << 16); } else { o[0] = (hi >> 16) | (lo << 16); o[1] = o[0]; }
  if (!on) { o[0] = 0u; o[1] = 0u; }
  return __builtin_bit_cast(bf16x8, o);
}

__global__ void __launch_bounds__(512, 2) k1_qkv(const u16* __restrict__ x, const u16* __restrict__ wqkv, const u16* __restrict__ wo, const u16* __restrict__ w1,
                                                 const u16* __restrict__ w2, const u16* __restrict__ w3, const u16* __restrict__ w4, char* __restrict__ ws) {
  extern __shared__ __attribute__((aligned(16))) char smem[];
  const unsigned lds0 = (unsigned)(uintptr_t)smem;
  const int b = blockIdx.x, l = lane();
  const unsigned w = rfl(threadIdx.x >> 6);
  const unsigned xcd = b & 7, r = b >> 3;
  const unsigned rb = (xcd >> 1) * 8 + (r >> 2), cb = (xcd & 1) * 4 + (r & 3);
  constexpr unsigned SLOT = 32768, LC = 4 * SLOT, LT = LC + 20480;
  STDECL; STAMP(0); STAMPC(40);
  const rsrc_t rx = mkrsrc(x, S * D * 2), rw = mkrsrc(wqkv, D * 1536 * 2), rws = mkrsrc(ws, WS_END);
  const int h = l >> 5, l31 = l & 31, G = (l >> 4) & 1, q = (l >> 2) & 3, p4 = l & 3;
  const unsigned hw = w & 3;
  const unsigned vx0 = (l >> 3) * 1024 + (((l & 7) ^ ((l >> 3) & 6)) << 4), vx1 = (vx0 ^ 16) + 8192;
  const unsigned vsw = ((l & 7) ^ (G << 2)) << 4, vw0 = (l >> 3) * 3072 + vsw, vw1 = vw0 + 8 * 3072;
  auto issue = [&](int c) {
    const unsigned sx = (rb * 64 + hw * 16) * 1024 + c * 128, sw = (c * 64 + hw * 16) * 3072 + cb * 128, lw = lds0 + (c & 3) * SLOT + hw * 2048;
    dma16<0, false>(rx, vx0, sx, lw);
    dma16<0, false>(rw, vw0, sw, lw + 8192); dma16<0, false>(rw, vw1, sw, lw + 8192 + 1024);
    dma16<0, false>(rx, vx1, sx, lw + 1024);
    dma16<0, false>(rw, vw0, sw + 1024, lw + 16384); dma16<0, false>(rw, vw1, sw + 1024, lw + 16384 + 1024);
    dma16<0, false>(rw, vw0, sw + 2048, lw + 24576); dma16<0, false>(rw, vw1, sw + 2048, lw + 24576 + 1024);
  };
  if (w >= 4) issue(0);
  asm volatile("s_barrier" ::: "memory");
  if (w >= 4) {
    issue(1);
    STAMP(1);
    const unsigned bit = hw >> 1, hf = hw & 1, big3 = b < 128;
    const unsigned bp = big3 ? F * 2 : 1024, bso = big3 ? (b >> 2) * 16 * (F * 2) + (b & 3) * 7 * 128 : ((b - 128) >> 3) * 7 * 16384 + (b & 7) * 128, bst = big3 ? 128 : 16384;
    const unsigned bdo = big3 ? TW_3 + (((b & 3) * 14 + hf) * 32 + (b >> 2)) * 1024 : TW_4 + (((b & 7) * 2 + hf) * 112 + ((b - 128) >> 3) * 7) * 1024, bdt = big3 ? 65536 : 1024;
    const unsigned sso = ((b >> 3) * 16 + hf * 8) * 1024 + (b & 7) * 128, sdo = (((b & 7) * 2 + hf) * 32 + (b >> 3)) * 1024;
    const rsrc_t rbig = mkrsrc(big3 ? w3 : w4, D * F * 2);
    const unsigned vs = (l >> 3) * 1024 + vsw, vb = (l >> 3) * bp + vsw;
    const rsrc_t rp0 = mkrsrc(bit ? w1 : wo, D * D * 2), rp1 = mkrsrc(bit ? (big3 ? w3 : w4) : w2, bit ? D * F * 2 : D * D * 2);
    const unsigned vp1 = bit ? vb : vs, bsh = bso + hf * 8 * bp, sp1 = bit ? bsh : sso, dp1 = bit ? bdo : TW_2 + sdo;
    auto piece = [&](int i) {
      const unsigned lc = lds0 + LC + (2 * i + bit) * 2048 + hf * 1024;
      if (i == 0) dma16<0, false>(rp0, vs, sso, lc);
      else if (i == 1) dma16<0, false>(rp1, vp1, sp1, lc);
      else dma16<0, false>(rbig, vb, bsh + (2 * i - 3 + bit) * bst, lc);
    };
    float s0 = 0.f, s1 = 0.f, q0 = 0.f, q1 = 0.f;
    const unsigned xs = lds0 + hw * 2048 + l * 16;
#pragma unroll
    for (int c = 0; c < 8; ++c) {
      if (c == 0) waitv<8>(); else if (c == 1) waitv<17>(); else if (c < 6) waitv<18>(); else if (c == 6) waitv<9>(); else waitv<0>();
      BAR();
      STAMP(4 + c);
      const u32x4 a0 = __builtin_bit_cast(u32x4, lds128(xs + (c & 3) * SLOT)), a1 = __builtin_bit_cast(u32x4, lds128(xs + 1024 + (c & 3) * SLOT));
      if (c == 0) issue(2);
      if (c < 5) { issue(c + 3); piece(c); }
#pragma unroll
      for (int j = 0; j < 4; ++j) { s0 = dot2(a0[j], 0x3f803f80u, s0); q0 = dot2(a0[j], a0[j], q0); s1 = dot2(a1[j], 0x3f803f80u, s1); q1 = dot2(a1[j], a1[j], q1); }
    }
    const float m0_ = sum8(s0) * (1.f / D), m1_ = sum8(s1) * (1.f / D);
    const float r0 = __builtin_amdgcn_rsqf(fmaxf(sum8(q0) * (1.f / D) - m0_ * m0_, 0.f) + EPS), r1 = __builtin_amdgcn_rsqf(fmaxf(sum8(q1) * (1.f / D) - m1_ * m1_, 0.f) + EPS);
    if ((l & 7) == 0) {
      LAS float* t = (LAS float*)(uintptr_t)(lds0 + LT + (hw * 16 + (l >> 3)) * 4);
      t[0] = m0_; t[8] = m1_; t[64] = r0; t[72] = r1;
    }
    BAR();
    const unsigned ta = lds0 + LC + bit * 2048 + (4 * h + q) * 128 + (((4 * hf + 2 * G + (p4 >> 1)) ^ ((q >> 1) << 2)) << 4) + 8 * (p4 & 1);
#pragma unroll
    for (int i = 0; i < 5; ++i) {
      const unsigned dst = i == 0 ? TW_O + bit * 524288 + sdo : i == 1 ? dp1 : bdo + (2 * i - 3 + bit) * bdt;
      st128s(rws, l * 16, dst, __builtin_bit_cast(u32x4, trfrag(ta + i * 4096, ta + i * 4096 + 1024)));
    }
    STAMP(2);
    if (w == 4) { unsigned* fz = (unsigned*)(ws + WS_FLAG) + b * 161; fz[l] = 0u; fz[64 + l] = 0u; if (l < 33) fz[128 + l] = 0u; }
  } else {
    const unsigned tt = w & 1, hp = w >> 1;
    unsigned wm0, wm1;
    if (w < 3) {
      const unsigned vo = (l >> 1) * 3072 + (l & 1) * 64, so = (128 + (r >> 2) * 48) * 3072 + w * 1024 + cb * 128;
      asm volatile("buffer_load_dword %0, %2, %3, %4 offen\n\tbuffer_load_dword %1, %2, %3, %5 offen" : "=&v"(wm0), "=&v"(wm1) : "v"(vo), "s"(rw), "s"(so), "s"(so + 16 * 3072) : "memory");
    } else {
      const unsigned vo = (l >> 2) * 1024 + (l & 3) * 64, so = (rb * 64 + (r & 3) * 16) * 1024;
      asm volatile("buffer_load_dword %0, %2, %3, %4 offen offset:256\n\tbuffer_load_dword %1, %2, %3, %4 offen offset:512\n\tbuffer_load_dword %0, %2, %3, %4 offen offset:768"
                   : "=&v"(wm0), "=&v"(wm1) : "v"(vo), "s"(rx), "s"(so) : "memory");
    }
    STAMP(1);
    unsigned xa[2][4];
#pragma unroll
    for (int s = 0; s < 4; ++s) {
      xa[0][s] = lds0 + (tt * 4 + (l31 >> 3)) * 1024 + (l31 & 7) * 128 + (((2 * s + h) ^ ((l31 & 6) | ((l31 >> 3) & 1))) << 4); xa[1][s] = xa[0][s] + 2 * SLOT;
      TOUCH(xa[0][s]); TOUCH(xa[1][s]);
    }
    unsigned wa0 = lds0 + 8192 + (8 * h + q) * 128 + (((4 * hp + 2 * G + (p4 >> 1)) ^ ((q >> 1) << 2)) << 4) + 8 * (p4 & 1), wa1 = wa0 + 2 * SLOT;
    TOUCH(wa0); TOUCH(wa1);
    f32x16 aq = {}, ak = {}, av = {};
    float cq = 0.f, ck = 0.f, cv = 0.f, dq = 0.f, dk = 0.f, dv = 0.f;
    auto body = [&](auto TT) {
      bf16x8 f[2][4];
      auto wadr = [&](int s, int p) { return ((s & 8) ? wa1 : wa0) + ((s >> 2) & 1) * SLOT + p * 8192 + (s & 3) * 2048; };
      auto xadr = [&](int s) { return xa[(s >> 3) & 1][s & 3] + ((s >> 2) & 1) * SLOT; };
      auto cs2 = [&](const bf16x8& v, float& c, float& d) {
        const u32x4 u = __builtin_bit_cast(u32x4, v); c = dot2(u[2 * TT.v], 0x3f803f80u, c); d = dot2(u[2 * TT.v + 1], 0x3f803f80u, d); };
      BAR();
      STAMP(4);
      f[0][0] = lds128(xadr(0)); f[0][1] = trfrag(wadr(0, 0), wadr(0, 0) + 512); f[0][2] = trfrag(wadr(0, 1), wadr(0, 1) + 512); f[0][3] = trfrag(wadr(0, 2), wadr(0, 2) + 512);
#pragma unroll
      for (int s = 0; s < 32; ++s) {
        const bf16x8* o = f[s & 1];
        bf16x8* n = f[(s + 1) & 1];
        if ((s & 3) == 3 && s < 31) { BAR(); STAMP(5 + (s >> 2)); }
        SB();
        aq = MFMA32(o[1], o[0], aq); TOUCH(aq);
        if (s < 31) { n[0] = lds128(xadr(s + 1)); n[1] = trfrag(wadr(s + 1, 0), wadr(s + 1, 0) + 512); }
        cs2(o[1], cq, dq);
        SB();
        ak = MFMA32(o[2], o[0], ak); TOUCH(ak);
        if (s < 31) n[2] = trfrag(wadr(s + 1, 1), wadr(s + 1, 1) + 512);
        cs2(o[2], ck, dk);
        SB();
        av = MFMA32(o[0], o[3], av); TOUCH(av);
        if (s < 31) n[3] = trfrag(wadr(s + 1, 2), wadr(s + 1, 2) + 512);
        cs2(o[3], cv, dv);
        SB();
      }
    };
    if (tt) body(IC<1>{}); else body(IC<0>{});
    asm volatile("s_waitcnt vmcnt(0)" :: "v"(wm0), "v"(wm1) : "memory");
    STAMP(12);
    cq = swapadd(cq + dq); ck = swapadd(ck + dk); cv = swapadd(cv + dv);
    LAS float* ct = (LAS float*)(uintptr_t)(lds0 + LT + 512 + l31 * 4);
    if (h == 0) { ct[w * 96] = cq; ct[w * 96 + 32] = ck; ct[w * 96 + 64] = cv; }
    BAR();
    cq += ct[(w ^ 1) * 96]; ck += ct[(w ^ 1) * 96 + 32]; cv += ct[(w ^ 1) * 96 + 64];
    const LAS float* st = (const LAS float*)(uintptr_t)(lds0 + LT + tt * 128);
    const float mu = st[l31], rs = st[64 + l31];
    f32x4 rv[4];
#pragma unroll
    for (int j = 0; j < 4; ++j) rv[j] = *(const LAS f32x4*)(st + 64 + 8 * j + 4 * h);
    const bf16x8 mo = split_op(-mu, false, true);
    aq = MFMA32(split_op(cq, true, h == 0), mo, aq); ak = MFMA32(split_op(ck, true, h == 0), mo, ak); av = MFMA32(mo, split_op(cv, true, h == 0), av);
    const float qs = rs * 0.36067376f;
    float n0 = 0.f, n1 = 0.f;
#pragma unroll
    for (int i = 0; i < 16; ++i) { aq[i] *= qs; ak[i] *= rs; av[i] *= rv[i >> 2][i & 3]; }
    const unsigned head = cb * 4 + hp * 2, t32 = rb * 2 + tt;
    const unsigned vq = (h * 2048 + l31) * 16, sq = WS_Q + head * 65536 + t32 * 512;
    const unsigned vk = h * 512 + ((l31 & 19) | ((l31 & 4) << 1) | ((l31 & 8) >> 1)) * 16, sk = WS_KV + head * 131072 + t32 * 2048;
    const unsigned vv = G * 131072 + 1024 + h * 512 + (l & 15) * 16;
    st128s(rws, vk, sk, pack8(ak, 0)); st128s(rws, vk, sk + 131072, pack8(ak, 8));
    st128s(rws, vv, sk, pack8(av, 0)); st128s(rws, vv + 256, sk, pack8(av, 8));
    st128s(rws, vq, sq, pack8(aq, 0)); st128s(rws, vq, sq + 65536, pack8(aq, 8));
#pragma unroll
    for (int i = 0; i < 8; ++i) { n0 += ak[i] * ak[i]; n1 += ak[8 + i] * ak[8 + i]; }
    n0 = swap16max(max16(swapadd(n0))); n1 = swap16max(max16(swapadd(n1)));
    if (l == 0) { float* km = (float*)(ws + WS_KMAX) + head * 64 + t32; km[0] = n0; km[64] = n1; }
  }
  STAMP(3); STAMPC(41); STDUMP(0);
}

DI void poll_flags(const unsigned* p, bool need) {
#pragma unroll 1
  for (unsigned sp = 0; sp < (1u << 20); ++sp) {
    const unsigned v = need ? __hip_atomic_load(p, RLX_AGENT) : 1u;
    if (__all(v != 0u)) break;
    __builtin_amdgcn_s_sleep(1);
  }
  asm volatile("" ::: "memory");
}
DI void set_flag(unsigned* p, int l) { drain(); if (l == 0) __hip_atomic_store(p, 1u, RLX_AGENT); }
template <int ST> DI void lds_stats(unsigned a, float& mu, float& var) {
  float s = 0.f, q = 0.f;
#pragma unroll
  for (int i = 0; i < 4; ++i) { const f32x4 v = lds128f(a + (i & 1) * 512 + (i >> 1) * ST); s += v[0] + v[2]; q += v[1] + v[3]; }
  s = swapadd(s); q = swapadd(q);
  mu = s * (1.f / D); var = fmaxf(q * (1.f / D) - mu * mu, 0.f);
}
DI void put_stats(rsrc_t rs, unsigned vl, unsigned soff, const f32x16& v) {
  float s = 0.f, q = 0.f;
#pragma unroll
  for (int i = 0; i < 16; ++i) { s += v[i]; q += v[i] * v[i]; }
  s = swapadd(s); q = swapadd(q);
  if (vl < 512) __builtin_amdgcn_raw_buffer_store_b64((u32x2){__float_as_uint(s), __float_as_uint(q)}, rs, vl, soff, 16);
}
DI f32x16 unpack16(const u32x4& p0, const u32x4& p1) {
  f32x16 o;
#pragma unroll
  for (int j = 0; j < 4; ++j) { o[2 * j] = bflo(p0[j]); o[2 * j + 1] = bfhi(p0[j]); o[8 + 2 * j] = bflo(p1[j]); o[8 + 2 * j + 1] = bfhi(p1[j]); }
  return o;
}
template <bool IO> DI f32x16 gemm32(const bf16x8 (&wf)[32], unsigned ba) {
  f32x16 acc = {};
  bf16x8 f[2][8];
  if (IO) waitv<6>();
  BAR();
#pragma unroll
  for (int k = 0; k < 8; ++k) f[0][k] = lds128(ba + (8 * (k >> 1) + (k & 1)) * 1024);
#pragma unroll
  for (int j = 0; j < 4; ++j) {
    if (IO) { if (j == 0) waitv<4>(); else if (j == 1) waitv<2>(); else if (j == 2) waitv<0>(); }
    BAR();
#pragma unroll
    for (int k = 0; k < 8; ++k) {
      SB();
      acc = MFMA32(wf[8 * (k >> 1) + 2 * j + (k & 1)], f[j & 1][k], acc);
      TOUCH(acc);
      if (j < 3) f[(j + 1) & 1][k] = lds128(ba + (8 * (k >> 1) + 2 * j + 2 + (k & 1)) * 1024);
    }
    SB();
  }
  return acc;
}
DI f32x16 colsum(const bf16x8 (&wf)[32]) {
  u32x4 o = {0x3f803f80u, 0x3f803f80u, 0x3f803f80u, 0x3f803f80u};
  f32x16 acc = {};
  TOUCH(o);
  SB();
#pragma unroll
  for (int k = 0; k < 32; ++k) acc = MFMA32(wf[k], __builtin_bit_cast(bf16x8, o), acc);
  TOUCH(acc);
  SB();
  return acc;
}
DI void tile_dma(rsrc_t rs, unsigned voff, unsigned soff, unsigned lds) { dmaN<4>(rs, voff, soff, lds); dmaN<4>(rs, voff, soff + 4096, lds + 4096); }

#define EX(B, i) "v_exp_f32 v[" #B "+" #i "], v[" #B "+" #i "]\n\t"
#define TILE(C, P, KC, KP, LC, LP, HC, HP, U, X0, X1)                                                 \
  EX(C, 0) EX(C, 1)                                                                                   \
  "v_smfmac_f32_32x32x32_bf16 v[2:17], v[" #HP ":" #HP "+3], v[" #P "+8:" #P "+15], v78\n\t"          \
  EX(C, 2) EX(C, 3) X0 EX(C, 4) EX(C, 5)                                                              \
  "v_mfma_f32_32x32x16_bf16 v[" #P ":" #P "+15], v[" #KP ":" #KP "+3], v[50:53], 0\n\t"               \
  "ds_read_b128 v[" #KC ":" #KC "+3], v79 offset:" #U "*2048+4096\n\t"                                \
  "s_mov_b64 exec, %[msk]\n\t"                                                                        \
  "ds_read_b128 v[" #LP ":" #LP "+3], v79 offset:" #U "*2048+3072\n\t"                                \
  "ds_read_b128 v[" #HC ":" #HC "+3], v79 offset:" #U "*2048+1280\n\t"                                \
  "s_mov_b64 exec, -1\n\t"                                                                            \
  EX(C, 6) EX(C, 7) EX(C, 8) EX(C, 9)                                                                 \
  "v_smfmac_f32_32x32x32_bf16 v[2:17], v[" #LC ":" #LC "+3], v[" #C ":" #C "+7], v78\n\t"             \
  EX(C, 10) EX(C, 11) EX(C, 12) X1 EX(C, 13) EX(C, 14) EX(C, 15)                                      \
  "s_waitcnt lgkmcnt(0)\n\t"
#define TILE4(A0, A1, B0, B1, C0, C1, D0, D1)                                                         \
  TILE(18, 34, 54, 58, 62, 66, 70, 74, 0, A0, A1) TILE(34, 18, 58, 54, 66, 62, 74, 70, 1, B0, B1)     \
  TILE(18, 34, 54, 58, 62, 66, 70, 74, 2, C0, C1) TILE(34, 18, 58, 54, 66, 62, 74, 70, 3, D0, D1) "v_add_u32 v79, 0x2000, v79"
#define ATT_OPS "+{v[2:17]}"(oacc), "+{v[18:33]}"(s0), "+{v[34:49]}"(s1), "+{v[54:57]}"(k0), "+{v[58:61]}"(k1), "+{v[62:65]}"(l0), "+{v[66:69]}"(l1), \
                "+{v[70:73]}"(h0), "+{v[74:77]}"(h1), "+{v79}"(ad)
#define ATT_IN "{v[50:53]}"(qf), "{v78}"(idx), [msk] "s"(msk)
#define WARM "buffer_load_dword v80, %[vl], %[rs], %[sw] offen\n\t"
#define WL(B, i) "buffer_load_dwordx4 v[" #B "+4*" #i ":" #B "+4*" #i "+3], %[vl], %[rs], %[sa] offen offset:" #i "*1024 sc1\n\t"
#define DL(i) "buffer_load_dwordx4 %[vl], %[rs], %[sa] offen offset:" #i "*1024 sc1 lds\n\t"
#define ATT_LD [vl] "v"(vl), [rs] "s"(rws)
#define ATT_1() asm volatile(TILE4("", "", "", "", "", "", "", "") : ATT_OPS : ATT_IN : "memory")
#define ATT_W(B, E, n, g) asm volatile(TILE4(WARM, WL(B, 0), "", WL(B, 1), "", WL(B, 2), "", WL(B, 3)) \
    : ATT_OPS, "+{v80}"(dum), "={v[" #B ":" #E "]}"(wt[n]) : ATT_IN, ATT_LD, [sa] "s"(wso + n * 4096), [sw] "s"(wrm + g * 8192) : "memory")
#define ATT_D0(src, dst) asm volatile("s_mov_b32 m0, %[ld]\n\t" TILE4("", DL(0), "", DL(1), "", DL(2), "", DL(3)) : ATT_OPS : ATT_IN, ATT_LD, [sa] "s"(src), [ld] "s"(dst) : "memory")
#define ATT_D(src, dst, g) asm volatile("s_mov_b32 m0, %[ld]\n\t" TILE4(WARM, DL(0), "", DL(1), "", DL(2), "", DL(3)) \
    : ATT_OPS, "+{v80}"(dum) : ATT_IN, ATT_LD, [sa] "s"(src), [ld] "s"(dst), [sw] "s"(wrm + g * 8192) : "memory")
#define ATT_DH(src, dst, g) asm volatile("s_mov_b32 m0, %[ld]\n\t" TILE4(WARM, DL(0), "", DL(1), "", "", "", "") \
    : ATT_OPS, "+{v80}"(dum) : ATT_IN, ATT_LD, [sa] "s"(src), [ld] "s"(dst), [sw] "s"(wrm + g * 8192) : "memory")

__global__ void __launch_bounds__(512, 2) k2_main(const u16* __restrict__ x, u16* __restrict__ out, char* __restrict__ ws) {
  extern __shared__ __attribute__((aligned(16))) char smem[];
  const unsigned lds0 = (unsigned)(uintptr_t)smem;
  const int b = blockIdx.x;
  const unsigned w = rfl(threadIdx.x >> 6);
  const unsigned xcd = b & 7, r = b >> 3;
  unsigned* flags = (unsigned*)(ws + WS_FLAG);
  const rsrc_t rws = mkrsrc(ws, WS_END);
  const unsigned rq = xcd * 8 + (r >> 2), c4 = r & 3;
  const unsigned rb = xcd * 4 + (r >> 3), c8 = r & 7;
  const unsigned wi = w & 3;
  const unsigned r2a = lds0 + wi * 16384, r2b = lds0 + 65536 + wi * 8192, r2c = lds0 + L_Z + wi * 7168;
  bf16x8 wf[32];
  u32x16 wt[8];
  STDECL; STAMP(0);
  {
    const int l = lane(), h = l >> 5, l31 = l & 31;
    const unsigned vl = l * 16;
    const unsigned head = xcd * 4 + (r & 3), qb = r >> 2;
    const unsigned tok = qb * 256 + w * 32 + l31;
    bf16x8 qf; float km;
    asm volatile("buffer_load_dwordx4 %0, %1, %2, %3 offen" : "={v[50:53]}"(qf) : "v"((h * 2048 + tok) * 16), "s"(rws), "s"(WS_Q + head * 65536) : "memory");
    asm volatile("buffer_load_dword %0, %1, %2, %3 offen" : "=v"(km) : "v"(l * 4), "s"(rws), "s"(WS_KMAX + head * 256) : "memory");
    const unsigned kv = WS_KV + head * 131072;
    dma16<0, true>(rws, vl, kv + w * 1024, lds0 + w * 1024); dma16<0, true>(rws, vl, kv + 8192 + w * 1024, lds0 + 8192 + w * 1024);
    dmaN<4>(rws, vl, kv + 16384 + w * 4096, lds0 + 16384 + w * 4096);
    asm volatile("s_waitcnt vmcnt(6)" : "+{v[50:53]}"(qf), "+v"(km) :: "memory");
    km = swapmax(swap16max(max16(km)));
    float qn = 0.f;
    { const u32x4 u = __builtin_bit_cast(u32x4, qf);
#pragma unroll
      for (int j = 0; j < 4; ++j) qn = dot2(u[j], u[j], qn); }
    qn = swapadd(qn);
    { const unsigned slowf = __all(qn * km * 1.05f <= 48.f * 48.f) ? 0u : 1u;
      if (l == 0) *(LAS unsigned*)(uintptr_t)(lds0 + L_ST + 1024 + w * 4) = slowf; }
    const unsigned cst = l31 == 16 ? 0x3f803f80u : 0u;
    const bf16x8 vc = __builtin_bit_cast(bf16x8, (u32x4){cst, cst, cst, cst});
    f32x16 oacc = {};
    int idx = 0xDDDD;
    unsigned ad = lds0 + h * 512 + l31 * 16;
    const unsigned wso = (w < 4 ? TW_O : TW_1) + (c4 * 4 + wi) * 32768, w2o = TW_2 + (c4 * 4 + wi) * 32768, hw = w >> 2;
    const unsigned wrm = TW_3 + r * 114688 + w * 1024;
    STAMP(1);
    waitv<4>();
    BAR();
    STAMP(2); STAMPC(40);
    const u32x4 sf0 = *(const LAS u32x4*)(uintptr_t)(lds0 + L_ST + 1024), sf1 = *(const LAS u32x4*)(uintptr_t)(lds0 + L_ST + 1040);
    if (rfl(sf0[0] | sf0[1] | sf0[2] | sf0[3] | sf1[0] | sf1[1] | sf1[2] | sf1[3]) == 0u) {
      f32x16 s0, s1 = {};
      bf16x8 k0, k1, l0 = vc, l1 = vc, h0 = vc, h1 = vc;
      const unsigned long long msk = 0x0000ffff0000ffffull;
      asm volatile("ds_read_b128 v[54:57], v79\n\tds_read_b128 v[58:61], v79 offset:2048\n\ts_mov_b64 exec, %[msk]\n\tds_read_b128 v[62:65], v79 offset:1024\n\ts_mov_b64 exec, -1\n\t"
                   "s_waitcnt lgkmcnt(0)\n\tv_mfma_f32_32x32x16_bf16 v[18:33], v[54:57], v[50:53], 0\n\ts_nop 7\n\ts_nop 4"
                   : "={v[18:33]}"(s0), "={v[54:57]}"(k0), "={v[58:61]}"(k1), "+{v[62:65]}"(l0) : "{v79}"(ad), "{v[50:53]}"(qf), [msk] "s"(msk) : "memory");
      unsigned dum = 0;
      STAMPC(44);
      ATT_D0(kv + 49152 + w * 4096, lds0 + 49152 + w * 4096);
      STAMPC(45);
      waitv<4>(); BAR();
      STAMPC(46);
      ATT_D(kv + 81920 + w * 4096, lds0 + 81920 + w * 4096, 0);
      ATT_DH(kv + 114688 + w * 2048, lds0 + 114688 + w * 2048, 1);
      STAMPC(47);
      ATT_W(128, 143, 0, 2);
      ATT_W(144, 159, 1, 3);
      STAMPC(48);
      waitv<10>(); BAR();
      STAMPC(49);
      ATT_W(160, 175, 2, 4);
      ATT_W(176, 191, 3, 5);
      STAMPC(50);
      ATT_W(192, 207, 4, 6);
      ATT_W(208, 223, 5, 7);
      STAMPC(51);
      ATT_W(224, 239, 6, 8);
      ATT_W(240, 255, 7, 9);
      STAMPC(52);
      ATT_D(w2o + 24576 + hw * 3072, r2c + hw * 3072, 10);
      STAMPC(53);
      BAR();
      STAMPC(54);
      ATT_D(w2o + hw * 8192, r2a + hw * 8192, 11);
      ATT_D(w2o + hw * 8192 + 4096, r2a + hw * 8192 + 4096, 12);
      STAMPC(55);
      ATT_D(w2o + 16384 + hw * 4096, r2b + hw * 4096, 13);
      ATT_1();
      STAMPC(56);
      asm volatile("s_waitcnt vmcnt(0)" : "+{v[128:143]}"(wt[0]), "+{v[144:159]}"(wt[1]), "+{v[160:175]}"(wt[2]), "+{v[176:191]}"(wt[3]), "+{v[192:207]}"(wt[4]),
                   "+{v[208:223]}"(wt[5]), "+{v[224:239]}"(wt[6]), "+{v[240:255]}"(wt[7]), "+{v80}"(dum) :: "memory");
      asm volatile("s_nop 1\n\tv_smfmac_f32_32x32x32_bf16 v[2:17], v[74:77], v[42:49], v78\n\ts_nop 7\n\ts_nop 4" : "+{v[2:17]}"(oacc) : "{v[34:49]}"(s1), "{v[74:77]}"(h1), "{v78}"(idx));
    } else {
      tile_dma(rws, vl, kv + 49152 + w * 10240, lds0 + 49152 + w * 10240); dmaN<2>(rws, vl, kv + 49152 + w * 10240 + 8192, lds0 + 49152 + w * 10240 + 8192);
      waitv<0>(); BAR();
      float mx = -3.0e38f;
      const f32x16 zero = {};
#pragma unroll 1
      for (int t = 0; t < 64; ++t) {
        const bf16x8 kf = lds128(ad); bf16x8 v0 = vc, v1 = vc;
        if (l31 < 16) { v0 = lds128(ad + 1024); v1 = lds128(ad + 1280); }
        f32x16 s = MFMA32(kf, qf, zero);
        float tm = s[0];
#pragma unroll
        for (int i = 1; i < 16; ++i) tm = fmaxf(tm, s[i]);
        const float mn = fmaxf(mx, swapmax(tm)), al = __builtin_amdgcn_exp2f(mx - mn);
        mx = mn;
#pragma unroll
        for (int i = 0; i < 16; ++i) { s[i] = __builtin_amdgcn_exp2f(s[i] - mn); oacc[i] *= al; }
        oacc = __builtin_amdgcn_smfmac_f32_32x32x32_bf16(__builtin_bit_cast(bf8_t, v0), __builtin_bit_cast(bf16_t, __builtin_shufflevector(s, s, 0, 1, 2, 3, 4, 5, 6, 7)), oacc, idx, 0, 0);
        oacc = __builtin_amdgcn_smfmac_f32_32x32x32_bf16(__builtin_bit_cast(bf8_t, v1), __builtin_bit_cast(bf16_t, __builtin_shufflevector(s, s, 8, 9, 10, 11, 12, 13, 14, 15)), oacc, idx, 0, 0);
        ad += 2048;
      }
      BAR();
#pragma unroll
      for (int k = 0; k < 32; ++k) { const u32x4 t = __builtin_amdgcn_raw_buffer_load_b128(rws, vl + (k & 3) * 1024, wso + (k >> 2) * 4096, 16);
        wt[k >> 2][(k & 3) * 4] = t[0]; wt[k >> 2][(k & 3) * 4 + 1] = t[1]; wt[k >> 2][(k & 3) * 4 + 2] = t[2]; wt[k >> 2][(k & 3) * 4 + 3] = t[3]; }
      tile_dma(rws, vl, w2o + hw * 8192, r2a + hw * 8192); dmaN<4>(rws, vl, w2o + 16384 + hw * 4096, r2b + hw * 4096); dmaN<4>(rws, vl, w2o + 24576 + hw * 3072, r2c + hw * 3072);
    }
    STAMP(3); STAMPC(41);
    const float inv = __builtin_amdgcn_rcpf(swapadd(h == 0 ? oacc[8] : 0.f));
#pragma unroll
    for (int i = 0; i < 8; ++i) oacc[i] *= inv;
    st128(rws, WS_O + (qb * 8 + w) * 32768 + (head * 2 + h) * 512 + l31 * 16, pack8(oacc, 0));
    set_flag(flags + FLAG_O + ((qb * 8 + w) * 4 + (head >> 3)) * 32 + (head & 7), l);
#pragma unroll
    for (int k = 0; k < 32; ++k) wf[k] = __builtin_bit_cast(bf16x8, (u32x4){wt[k >> 2][(k & 3) * 4], wt[k >> 2][(k & 3) * 4 + 1], wt[k >> 2][(k & 3) * 4 + 2], wt[k >> 2][(k & 3) * 4 + 3]});
    STAMP(4);
    BAR();
  }
  f32x16 cs;
  unsigned wsel = w;
  asm volatile("" : "+s"(wsel));
  const unsigned wv = w < 4 ? w : w - 1;
  {
  const int l = lane(), h = l >> 5, l31 = l & 31;
  const unsigned vl = l * 16;
  unsigned ba = lds0 + L_P + vl;
  TOUCH(ba);
  const unsigned tileo = rq * 32768 + (c4 * 16 + wi * 4) * 512;
  const unsigned stato = rq * 4096 + c4 * 1024 + (wi >> 1) * 512 + (wi & 1) * 8;
  const unsigned pq = lds0 + L_P + wi * 8192;
  auto unstage = [&](unsigned last) {
    const unsigned ua = r2a + vl, ub_ = r2b + vl, uc = r2c + vl;
    ldw<0>(wf[31], rws, vl, last);
    sfor<0, 16>([&](auto K) { ldsw<K.v * 1024>(wf[K.v], ua); });
    sfor<0, 8>([&](auto K) { ldsw<K.v * 1024>(wf[16 + K.v], ub_); });
    sfor<0, 7>([&](auto K) { ldsw<K.v * 1024>(wf[24 + K.v], uc); });
  };
  if (w < 4) {
    u32x2 xr[4];
#pragma unroll
    for (int g = 0; g < 4; ++g) xr[g] = *(const u32x2*)(x + (rq * 32 + l31) * D + c4 * 128 + wi * 32 + 8 * g + 4 * h);
    __builtin_amdgcn_s_setprio(1);
    poll_flags(flags + FLAG_O + (rq * 4 + wi) * 32 + (l & 7), true);
    STAMP(5);
    tile_dma(rws, vl, WS_O + rq * 32768 + wi * 8192, pq);
    STAMP(6);
    f32x16 acc = gemm32<true>(wf, ba);
    STAMP(7);
#pragma unroll
    for (int g = 0; g < 4; ++g) { acc[4 * g] += bflo(xr[g][0]); acc[4 * g + 1] += bfhi(xr[g][0]); acc[4 * g + 2] += bflo(xr[g][1]); acc[4 * g + 3] += bfhi(xr[g][1]); }
    const u32x4 p0 = pack8(acc, 0), p1 = pack8(acc, 8);
    st128s(rws, vl, WS_H + tileo, p0); st128s(rws, vl + 1024, WS_H + tileo, p1);
    f32x16 hres = unpack16(p0, p1);
    put_stats(rws, vl, WS_STH + stato, hres);
    STAMP(8);
    unstage(TW_2 + (c4 * 4 + wi) * 32768 + 31 * 1024);
    set_flag(flags + FLAG_H + (rq * 4 + c4) * 32 + wi, l);
    STAMP(9);
    const unsigned so = TW_3 + (c8 * 7 + (w < 3 ? w : 6)) * 32768;
    asm volatile("s_waitcnt lgkmcnt(0)" ::: "memory");
    tile_dma(rws, vl, so, r2a);
    poll_flags(flags + FLAG_H + (rq * 4 + wi) * 32 + (l & 3), true);
    STAMP(10);
    dmaN<1>(rws, vl, WS_STH + rq * 4096 + wi * 1024, lds0 + L_ST + wi * 1024);
    tile_dma(rws, vl, WS_H + rq * 32768 + wi * 8192, pq);
    __builtin_amdgcn_s_setprio(0);
    waitv<6>(); BAR(); waitv<4>(); BAR(); waitv<2>(); BAR(); waitv<0>(); BAR();
    sfor<8, 31>([&](auto K) {
      constexpr int k = K.v;
      dma16<0, true>(rws, vl, so + k * 1024, k < 16 ? r2a + k * 1024 : k < 24 ? r2b + (k - 16) * 1024 : r2c + (k - 24) * 1024);
      if constexpr (k == 10) BAR();
      waitv<2>();
    });
    STAMP(11);
    float mu, var; { const int l2 = lane(); lds_stats<1024>(lds0 + L_ST + (l2 >> 5) * 2048 + (l2 & 31) * 16, mu, var); }
    const float rs = __builtin_amdgcn_rsqf(var + EPS);
#pragma unroll
    for (int i = 0; i < 16; ++i) hres[i] = (hres[i] - mu) * rs;
    const int dl = (wi & 1) ? (int)(wi >> 1) + 1 : -(int)(wi >> 1) - 1;
    const int rn = (l & 4) ? (int)rq + ((wi & 1) ? 1 : -1) : (int)rq;
    __builtin_amdgcn_s_setprio(1);
    poll_flags(flags + FLAG_G + (rn * 4 + wi) * 32 + (l & 3), rn >= 0 && rn < 64);
    drain();
    STAMP(15);
    { const int l2 = lane(), src = (int)rq * 32 + (l2 & 31) + dl;
      tile_dma(mkrsrc(ws + WS_G, 2u << 20), (src >= 0 && src < S) ? (unsigned)((src >> 5) * 32768 + (src & 31) * 16 + (l2 >> 5) * 512) : 0x7ff00000u, wi * 8192, pq); }
    acc = gemm32<true>(wf, ba);
    STAMP(17);
#pragma unroll
    for (int i = 0; i < 16; ++i) acc[i] += hres[i];
    const u32x4 p2 = pack8(acc, 0), p3 = pack8(acc, 8);
    st128s(rws, vl, WS_X1 + tileo, p2); st128s(rws, vl + 1024, WS_X1 + tileo, p3);
    put_stats(rws, vl, WS_STX + stato, unpack16(p2, p3));
    STAMP(18);
    unstage(TW_3 + (c8 * 7 + wi) * 32768 + 31 * 1024);
    set_flag(flags + FLAG_X + (rq * 4 + c4) * 32 + wi, l);
    STAMP(19);
    poll_flags(flags + FLAG_X + ((rb * 2 + ((l >> 2) & 1)) * 4 + wi) * 32 + (l & 3), true);
    asm volatile("s_waitcnt lgkmcnt(0)" ::: "memory");
    STAMP(21);
#pragma unroll
    for (int j = 0; j < 4; ++j) {
      dmaN<2>(rws, vl, WS_X1 + rb * 65536 + wi * 8192 + j * 2048, r2a + j * 2048);
      dmaN<2>(rws, vl, WS_X1 + rb * 65536 + 32768 + wi * 8192 + j * 2048, r2a + 8192 + j * 2048);
    }
    dmaN<1>(rws, vl, WS_STX + rb * 8192 + wi * 1024, r2b); dmaN<1>(rws, vl, WS_STX + rb * 8192 + 4096 + wi * 1024, r2b + 1024);
    cs = colsum(wf);
  }
  if (wsel >= 4) {
    cs = colsum(wf);
    BAR(); BAR(); BAR(); BAR(); BAR();
    __builtin_amdgcn_s_setprio(1);
    f32x16 acc = gemm32<false>(wf, ba);
    STAMP(12);
    const unsigned so = TW_3 + (c8 * 7 + wv) * 32768;
    if (w != 7) sfor<0, 8>([&](auto K) { ldw<(K.v & 3) * 1024>(wf[K.v], rws, vl, so + (K.v >> 2) * 4096); });
    float mu, var; { const int l2 = lane(); lds_stats<1024>(lds0 + L_ST + (l2 >> 5) * 2048 + (l2 & 31) * 16, mu, var); }
    const float rs = __builtin_amdgcn_rsqf(var + EPS), cz = rs * __builtin_amdgcn_rsqf(var * rs * rs + EPS);
#pragma unroll
    for (int i = 0; i < 16; ++i) acc[i] = gelu((acc[i] - mu * cs[i]) * cz);
    st128s(rws, vl, WS_G + tileo, pack8(acc, 0)); st128s(rws, vl + 1024, WS_G + tileo, pack8(acc, 8));
    STAMP(13);
    set_flag(flags + FLAG_G + (rq * 4 + c4) * 32 + wi, l);
    __builtin_amdgcn_s_setprio(0);
    STAMP(14);
    if (w != 7) sfor<8, 18>([&](auto K) { ldw<(K.v & 3) * 1024>(wf[K.v], rws, vl, so + (K.v >> 2) * 4096); waitv<3>(); });
    STAMP(16);
    BAR();
    if (wsel == 7) unstage(so + 31 * 1024);
    BAR(); BAR(); BAR();
    if (w != 7) sfor<18, 21>([&](auto K) { ldw<(K.v & 3) * 1024>(wf[K.v], rws, vl, so + (K.v >> 2) * 4096); });
    BAR();
    if (w != 7) sfor<21, 32>([&](auto K) { ldw<(K.v & 3) * 1024>(wf[K.v], rws, vl, so + (K.v >> 2) * 4096); waitv<3>(); });
    waitv<0>();
    cs = colsum(wf);
    STAMP(20);
  }
  }
#define KS5(t) (8 * (((t) >> 1) & 3) + 2 * ((t) >> 3) + ((t) & 1))
#define XA5(t) (bb + (KS5(t) >> 3) * 16384 + (KS5(t) & 7) * 1024)
#define W4LD(k) ldw<(((k) % 14) & 3) * 1024>(wf[KS5(k)], rws, vl, so4 + ((k) / 14) * 114688 + (((k) % 14) >> 2) * 4096)
  u32x4 rh, rx1;
  const bool early = w < 4 || w == 7;
  {
    int l = lane();
    const int h = l >> 5, l31 = l & 31;
    const unsigned vl = l * 16;
    const unsigned so4 = TW_4 + (c8 * 2 * 112 + w * 14) * 1024;
    const unsigned bb = lds0 + vl;
    auto body = [&](auto EARLY) {
      constexpr bool E = EARLY.v;
      f32x16 a0 = {}, a1 = {};
      bf16x8 f[3][2];
      const unsigned uo = (h * 64 + l31) * 16, us = WS_U + rb * (224 * 1024) + (c8 * 28 + wv * 4) * 1024;
      if (!E) { BAR(); BAR(); BAR(); }
      f[0][0] = lds128(XA5(0)); f[0][1] = lds128(XA5(0) + 8192); f[1][0] = lds128(XA5(1)); f[1][1] = lds128(XA5(1) + 8192);
      sfor<0, 32>([&](auto T) {
        constexpr int t = T.v, nl = t < 5 ? 0 : (t - 4) - (t - 4) / 4;
        if constexpr (E && (t == 6 || t == 14 || t == 22)) { waitv<(t == 6 ? 10 : t == 14 ? 6 : 0) + nl>(); BAR(); }
        SB();
        a0 = MFMA32(wf[KS5(t)], f[t % 3][0], a0);
        TOUCH(a0);
        if constexpr (t < 30) f[(t + 2) % 3][0] = lds128(XA5(t + 2));
        SB();
        a1 = MFMA32(wf[KS5(t)], f[t % 3][1], a1);
        TOUCH(a1);
        if constexpr (t < 30) f[(t + 2) % 3][1] = lds128(XA5(t + 2) + 8192);
        if constexpr (t >= 4 && (t & 3) != 3) W4LD(nl);
        SB();
      });
      STAMP(22);
      float mu0, mu1, v0, v1;
      lds_stats<8192>(lds0 + 65536 + h * 16384 + l31 * 16, mu0, v0); lds_stats<8192>(lds0 + 65536 + 1024 + h * 16384 + l31 * 16, mu1, v1);
      const float rs0 = __builtin_amdgcn_rsqf(v0 + EPS), rs1 = __builtin_amdgcn_rsqf(v1 + EPS);
      __builtin_amdgcn_s_setprio(0);
      sfor<0, 7>([&](auto K) { W4LD(21 + K.v); });
#pragma unroll
      for (int i = 0; i < 8; ++i) a0[i] = gelu((a0[i] - mu0 * cs[i]) * rs0);
      st128s(rws, uo, us, pack8(a0, 0));
#pragma unroll
      for (int i = 0; i < 8; ++i) a1[i] = gelu((a1[i] - mu1 * cs[i]) * rs1);
      st128s(rws, uo + 512, us, pack8(a1, 0));
#pragma unroll
      for (int i = 8; i < 16; ++i) a0[i] = gelu((a0[i] - mu0 * cs[i]) * rs0);
      st128s(rws, uo + 2048, us, pack8(a0, 8));
#pragma unroll
      for (int i = 8; i < 16; ++i) a1[i] = gelu((a1[i] - mu1 * cs[i]) * rs1);
      st128s(rws, uo + 2048 + 512, us, pack8(a1, 8));
      STAMP(23);
    };
    waitv<14>(); BAR();
    STAMP(31);
    unsigned ws2 = w;
    asm volatile("" : "+s"(ws2));
    if (w < 3 || w == 7) { __builtin_amdgcn_s_setprio(2); body(IC<1>{}); }
    if (ws2 - 4 < 3) { __builtin_amdgcn_s_setprio(1); body(IC<0>{}); }
    if (wsel == 3) {
      waitv<10>(); BAR(); waitv<6>(); BAR(); waitv<0>(); BAR();
      sfor<0, 28>([&](auto K) { W4LD(K.v); waitv<1>(); });
    }
    if (!early) BAR();
    l = lane();
    set_flag(flags + FLAG_U + (rb * 8 + c8) * 32 + (w == 3 ? 7 : wv), l);
    { const unsigned ro = (rb * 2 + (w >> 2)) * 32768 + (c8 * 8 + (w & 1) * 4 + ((w >> 1) & 1) * 2) * 512;
      rh = __builtin_amdgcn_raw_buffer_load_b128(rws, l * 16, WS_H + ro, 16); rx1 = __builtin_amdgcn_raw_buffer_load_b128(rws, l * 16, WS_X1 + ro, 16); }
    STAMP(24);
  }
  {
    const int l = lane(), h = l >> 5, l31 = l & 31;
    const unsigned vl = l * 16, vu = (h * 64 + l31) * 16, ub = WS_U + rb * (224 * 1024) + w * 28672;
    f32x16 ac[4] = {};
    bf16x8 f[3][4];
    auto ldu = [&](auto R, auto P) { sfor<0, 4>([&](auto J) { ldn<(J.v >> 1) * 2048 + (J.v & 1) * 512>(f[R.v][J.v], rws, vu, ub + P.v * 4096); }); };
    auto mm = [&](auto R, auto P) {
      sfor<0, 8>([&](auto J) { constexpr int ks = J.v >> 2, tt = (J.v >> 1) & 1, nt = J.v & 1; ac[tt * 2 + nt] = MFMA32(wf[KS5(nt * 14 + P.v * 2 + ks)], f[R.v][ks * 2 + tt], ac[tt * 2 + nt]); });
      TOUCH(ac[0]); TOUCH(ac[1]); TOUCH(ac[2]); TOUCH(ac[3]);
      SB();
    };
    const unsigned* fu = flags + FLAG_U + (rb * 8 + w) * 32;
    poll_flags(fu + ((l & 3) == 3 ? 6 : (l & 3)), true);
    drain();
    STAMP(26);
    ldu(IC<0>{}, IC<0>{}); ldu(IC<1>{}, IC<1>{}); ldu(IC<2>{}, IC<2>{});
    got4<8>(f[0]);
    STAMP(35);
    mm(IC<0>{}, IC<0>{});
    ldu(IC<0>{}, IC<6>{});
    got4<8>(f[1]); mm(IC<1>{}, IC<1>{});
    got4<4>(f[2]); mm(IC<2>{}, IC<2>{});
    got4<0>(f[0]); mm(IC<0>{}, IC<6>{});
    STAMP(36);
    if (early) BAR();
    poll_flags(fu + 3 + ((l & 3) == 3 ? 0 : (l & 3)), true);
    STAMP(37);
    ldu(IC<0>{}, IC<3>{}); ldu(IC<1>{}, IC<4>{}); ldu(IC<2>{}, IC<5>{});
    const unsigned pw = lds0 + w * 16384 + vl, me = (w >> 2) * 4 + (w & 1) * 2 + ((w >> 1) & 1);
    f32x4 s0 = {bflo(rh[0]) + bflo(rx1[0]), bfhi(rh[0]) + bfhi(rx1[0]), bflo(rh[1]) + bflo(rx1[1]), bfhi(rh[1]) + bfhi(rx1[1])};
    f32x4 s1 = {bflo(rh[2]) + bflo(rx1[2]), bfhi(rh[2]) + bfhi(rx1[2]), bflo(rh[3]) + bflo(rx1[3]), bfhi(rh[3]) + bfhi(rx1[3])};
    got4<8>(f[0]); got4<4>(f[1]); got4<0>(f[2]);
    STAMP(25);
    sfor<0, 4>([&](auto Q) {
      constexpr int tt = Q.v >> 1, nt = Q.v & 1;
      sfor<0, 6>([&](auto J) { constexpr int rr = J.v >> 1, ks = J.v & 1; ac[Q.v] = MFMA32(wf[KS5(nt * 14 + (3 + rr) * 2 + ks)], f[rr][ks * 2 + tt], ac[Q.v]); });
      TOUCH(ac[Q.v]);
      SB();
      if constexpr (Q.v > 0) {
        const f32x16& v = ac[Q.v - 1];
#pragma unroll
        for (int j = 0; j < 4; ++j) *(LAS f32x4*)(uintptr_t)(pw + (Q.v - 1) * 4096 + j * 1024) = (f32x4){v[4 * j], v[4 * j + 1], v[4 * j + 2], v[4 * j + 3]};
        SB();
      }
    });
    STAMP(27);
#pragma unroll
    for (int j = 0; j < 4; ++j) *(LAS f32x4*)(uintptr_t)(pw + 3 * 4096 + j * 1024) = (f32x4){ac[3][4 * j], ac[3][4 * j + 1], ac[3][4 * j + 2], ac[3][4 * j + 3]};
    BAR();
    { const unsigned a = lds0 + me * 2048 + vl;
#pragma unroll
      for (int k = 0; k < 8; ++k) { s0 += lds128f(a + (k & 3) * 16384 + (k >> 2) * 65536); s1 += lds128f(a + 1024 + (k & 3) * 16384 + (k >> 2) * 65536); } }
    const auto e0 = __builtin_amdgcn_permlane32_swap(pk2(s0[0], s0[1]), pk2(s1[0], s1[1]), false, false);
    const auto e1 = __builtin_amdgcn_permlane32_swap(pk2(s0[2], s0[3]), pk2(s1[2], s1[3]), false, false);
    *(u32x4*)(out + (rb * 64 + (w >> 2) * 32 + l31) * D + c8 * 64 + (w & 1) * 32 + ((w >> 1) & 1) * 16 + h * 8) = (u32x4){e0[0], e1[0], e0[1], e1[1]};
  }
  STAMPC(42); STAMP(28);
  { const int l = lane(); (void)l; STDUMP(1); }
}

extern "C" void kernel_launch(void* const* d_in, const int* in_sizes, int n_in, void* d_out, int out_size, void* d_ws, size_t ws_size, hipStream_t stream) {
  const u16* x = (const u16*)d_in[0]; const u16* wqkv = (const u16*)d_in[1]; const u16* wo = (const u16*)d_in[2];
  const u16* w1 = (const u16*)d_in[4]; const u16* w2 = (const u16*)d_in[5]; const u16* w3 = (const u16*)d_in[6]; const u16* w4 = (const u16*)d_in[7];
  (void)hipFuncSetAttribute((const void*)k1_qkv, hipFuncAttributeMaxDynamicSharedMemorySize, LDS_BYTES);
  (void)hipFuncSetAttribute((const void*)k2_main, hipFuncAttributeMaxDynamicSharedMemorySize, LDS_BYTES);
  k1_qkv<<<256, 512, LDS_BYTES, stream>>>(x, wqkv, wo, w1, w2, w3, w4, (char*)d_ws);
  k2_main<<<256, 512, LDS_BYTES, stream>>>(x, (u16*)d_out, (char*)d_ws);
}
```

```cpp
#define STAMPS
#define PKA 0
#define PIA 0
#define PAMAX 0
#define PKB 1
#define PIB 28
#define PM 0
#include <hip/hip_runtime.h>
#include <stdint.h>

typedef unsigned short u16;
typedef __attribute__((ext_vector_type(8))) short bf16x8;
typedef __attribute__((ext_vector_type(4))) short s16x4;
typedef __attribute__((ext_vector_type(8))) __bf16 bf8_t;
typedef __attribute__((ext_vector_type(16))) __bf16 bf16_t;
typedef __attribute__((ext_vector_type(2))) __bf16 bf2_t;
typedef __attribute__((ext_vector_type(16))) float f32x16;
typedef __attribute__((ext_vector_type(8))) float f32x8;
typedef __attribute__((ext_vector_type(4))) float f32x4;
typedef __attribute__((ext_vector_type(2))) float f32x2;
typedef __attribute__((ext_vector_type(16))) unsigned u32x16;
typedef __attribute__((ext_vector_type(4))) unsigned u32x4;
typedef __attribute__((ext_vector_type(2))) unsigned u32x2;
typedef __amdgpu_buffer_rsrc_t rsrc_t;
#define LAS __attribute__((address_space(3)))
#define DI __device__ __forceinline__
#define RLX_AGENT __ATOMIC_RELAXED, __HIP_MEMORY_SCOPE_AGENT

constexpr int S = 2048, D = 512, F = 1792;
constexpr float EPS = 1e-5f;
constexpr unsigned WS_FLAG = 0;
constexpr unsigned WS_KMAX = 196608;
constexpr unsigned WS_STH  = 262144;
constexpr unsigned WS_STX  = WS_STH + 262144;
constexpr unsigned WS_Q    = 1u << 20;
constexpr unsigned WS_KV   = WS_Q + (2u << 20);
constexpr unsigned WS_O    = WS_KV + (4u << 20);
constexpr unsigned WS_H    = WS_O + (2u << 20);
constexpr unsigned WS_G    = WS_H + (2u << 20);
constexpr unsigned WS_X1   = WS_G + (2u << 20);
constexpr unsigned WS_U    = WS_X1 + (2u << 20);
constexpr unsigned WS_TW   = WS_U + 32u * 224 * 1024;
constexpr unsigned TW_O = WS_TW, TW_1 = TW_O + 524288, TW_2 = TW_1 + 524288, TW_3 = TW_2 + 524288, TW_4 = TW_3 + 1835008;
constexpr unsigned WS_END  = TW_4 + 1835008;
constexpr int FLAG_O = 0, FLAG_H = 8192, FLAG_G = 16384, FLAG_X = 24576, FLAG_U = 32768, FLAG_W = 40960;

constexpr int LDS_BYTES = 163840;
constexpr unsigned L_P = 96 * 1024, L_Z = 128 * 1024, L_ST = 156 * 1024;

#define STAMP(i) do { unsigned t_ = (unsigned)__builtin_amdgcn_s_memrealtime(); asm volatile("v_writelane_b32 %0, %1, %2" : "+v"(stv) : "s"(t_), "i"(i)); } while (0)
#define STAMPC(i) do { unsigned t_ = (unsigned)__builtin_amdgcn_s_memtime(); asm volatile("v_writelane_b32 %0, %1, %2" : "+v"(stv) : "s"(t_), "i"(i)); } while (0)
#define STDECL unsigned stv = 0
#define STDUMP(k) ((unsigned*)(ws + (64u << 20)))[(((k) * 256 + b) * 8 + w) * 64 + l] = stv

DI rsrc_t mkrsrc(const void* p, unsigned bytes) { return __builtin_amdgcn_make_buffer_rsrc((void*)p, 0, bytes, 0x00020000); }
DI unsigned rfl(unsigned v) { return __builtin_amdgcn_readfirstlane(v); }
DI int lane() { int v; asm volatile("v_mbcnt_lo_u32_b32 %0, -1, 0\n\tv_mbcnt_hi_u32_b32 %0, -1, %0" : "=v"(v)); return v; }
#define MFMA32(a, b, c) __builtin_amdgcn_mfma_f32_32x32x16_bf16((a), (b), (c), 0, 0, 0)
#define MFMA16(a, b, c) __builtin_amdgcn_mfma_f32_16x16x32_bf16((a), (b), (c), 0, 0, 0)
#define BAR() asm volatile("s_waitcnt lgkmcnt(0)\n\ts_barrier" ::: "memory")
template <int N> DI void waitv() { asm volatile("s_waitcnt vmcnt(%0)" ::"i"(N) : "memory"); }
#define TOUCH(x) asm volatile("" : "+v"(x))
#define SB() __builtin_amdgcn_sched_barrier(0)
DI void drain() { waitv<0>(); __builtin_amdgcn_s_waitcnt(0x0F70); }

template <int OFF, bool SC1> DI void dma16(rsrc_t rs, unsigned voff, unsigned soff, unsigned lds) {
  if constexpr (SC1) asm volatile("s_mov_b32 m0, %0\n\ts_nop 0\n\tbuffer_load_dwordx4 %1, %2, %3 offen offset:%4 sc1 lds" ::"s"(lds), "v"(voff), "s"(rs), "s"(soff), "i"(OFF) : "memory");
  else asm volatile("s_mov_b32 m0, %0\n\ts_nop 0\n\tbuffer_load_dwordx4 %1, %2, %3 offen offset:%4 lds" ::"s"(lds), "v"(voff), "s"(rs), "s"(soff), "i"(OFF) : "memory");
}
template <int N> DI void dmaN(rsrc_t rs, unsigned voff, unsigned soff, unsigned lds) {
  if constexpr (N == 1) dma16<0, true>(rs, voff, soff, lds);
  else if constexpr (N == 2) asm volatile("s_mov_b32 m0, %0\n\ts_nop 0\n\tbuffer_load_dwordx4 %1, %2, %3 offen sc1 lds\n\tbuffer_load_dwordx4 %1, %2, %3 offen offset:1024 sc1 lds" ::"s"(lds), "v"(voff), "s"(rs), "s"(soff) : "memory");
  else if constexpr (N == 3) asm volatile("s_mov_b32 m0, %0\n\ts_nop 0\n\tbuffer_load_dwordx4 %1, %2, %3 offen sc1 lds\n\tbuffer_load_dwordx4 %1, %2, %3 offen offset:1024 sc1 lds\n\tbuffer_load_dwordx4 %1, %2, %3 offen offset:2048 sc1 lds" ::"s"(lds), "v"(voff), "s"(rs), "s"(soff) : "memory");
  else asm volatile("s_mov_b32 m0, %0\n\ts_nop 0\n\tbuffer_load_dwordx4 %1, %2, %3 offen sc1 lds\n\tbuffer_load_dwordx4 %1, %2, %3 offen offset:1024 sc1 lds\n\tbuffer_load_dwordx4 %1, %2, %3 offen offset:2048 sc1 lds\n\tbuffer_load_dwordx4 %1, %2, %3 offen offset:3072 sc1 lds" ::"s"(lds), "v"(voff), "s"(rs), "s"(soff) : "memory");
}
DI bf16x8 lds128(unsigned a) { return *(const LAS bf16x8*)(uintptr_t)a; }
DI f32x4 lds128f(unsigned a) { return *(const LAS f32x4*)(uintptr_t)a; }
DI s16x4 ldstr(unsigned a) { return __builtin_amdgcn_ds_read_tr16_b64_v4i16((LAS s16x4*)(uintptr_t)a); }
DI bf16x8 trfrag(unsigned a0, unsigned a1) { s16x4 lo = ldstr(a0), hi = ldstr(a1); return __builtin_shufflevector(lo, hi, 0, 1, 2, 3, 4, 5, 6, 7); }
DI unsigned pk2(float lo, float hi) { f32x2 v = {lo, hi}; bf2_t r = __builtin_convertvector(v, bf2_t); return __builtin_bit_cast(unsigned, r); }
DI float bflo(unsigned u) { return __uint_as_float(u << 16); }
DI float bfhi(unsigned u) { return __uint_as_float(u & 0xffff0000u); }
DI float swapadd(float v) { auto r = __builtin_amdgcn_permlane32_swap(__float_as_uint(v), __float_as_uint(v), false, false); return __uint_as_float(r[0]) + __uint_as_float(r[1]); }
DI float swapmax(float v) { auto r = __builtin_amdgcn_permlane32_swap(__float_as_uint(v), __float_as_uint(v), false, false); return fmaxf(__uint_as_float(r[0]), __uint_as_float(r[1])); }
DI float swap16add(float v) { auto r = __builtin_amdgcn_permlane16_swap(__float_as_uint(v), __float_as_uint(v), false, false); return __uint_as_float(r[0]) + __uint_as_float(r[1]); }
DI float swap16max(float v) { auto r = __builtin_amdgcn_permlane16_swap(__float_as_uint(v), __float_as_uint(v), false, false); return fmaxf(__uint_as_float(r[0]), __uint_as_float(r[1])); }
template <int CTRL> DI float dppf(float v) { return __uint_as_float(__builtin_amdgcn_update_dpp(0, __float_as_uint(v), CTRL, 0xf, 0xf, true)); }
DI float max16(float v) { v = fmaxf(v, dppf<0xB1>(v)); v = fmaxf(v, dppf<0x4E>(v)); v = fmaxf(v, dppf<0x141>(v)); return fmaxf(v, dppf<0x140>(v)); }
DI float sum8(float v) { v += dppf<0xB1>(v); v += dppf<0x4E>(v); return v + dppf<0x141>(v); }
DI float dot2(unsigned a, unsigned b, float c) { return __builtin_amdgcn_fdot2_f32_bf16(__builtin_bit_cast(bf2_t, a), __builtin_bit_cast(bf2_t, b), c, false); }
DI float gelu(float x) {
  const float t = x * __builtin_fmaf(x * x, -0.10294324f, -2.3022082f);
  return x * __builtin_amdgcn_rcpf(1.0f + __builtin_amdgcn_exp2f(t));
}
DI void st128(rsrc_t rs, unsigned off, u32x4 v) { __builtin_amdgcn_raw_buffer_store_b128(v, rs, off, 0, 16); }
DI void st128s(rsrc_t rs, unsigned voff, unsigned soff, u32x4 v) { __builtin_amdgcn_raw_buffer_store_b128(v, rs, voff, soff, 16); }
DI bf16x8 ld128(rsrc_t rs, unsigned voff, unsigned soff) { return __builtin_bit_cast(bf16x8, __builtin_amdgcn_raw_buffer_load_b128(rs, voff, soff, 16)); }
template <int OFF> DI void ldw(bf16x8& r, rsrc_t rs, unsigned voff, unsigned soff) {
  asm volatile("buffer_load_dwordx4 %0, %1, %2, %3 offen offset:%4 sc1" : "+v"(r) : "v"(voff), "s"(rs), "s"(soff), "i"(OFF) : "memory");
}
template <int OFF> DI void ldn(bf16x8& r, rsrc_t rs, unsigned voff, unsigned soff) {
  asm volatile("buffer_load_dwordx4 %0, %1, %2, %3 offen offset:%4 sc1" : "=v"(r) : "v"(voff), "s"(rs), "s"(soff), "i"(OFF) : "memory");
}
template <int N> DI void got4(bf16x8 (&f)[4]) { asm volatile("s_waitcnt vmcnt(%4)" : "+v"(f[0]), "+v"(f[1]), "+v"(f[2]), "+v"(f[3]) : "i"(N) : "memory"); }
template <int OFF> DI void ldsw(bf16x8& r, unsigned a) { asm volatile("ds_read_b128 %0, %1 offset:%2" : "+v"(r) : "v"(a), "i"(OFF) : "memory"); }
template <int V> struct IC { static constexpr int v = V; };
template <int B, int E, class Fn> DI void sfor(Fn&& fn) { if constexpr (B < E) { fn(IC<B>{}); sfor<B + 1, E>(fn); } }
DI u32x4 pack8(const f32x16& a, int o) { u32x4 p; p[0] = pk2(a[o], a[o + 1]); p[1] = pk2(a[o + 2], a[o + 3]); p[2] = pk2(a[o + 4], a[o + 5]); p[3] = pk2(a[o + 6], a[o + 7]); return p; }

DI bf16x8 split_op(float v, bool first, bool on) {
  const unsigned hi = __float_as_uint(v) & 0xffff0000u, lo = pk2(v - __uint_as_float(hi), 0.f);
  u32x4 o = {0u, 0u, 0u, 0u};
  if (first) { o[0] = hi | (hi >> 16); o[1] = lo | (lo << 16); } else { o[0] = (hi >> 16) | (lo << 16); o[1] = o[0]; }
  if (!on) { o[0] = 0u; o[1] = 0u; }
  return __builtin_bit_cast(bf16x8, o);
}

__global__ void __launch_bounds__(512, 2) k1_qkv(const u16* __restrict__ x, const u16* __restrict__ wqkv, const u16* __restrict__ wo, const u16* __restrict__ w1,
                                                 const u16* __restrict__ w2, const u16* __restrict__ w3, const u16* __restrict__ w4, char* __restrict__ ws) {
  extern __shared__ __attribute__((aligned(16))) char smem[];
  const unsigned lds0 = (unsigned)(uintptr_t)smem;
  const int b = blockIdx.x, l = lane();
  const unsigned w = rfl(threadIdx.x >> 6);
  const unsigned xcd = b & 7, r = b >> 3;
  const unsigned rb = (xcd >> 1) * 8 + (r >> 2), cb = (xcd & 1) * 4 + (r & 3);
  constexpr unsigned SLOT = 32768, LC = 4 * SLOT, LT = LC + 20480;
  STDECL; STAMP(0); STAMPC(40);
  const rsrc_t rx = mkrsrc(x, S * D * 2), rw = mkrsrc(wqkv, D * 1536 * 2), rws = mkrsrc(ws, WS_END);
  const int h = l >> 5, l31 = l & 31, G = (l >> 4) & 1, q = (l >> 2) & 3, p4 = l & 3;
  const unsigned hw = w & 3;
  const unsigned vx0 = (l >> 3) * 1024 + (((l & 7) ^ ((l >> 3) & 6)) << 4), vx1 = (vx0 ^ 16) + 8192;
  const unsigned vsw = ((l & 7) ^ (G << 2)) << 4, vw0 = (l >> 3) * 3072 + vsw, vw1 = vw0 + 8 * 3072;
  auto issue = [&](int c) {
    const unsigned sx = (rb * 64 + hw * 16) * 1024 + c * 128, sw = (c * 64 + hw * 16) * 3072 + cb * 128, lw = lds0 + (c & 3) * SLOT + hw * 2048;
    dma16<0, false>(rx, vx0, sx, lw);
    dma16<0, false>(rw, vw0, sw, lw + 8192); dma16<0, false>(rw, vw1, sw, lw + 8192 + 1024);
    dma16<0, false>(rx, vx1, sx, lw + 1024);
    dma16<0, false>(rw, vw0, sw + 1024, lw + 16384); dma16<0, false>(rw, vw1, sw + 1024, lw + 16384 + 1024);
    dma16<0, false>(rw, vw0, sw + 2048, lw + 24576); dma16<0, false>(rw, vw1, sw + 2048, lw + 24576 + 1024);
  };
  if (w >= 4) issue(0);
  asm volatile("s_barrier" ::: "memory");
  if (w >= 4) {
    issue(1);
    STAMP(1);
    const unsigned bit = hw >> 1, hf = hw & 1, big3 = b < 128;
    const unsigned bp = big3 ? F * 2 : 1024, bso = big3 ? (b >> 2) * 16 * (F * 2) + (b & 3) * 7 * 128 : ((b - 128) >> 3) * 7 * 16384 + (b & 7) * 128, bst = big3 ? 128 : 16384;
    const unsigned bdo = big3 ? TW_3 + (((b & 3) * 14 + hf) * 32 + (b >> 2)) * 1024 : TW_4 + (((b & 7) * 2 + hf) * 112 + ((b - 128) >> 3) * 7) * 1024, bdt = big3 ? 65536 : 1024;
    const unsigned sso = ((b >> 3) * 16 + hf * 8) * 1024 + (b & 7) * 128, sdo = (((b & 7) * 2 + hf) * 32 + (b >> 3)) * 1024;
    const rsrc_t rbig = mkrsrc(big3 ? w3 : w4, D * F * 2);
    const unsigned vs = (l >> 3) * 1024 + vsw, vb = (l >> 3) * bp + vsw;
    const rsrc_t rp0 = mkrsrc(bit ? w1 : wo, D * D * 2), rp1 = mkrsrc(bit ? (big3 ? w3 : w4) : w2, bit ? D * F * 2 : D * D * 2);
    const unsigned vp1 = bit ? vb : vs, bsh = bso + hf * 8 * bp, sp1 = bit ? bsh : sso, dp1 = bit ? bdo : TW_2 + sdo;
    auto piece = [&](int i) {
      const unsigned lc = lds0 + LC + (2 * i + bit) * 2048 + hf * 1024;
      if (i == 0) dma16<0, false>(rp0, vs, sso, lc);
      else if (i == 1) dma16<0, false>(rp1, vp1, sp1, lc);
      else dma16<0, false>(rbig, vb, bsh + (2 * i - 3 + bit) * bst, lc);
    };
    float s0 = 0.f, s1 = 0.f, q0 = 0.f, q1 = 0.f;
    const unsigned xs = lds0 + hw * 2048 + l * 16;
#pragma unroll
    for (int c = 0; c < 8; ++c) {
      if (c == 0) waitv<8>(); else if (c == 1) waitv<17>(); else if (c < 6) waitv<18>(); else if (c == 6) waitv<9>(); else waitv<0>();
      BAR();
      STAMP(4 + c);
      const u32x4 a0 = __builtin_bit_cast(u32x4, lds128(xs + (c & 3) * SLOT)), a1 = __builtin_bit_cast(u32x4, lds128(xs + 1024 + (c & 3) * SLOT));
      if (c == 0) issue(2);
      if (c < 5) { issue(c + 3); piece(c); }
#pragma unroll
      for (int j = 0; j < 4; ++j) { s0 = dot2(a0[j], 0x3f803f80u, s0); q0 = dot2(a0[j], a0[j], q0); s1 = dot2(a1[j], 0x3f803f80u, s1); q1 = dot2(a1[j], a1[j], q1); }
    }
    const float m0_ = sum8(s0) * (1.f / D), m1_ = sum8(s1) * (1.f / D);
    const float r0 = __builtin_amdgcn_rsqf(fmaxf(sum8(q0) * (1.f / D) - m0_ * m0_, 0.f) + EPS), r1 = __builtin_amdgcn_rsqf(fmaxf(sum8(q1) * (1.f / D) - m1_ * m1_, 0.f) + EPS);
    if ((l & 7) == 0) {
      LAS float* t = (LAS float*)(uintptr_t)(lds0 + LT + (hw * 16 + (l >> 3)) * 4);
      t[0] = m0_; t[8] = m1_; t[64] = r0; t[72] = r1;
    }
    BAR();
    const unsigned ta = lds0 + LC + bit * 2048 + (4 * h + q) * 128 + (((4 * hf + 2 * G + (p4 >> 1)) ^ ((q >> 1) << 2)) << 4) + 8 * (p4 & 1);
#pragma unroll
    for (int i = 0; i < 5; ++i) {
      const unsigned dst = i == 0 ? TW_O + bit * 524288 + sdo : i == 1 ? dp1 : bdo + (2 * i - 3 + bit) * bdt;
      st128s(rws, l * 16, dst, __builtin_bit_cast(u32x4, trfrag(ta + i * 4096, ta + i * 4096 + 1024)));
    }
    STAMP(2);
    if (w == 4) { unsigned* fz = (unsigned*)(ws + WS_FLAG) + b * 161; fz[l] = 0u; fz[64 + l] = 0u; if (l < 33) fz[128 + l] = 0u; }
  } else {
    const unsigned tt = w & 1, hp = w >> 1;
    unsigned wm0, wm1;
    if (w < 3) {
      const unsigned vo = (l >> 1) * 3072 + (l & 1) * 64, so = (128 + (r >> 2) * 48) * 3072 + w * 1024 + cb * 128;
      asm volatile("buffer_load_dword %0, %2, %3, %4 offen\n\tbuffer_load_dword %1, %2, %3, %5 offen" : "=&v"(wm0), "=&v"(wm1) : "v"(vo), "s"(rw), "s"(so), "s"(so + 16 * 3072) : "memory");
    } else {
      const unsigned vo = (l >> 2) * 1024 + (l & 3) * 64, so = (rb * 64 + (r & 3) * 16) * 1024;
      asm volatile("buffer_load_dword %0, %2, %3, %4 offen offset:256\n\tbuffer_load_dword %1, %2, %3, %4 offen offset:512\n\tbuffer_load_dword %0, %2, %3, %4 offen offset:768"
                   : "=&v"(wm0), "=&v"(wm1) : "v"(vo), "s"(rx), "s"(so) : "memory");
    }
    STAMP(1);
    unsigned xa[2][4];
#pragma unroll
    for (int s = 0; s < 4; ++s) {
      xa[0][s] = lds0 + (tt * 4 + (l31 >> 3)) * 1024 + (l31 & 7) * 128 + (((2 * s + h) ^ ((l31 & 6) | ((l31 >> 3) & 1))) << 4); xa[1][s] = xa[0][s] + 2 * SLOT;
      TOUCH(xa[0][s]); TOUCH(xa[1][s]);
    }
    unsigned wa0 = lds0 + 8192 + (8 * h + q) * 128 + (((4 * hp + 2 * G + (p4 >> 1)) ^ ((q >> 1) << 2)) << 4) + 8 * (p4 & 1), wa1 = wa0 + 2 * SLOT;
    TOUCH(wa0); TOUCH(wa1);
    f32x16 aq = {}, ak = {}, av = {};
    float cq = 0.f, ck = 0.f, cv = 0.f, dq = 0.f, dk = 0.f, dv = 0.f;
    auto body = [&](auto TT) {
      bf16x8 f[2][4];
      auto wadr = [&](int s, int p) { return ((s & 8) ? wa1 : wa0) + ((s >> 2) & 1) * SLOT + p * 8192 + (s & 3) * 2048; };
      auto xadr = [&](int s) { return xa[(s >> 3) & 1][s & 3] + ((s >> 2) & 1) * SLOT; };
      auto cs2 = [&](const bf16x8& v, float& c, float& d) {
        const u32x4 u = __builtin_bit_cast(u32x4, v); c = dot2(u[2 * TT.v], 0x3f803f80u, c); d = dot2(u[2 * TT.v + 1], 0x3f803f80u, d); };
      BAR();
      STAMP(4);
      f[0][0] = lds128(xadr(0)); f[0][1] = trfrag(wadr(0, 0), wadr(0, 0) + 512); f[0][2] = trfrag(wadr(0, 1), wadr(0, 1) + 512); f[0][3] = trfrag(wadr(0, 2), wadr(0, 2) + 512);
#pragma unroll
      for (int s = 0; s < 32; ++s) {
        const bf16x8* o = f[s & 1];
        bf16x8* n = f[(s + 1) & 1];
        if ((s & 3) == 3 && s < 31) { BAR(); STAMP(5 + (s >> 2)); }
        SB();
        aq = MFMA32(o[1], o[0], aq); TOUCH(aq);
        if (s < 31) { n[0] = lds128(xadr(s + 1)); n[1] = trfrag(wadr(s + 1, 0), wadr(s + 1, 0) + 512); }
        cs2(o[1], cq, dq);
        SB();
        ak = MFMA32(o[2], o[0], ak); TOUCH(ak);
        if (s < 31) n[2] = trfrag(wadr(s + 1, 1), wadr(s + 1, 1) + 512);
        cs2(o[2], ck, dk);
        SB();
        av = MFMA32(o[0], o[3], av); TOUCH(av);
        if (s < 31) n[3] = trfrag(wadr(s + 1, 2), wadr(s + 1, 2) + 512);
        cs2(o[3], cv, dv);
        SB();
      }
    };
    if (tt) body(IC<1>{}); else body(IC<0>{});
    asm volatile("s_waitcnt vmcnt(0)" :: "v"(wm0), "v"(wm1) : "memory");
    STAMP(12);
    cq = swapadd(cq + dq); ck = swapadd(ck + dk); cv = swapadd(cv + dv);
    LAS float* ct = (LAS float*)(uintptr_t)(lds0 + LT + 512 + l31 * 4);
    if (h == 0) { ct[w * 96] = cq; ct[w * 96 + 32] = ck; ct[w * 96 + 64] = cv; }
    BAR();
    cq += ct[(w ^ 1) * 96]; ck += ct[(w ^ 1) * 96 + 32]; cv += ct[(w ^ 1) * 96 + 64];
    const LAS float* st = (const LAS float*)(uintptr_t)(lds0 + LT + tt * 128);
    const float mu = st[l31], rs = st[64 + l31];
    f32x4 rv[4];
#pragma unroll
    for (int j = 0; j < 4; ++j) rv[j] = *(const LAS f32x4*)(st + 64 + 8 * j + 4 * h);
    const bf16x8 mo = split_op(-mu, false, true);
    aq = MFMA32(split_op(cq, true, h == 0), mo, aq); ak = MFMA32(split_op(ck, true, h == 0), mo, ak); av = MFMA32(mo, split_op(cv, true, h == 0), av);
    const float qs = rs * 0.36067376f;
    float n0 = 0.f, n1 = 0.f;
#pragma unroll
    for (int i = 0; i < 16; ++i) { aq[i] *= qs; ak[i] *= rs; av[i] *= rv[i >> 2][i & 3]; }
    const unsigned head = cb * 4 + hp * 2, t32 = rb * 2 + tt;
    const unsigned vq = (h * 2048 + l31) * 16, sq = WS_Q + head * 65536 + t32 * 512;
    const unsigned vk = h * 512 + ((l31 & 19) | ((l31 & 4) << 1) | ((l31 & 8) >> 1)) * 16, sk = WS_KV + head * 131072 + t32 * 2048;
    const unsigned vv = G * 131072 + 1024 + h * 512 + (l & 15) * 16;
    st128s(rws, vk, sk, pack8(ak, 0)); st128s(rws, vk, sk + 131072, pack8(ak, 8));
    st128s(rws, vv, sk, pack8(av, 0)); st128s(rws, vv + 256, sk, pack8(av, 8));
    st128s(rws, vq, sq, pack8(aq, 0)); st128s(rws, vq, sq + 65536, pack8(aq, 8));
#pragma unroll
    for (int i = 0; i < 8; ++i) { n0 += ak[i] * ak[i]; n1 += ak[8 + i] * ak[8 + i]; }
    n0 = swap16max(max16(swapadd(n0))); n1 = swap16max(max16(swapadd(n1)));
    if (l == 0) { float* km = (float*)(ws + WS_KMAX) + head * 64 + t32; km[0] = n0; km[64] = n1; }
  }
  STAMP(3); STAMPC(41); STDUMP(0);
}

DI void poll_flags(const unsigned* p, bool need) {
#pragma unroll 1
  for (unsigned sp = 0; sp < (1u << 20); ++sp) {
    const unsigned v = need ? __hip_atomic_load(p, RLX_AGENT) : 1u;
    if (__all(v != 0u)) break;
    __builtin_amdgcn_s_sleep(1);
  }
  asm volatile("" ::: "memory");
}
DI void set_flag(unsigned* p, int l) { drain(); if (l == 0) __hip_atomic_store(p, 1u, RLX_AGENT); }
template <int ST> DI void lds_stats(unsigned a, float& mu, float& var) {
  float s = 0.f, q = 0.f;
#pragma unroll
  for (int i = 0; i < 4; ++i) { const f32x4 v = lds128f(a + (i & 1) * 512 + (i >> 1) * ST); s += v[0] + v[2]; q += v[1] + v[3]; }
  s = swapadd(s); q = swapadd(q);
  mu = s * (1.f / D); var = fmaxf(q * (1.f / D) - mu * mu, 0.f);
}
DI void put_stats(rsrc_t rs, unsigned vl, unsigned soff, const f32x16& v) {
  float s = 0.f, q = 0.f;
#pragma unroll
  for (int i = 0; i < 16; ++i) { s += v[i]; q += v[i] * v[i]; }
  s = swapadd(s); q = swapadd(q);
  if (vl < 512) __builtin_amdgcn_raw_buffer_store_b64((u32x2){__float_as_uint(s), __float_as_uint(q)}, rs, vl, soff, 16);
}
DI f32x16 unpack16(const u32x4& p0, const u32x4& p1) {
  f32x16 o;
#pragma unroll
  for (int j = 0; j < 4; ++j) { o[2 * j] = bflo(p0[j]); o[2 * j + 1] = bfhi(p0[j]); o[8 + 2 * j] = bflo(p1[j]); o[8 + 2 * j + 1] = bfhi(p1[j]); }
  return o;
}
template <bool IO> DI f32x16 gemm32(const bf16x8 (&wf)[32], unsigned ba) {
  f32x16 acc = {};
  bf16x8 f[2][8];
  if (IO) waitv<6>();
  BAR();
#pragma unroll
  for (int k = 0; k < 8; ++k) f[0][k] = lds128(ba + (8 * (k >> 1) + (k & 1)) * 1024);
#pragma unroll
  for (int j = 0; j < 4; ++j) {
    if (IO) { if (j == 0) waitv<4>(); else if (j == 1) waitv<2>(); else if (j == 2) waitv<0>(); }
    BAR();
#pragma unroll
    for (int k = 0; k < 8; ++k) {
      SB();
      acc = MFMA32(wf[8 * (k >> 1) + 2 * j + (k & 1)], f[j & 1][k], acc);
      TOUCH(acc);
      if (j < 3) f[(j + 1) & 1][k] = lds128(ba + (8 * (k >> 1) + 2 * j + 2 + (k & 1)) * 1024);
    }
    SB();
  }
  return acc;
}
DI f32x16 colsum(const bf16x8 (&wf)[32]) {
  u32x4 o = {0x3f803f80u, 0x3f803f80u, 0x3f803f80u, 0x3f803f80u};
  f32x16 acc = {};
  TOUCH(o);
  SB();
#pragma unroll
  for (int k = 0; k < 32; ++k) acc = MFMA32(wf[k], __builtin_bit_cast(bf16x8, o), acc);
  TOUCH(acc);
  SB();
  return acc;
}
DI void tile_dma(rsrc_t rs, unsigned voff, unsigned soff, unsigned lds) { dmaN<4>(rs, voff, soff, lds); dmaN<4>(rs, voff, soff + 4096, lds + 4096); }

#define EX(B, i) "v_exp_f32 v[" #B "+" #i "], v[" #B "+" #i "]\n\t"
#define TILE(C, P, KC, KP, LC, LP, HC, HP, U, X0, X1)                                                 \
  EX(C, 0) EX(C, 1)                                                                                   \
  "v_smfmac_f32_32x32x32_bf16 v[2:17], v[" #HP ":" #HP "+3], v[" #P "+8:" #P "+15], v78\n\t"          \
  EX(C, 2) EX(C, 3) X0 EX(C, 4) EX(C, 5)                                                              \
  "v_mfma_f32_32x32x16_bf16 v[" #P ":" #P "+15], v[" #KP ":" #KP "+3], v[50:53], 0\n\t"               \
  "ds_read_b128 v[" #KC ":" #KC "+3], v79 offset:" #U "*2048+4096\n\t"                                \
  "s_mov_b64 exec, %[msk]\n\t"                                                                        \
  "ds_read_b128 v[" #LP ":" #LP "+3], v79 offset:" #U "*2048+3072\n\t"                                \
  "ds_read_b128 v[" #HC ":" #HC "+3], v79 offset:" #U "*2048+1280\n\t"                                \
  "s_mov_b64 exec, -1\n\t"                                                                            \
  EX(C, 6) EX(C, 7) EX(C, 8) EX(C, 9)                                                                 \
  "v_smfmac_f32_32x32x32_bf16 v[2:17], v[" #LC ":" #LC "+3], v[" #C ":" #C "+7], v78\n\t"             \
  EX(C, 10) EX(C, 11) EX(C, 12) X1 EX(C, 13) EX(C, 14) EX(C, 15)                                      \
  "s_waitcnt lgkmcnt(0)\n\t"
#define TILE4(A0, A1, B0, B1, C0, C1, D0, D1)                                                         \
  TILE(18, 34, 54, 58, 62, 66, 70, 74, 0, A0, A1) TILE(34, 18, 58, 54, 66, 62, 74, 70, 1, B0, B1)     \
  TILE(18, 34, 54, 58, 62, 66, 70, 74, 2, C0, C1) TILE(34, 18, 58, 54, 66, 62, 74, 70, 3, D0, D1) "v_add_u32 v79, 0x2000, v79"
#define ATT_OPS "+{v[2:17]}"(oacc), "+{v[18:33]}"(s0), "+{v[34:49]}"(s1), "+{v[54:57]}"(k0), "+{v[58:61]}"(k1), "+{v[62:65]}"(l0), "+{v[66:69]}"(l1), \
                "+{v[70:73]}"(h0), "+{v[74:77]}"(h1), "+{v79}"(ad)
#define ATT_IN "{v[50:53]}"(qf), "{v78}"(idx), [msk] "s"(msk)
#define WARM "buffer_load_dword v80, %[vl], %[rs], %[sw] offen\n\t"
#define WL(B, i) "buffer_load_dwordx4 v[" #B "+4*" #i ":" #B "+4*" #i "+3], %[vl], %[rs], %[sa] offen offset:" #i "*1024 sc1\n\t"
#define DL(i) "buffer_load_dwordx4 %[vl], %[rs], %[sa] offen offset:" #i "*1024 sc1 lds\n\t"
#define ATT_LD [vl] "v"(vl), [rs] "s"(rws)
#define ATT_1() asm volatile(TILE4("", "", "", "", "", "", "", "") : ATT_OPS : ATT_IN : "memory")
#define ATT_W(B, E, n, g) asm volatile(TILE4(WARM, WL(B, 0), "", WL(B, 1), "", WL(B, 2), "", WL(B, 3)) \
    : ATT_OPS, "+{v80}"(dum), "={v[" #B ":" #E "]}"(wt[n]) : ATT_IN, ATT_LD, [sa] "s"(wso + n * 4096), [sw] "s"(wrm + g * 8192) : "memory")
#define ATT_D0(src, dst) asm volatile("s_mov_b32 m0, %[ld]\n\t" TILE4("", DL(0), "", DL(1), "", DL(2), "", DL(3)) : ATT_OPS : ATT_IN, ATT_LD, [sa] "s"(src), [ld] "s"(dst) : "memory")
#define ATT_D(src, dst, g) asm volatile("s_mov_b32 m0, %[ld]\n\t" TILE4(WARM, DL(0), "", DL(1), "", DL(2), "", DL(3)) \
    : ATT_OPS, "+{v80}"(dum) : ATT_IN, ATT_LD, [sa] "s"(src), [ld] "s"(dst), [sw] "s"(wrm + g * 8192) : "memory")
#define ATT_DH(src, dst, g) asm volatile("s_mov_b32 m0, %[ld]\n\t" TILE4(WARM, DL(0), "", DL(1), "", "", "", "") \
    : ATT_OPS, "+{v80}"(dum) : ATT_IN, ATT_LD, [sa] "s"(src), [ld] "s"(dst), [sw] "s"(wrm + g * 8192) : "memory")

__global__ void __launch_bounds__(512, 2) k2_main(const u16* __restrict__ x, u16* __restrict__ out, char* __restrict__ ws) {
  extern __shared__ __attribute__((aligned(16))) char smem[];
  const unsigned lds0 = (unsigned)(uintptr_t)smem;
  const int b = blockIdx.x;
  const unsigned w = rfl(threadIdx.x >> 6);
  const unsigned xcd = b & 7, r = b >> 3;
  unsigned* flags = (unsigned*)(ws + WS_FLAG);
  const rsrc_t rws = mkrsrc(ws, WS_END);
  const unsigned rq = xcd * 8 + (r >> 2), c4 = r & 3;
  const unsigned rb = xcd * 4 + (r >> 3), c8 = r & 7;
  const unsigned wi = w & 3;
  const unsigned r2a = lds0 + wi * 16384, r2b = lds0 + 65536 + wi * 8192, r2c = lds0 + L_Z + wi * 7168;
  bf16x8 wf[32];
  u32x16 wt[8];
  STDECL; STAMP(0);
  {
    const int l = lane(), h = l >> 5, l31 = l & 31;
    const unsigned vl = l * 16;
    const unsigned head = xcd * 4 + (r & 3), qb = r >> 2;
    const unsigned tok = qb * 256 + w * 32 + l31;
    bf16x8 qf; float km;
    asm volatile("buffer_load_dwordx4 %0, %1, %2, %3 offen" : "={v[50:53]}"(qf) : "v"((h * 2048 + tok) * 16), "s"(rws), "s"(WS_Q + head * 65536) : "memory");
    asm volatile("buffer_load_dword %0, %1, %2, %3 offen" : "=v"(km) : "v"(l * 4), "s"(rws), "s"(WS_KMAX + head * 256) : "memory");
    const unsigned kv = WS_KV + head * 131072;
    dma16<0, true>(rws, vl, kv + w * 1024, lds0 + w * 1024); dma16<0, true>(rws, vl, kv + 8192 + w * 1024, lds0 + 8192 + w * 1024);
    dmaN<4>(rws, vl, kv + 16384 + w * 4096, lds0 + 16384 + w * 4096);
    asm volatile("s_waitcnt vmcnt(6)" : "+{v[50:53]}"(qf), "+v"(km) :: "memory");
    km = swapmax(swap16max(max16(km)));
    float qn = 0.f;
    { const u32x4 u = __builtin_bit_cast(u32x4, qf);
#pragma unroll
      for (int j = 0; j < 4; ++j) qn = dot2(u[j], u[j], qn); }
    qn = swapadd(qn);
    { const unsigned slowf = __all(qn * km * 1.05f <= 48.f * 48.f) ? 0u : 1u;
      if (l == 0) *(LAS unsigned*)(uintptr_t)(lds0 + L_ST + 1024 + w * 4) = slowf; }
    const unsigned cst = l31 == 16 ? 0x3f803f80u : 0u;
    const bf16x8 vc = __builtin_bit_cast(bf16x8, (u32x4){cst, cst, cst, cst});
    f32x16 oacc = {};
    int idx = 0xDDDD;
    unsigned ad = lds0 + h * 512 + l31 * 16;
    const unsigned wso = (w < 4 ? TW_O : TW_1) + (c4 * 4 + wi) * 32768, w2o = TW_2 + (c4 * 4 + wi) * 32768, hw = w >> 2;
    const unsigned wrm = TW_3 + r * 114688 + w * 1024;
    STAMP(1);
    waitv<4>();
    BAR();
    STAMP(2); STAMPC(40);
    const u32x4 sf0 = *(const LAS u32x4*)(uintptr_t)(lds0 + L_ST + 1024), sf1 = *(const LAS u32x4*)(uintptr_t)(lds0 + L_ST + 1040);
    if (rfl(sf0[0] | sf0[1] | sf0[2] | sf0[3] | sf1[0] | sf1[1] | sf1[2] | sf1[3]) == 0u) {
      f32x16 s0, s1 = {};
      bf16x8 k0, k1, l0 = vc, l1 = vc, h0 = vc, h1 = vc;
      const unsigned long long msk = 0x0000ffff0000ffffull;
      asm volatile("ds_read_b128 v[54:57], v79\n\tds_read_b128 v[58:61], v79 offset:2048\n\ts_mov_b64 exec, %[msk]\n\tds_read_b128 v[62:65], v79 offset:1024\n\ts_mov_b64 exec, -1\n\t"
                   "s_waitcnt lgkmcnt(0)\n\tv_mfma_f32_32x32x16_bf16 v[18:33], v[54:57], v[50:53], 0\n\ts_nop 7\n\ts_nop 4"
                   : "={v[18:33]}"(s0), "={v[54:57]}"(k0), "={v[58:61]}"(k1), "+{v[62:65]}"(l0) : "{v79}"(ad), "{v[50:53]}"(qf), [msk] "s"(msk) : "memory");
      unsigned dum = 0;
      STAMPC(44);
      ATT_D0(kv + 49152 + w * 4096, lds0 + 49152 + w * 4096);
      STAMPC(45);
      waitv<4>(); BAR();
      STAMPC(46);
      ATT_D(kv + 81920 + w * 4096, lds0 + 81920 + w * 4096, 0);
      ATT_DH(kv + 114688 + w * 2048, lds0 + 114688 + w * 2048, 1);
      STAMPC(47);
      ATT_W(128, 143, 0, 2);
      ATT_W(144, 159, 1, 3);
      STAMPC(48);
      waitv<10>(); BAR();
      STAMPC(49);
      ATT_W(160, 175, 2, 4);
      ATT_W(176, 191, 3, 5);
      STAMPC(50);
      ATT_W(192, 207, 4, 6);
      ATT_W(208, 223, 5, 7);
      STAMPC(51);
      ATT_W(224, 239, 6, 8);
      ATT_W(240, 255, 7, 9);
      STAMPC(52);
      ATT_D(w2o + 24576 + hw * 3072, r2c + hw * 3072, 10);
      STAMPC(53);
      BAR();
      STAMPC(54);
      ATT_D(w2o + hw * 8192, r2a + hw * 8192, 11);
      ATT_D(w2o + hw * 8192 + 4096, r2a + hw * 8192 + 4096, 12);
      STAMPC(55);
      ATT_D(w2o + 16384 + hw * 4096, r2b + hw * 4096, 13);
      ATT_1();
      STAMPC(56);
      asm volatile("s_waitcnt vmcnt(0)" : "+{v[128:143]}"(wt[0]), "+{v[144:159]}"(wt[1]), "+{v[160:175]}"(wt[2]), "+{v[176:191]}"(wt[3]), "+{v[192:207]}"(wt[4]),
                   "+{v[208:223]}"(wt[5]), "+{v[224:239]}"(wt[6]), "+{v[240:255]}"(wt[7]), "+{v80}"(dum) :: "memory");
      asm volatile("s_nop 1\n\tv_smfmac_f32_32x32x32_bf16 v[2:17], v[74:77], v[42:49], v78\n\ts_nop 7\n\ts_nop 4" : "+{v[2:17]}"(oacc) : "{v[34:49]}"(s1), "{v[74:77]}"(h1), "{v78}"(idx));
    } else {
      tile_dma(rws, vl, kv + 49152 + w * 10240, lds0 + 49152 + w * 10240); dmaN<2>(rws, vl, kv + 49152 + w * 10240 + 8192, lds0 + 49152 + w * 10240 + 8192);
      waitv<0>(); BAR();
      float mx = -3.0e38f;
      const f32x16 zero = {};
#pragma unroll 1
      for (int t = 0; t < 64; ++t) {
        const bf16x8 kf = lds128(ad); bf16x8 v0 = vc, v1 = vc;
        if (l31 < 16) { v0 = lds128(ad + 1024); v1 = lds128(ad + 1280); }
        f32x16 s = MFMA32(kf, qf, zero);
        float tm = s[0];
#pragma unroll
        for (int i = 1; i < 16; ++i) tm = fmaxf(tm, s[i]);
        const float mn = fmaxf(mx, swapmax(tm)), al = __builtin_amdgcn_exp2f(mx - mn);
        mx = mn;
#pragma unroll
        for (int i = 0; i < 16; ++i) { s[i] = __builtin_amdgcn_exp2f(s[i] - mn); oacc[i] *= al; }
        oacc = __builtin_amdgcn_smfmac_f32_32x32x32_bf16(__builtin_bit_cast(bf8_t, v0), __builtin_bit_cast(bf16_t, __builtin_shufflevector(s, s, 0, 1, 2, 3, 4, 5, 6, 7)), oacc, idx, 0, 0);
        oacc = __builtin_amdgcn_smfmac_f32_32x32x32_bf16(__builtin_bit_cast(bf8_t, v1), __builtin_bit_cast(bf16_t, __builtin_shufflevector(s, s, 8, 9, 10, 11, 12, 13, 14, 15)), oacc, idx, 0, 0);
        ad += 2048;
      }
      BAR();
#pragma unroll
      for (int k = 0; k < 32; ++k) { const u32x4 t = __builtin_amdgcn_raw_buffer_load_b128(rws, vl + (k & 3) * 1024, wso + (k >> 2) * 4096, 16);
        wt[k >> 2][(k & 3) * 4] = t[0]; wt[k >> 2][(k & 3) * 4 + 1] = t[1]; wt[k >> 2][(k & 3) * 4 + 2] = t[2]; wt[k >> 2][(k & 3) * 4 + 3] = t[3]; }
      tile_dma(rws, vl, w2o + hw * 8192, r2a + hw * 8192); dmaN<4>(rws, vl, w2o + 16384 + hw * 4096, r2b + hw * 4096); dmaN<4>(rws, vl, w2o + 24576 + hw * 3072, r2c + hw * 3072);
    }
    STAMP(3); STAMPC(41);
    const float inv = __builtin_amdgcn_rcpf(swapadd(h == 0 ? oacc[8] : 0.f));
#pragma unroll
    for (int i = 0; i < 8; ++i) oacc[i] *= inv;
    st128(rws, WS_O + (qb * 8 + w) * 32768 + (head * 2 + h) * 512 + l31 * 16, pack8(oacc, 0));
    set_flag(flags + FLAG_O + ((qb * 8 + w) * 4 + (head >> 3)) * 32 + (head & 7), l);
#pragma unroll
    for (int k = 0; k < 32; ++k) wf[k] = __builtin_bit_cast(bf16x8, (u32x4){wt[k >> 2][(k & 3) * 4], wt[k >> 2][(k & 3) * 4 + 1], wt[k >> 2][(k & 3) * 4 + 2], wt[k >> 2][(k & 3) * 4 + 3]});
    STAMP(4);
    BAR();
  }
  f32x16 cs;
  unsigned wsel = w;
  asm volatile("" : "+s"(wsel));
  const unsigned wv = w < 4 ? w : w - 1;
  {
  const int l = lane(), h = l >> 5, l31 = l & 31;
  const unsigned vl = l * 16;
  unsigned ba = lds0 + L_P + vl;
  TOUCH(ba);
  const unsigned tileo = rq * 32768 + (c4 * 16 + wi * 4) * 512;
  const unsigned stato = rq * 4096 + c4 * 1024 + (wi >> 1) * 512 + (wi & 1) * 8;
  const unsigned pq = lds0 + L_P + wi * 8192;
  auto unstage = [&](unsigned last) {
    const unsigned ua = r2a + vl, ub_ = r2b + vl, uc = r2c + vl;
    ldw<0>(wf[31], rws, vl, last);
    sfor<0, 16>([&](auto K) { ldsw<K.v * 1024>(wf[K.v], ua); });
    sfor<0, 8>([&](auto K) { ldsw<K.v * 1024>(wf[16 + K.v], ub_); });
    sfor<0, 7>([&](auto K) { ldsw<K.v * 1024>(wf[24 + K.v], uc); });
  };
  if (w < 4) {
    u32x2 xr[4];
#pragma unroll
    for (int g = 0; g < 4; ++g) xr[g] = *(const u32x2*)(x + (rq * 32 + l31) * D + c4 * 128 + wi * 32 + 8 * g + 4 * h);
    __builtin_amdgcn_s_setprio(1);
    poll_flags(flags + FLAG_O + (rq * 4 + wi) * 32 + (l & 7), true);
    STAMP(5);
    tile_dma(rws, vl, WS_O + rq * 32768 + wi * 8192, pq);
    STAMP(6);
    f32x16 acc = gemm32<true>(wf, ba);
    STAMP(7);
#pragma unroll
    for (int g = 0; g < 4; ++g) { acc[4 * g] += bflo(xr[g][0]); acc[4 * g + 1] += bfhi(xr[g][0]); acc[4 * g + 2] += bflo(xr[g][1]); acc[4 * g + 3] += bfhi(xr[g][1]); }
    const u32x4 p0 = pack8(acc, 0), p1 = pack8(acc, 8);
    st128s(rws, vl, WS_H + tileo, p0); st128s(rws, vl + 1024, WS_H + tileo, p1);
    f32x16 hres = unpack16(p0, p1);
    put_stats(rws, vl, WS_STH + stato, hres);
    STAMP(8);
    unstage(TW_2 + (c4 * 4 + wi) * 32768 + 31 * 1024);
    set_flag(flags + FLAG_H + (rq * 4 + c4) * 32 + wi, l);
    STAMP(9);
    const unsigned so = TW_3 + (c8 * 7 + (w < 3 ? w : 6)) * 32768;
    asm volatile("s_waitcnt lgkmcnt(0)" ::: "memory");
    tile_dma(rws, vl, so, r2a);
    poll_flags(flags + FLAG_H + (rq * 4 + wi) * 32 + (l & 3), true);
    STAMP(10);
    dmaN<1>(rws, vl, WS_STH + rq * 4096 + wi * 1024, lds0 + L_ST + wi * 1024);
    tile_dma(rws, vl, WS_H + rq * 32768 + wi * 8192, pq);
    __builtin_amdgcn_s_setprio(0);
    waitv<6>(); BAR(); waitv<4>(); BAR(); waitv<2>(); BAR(); waitv<0>(); BAR();
    sfor<8, 31>([&](auto K) {
      constexpr int k = K.v;
      dma16<0, true>(rws, vl, so + k * 1024, k < 16 ? r2a + k * 1024 : k < 24 ? r2b + (k - 16) * 1024 : r2c + (k - 24) * 1024);
      if constexpr (k == 10) BAR();
      waitv<2>();
    });
    STAMP(11);
    float mu, var; { const int l2 = lane(); lds_stats<1024>(lds0 + L_ST + (l2 >> 5) * 2048 + (l2 & 31) * 16, mu, var); }
    const float rs = __builtin_amdgcn_rsqf(var + EPS);
#pragma unroll
    for (int i = 0; i < 16; ++i) hres[i] = (hres[i] - mu) * rs;
    const int dl = (wi & 1) ? (int)(wi >> 1) + 1 : -(int)(wi >> 1) - 1;
    const int rn = (l & 4) ? (int)rq + ((wi & 1) ? 1 : -1) : (int)rq;
    __builtin_amdgcn_s_setprio(1);
    poll_flags(flags + FLAG_G + (rn * 4 + wi) * 32 + (l & 3), rn >= 0 && rn < 64);
    drain();
    STAMP(15);
    { const int l2 = lane(), src = (int)rq * 32 + (l2 & 31) + dl;
      tile_dma(mkrsrc(ws + WS_G, 2u << 20), (src >= 0 && src < S) ? (unsigned)((src >> 5) * 32768 + (src & 31) * 16 + (l2 >> 5) * 512) : 0x7ff00000u, wi * 8192, pq); }
    acc = gemm32<true>(wf, ba);
    STAMP(17);
#pragma unroll
    for (int i = 0; i < 16; ++i) acc[i] += hres[i];
    const u32x4 p2 = pack8(acc, 0), p3 = pack8(acc, 8);
    st128s(rws, vl, WS_X1 + tileo, p2); st128s(rws, vl + 1024, WS_X1 + tileo, p3);
    put_stats(rws, vl, WS_STX + stato, unpack16(p2, p3));
    STAMP(18);
    unstage(TW_3 + (c8 * 7 + wi) * 32768 + 31 * 1024);
    set_flag(flags + FLAG_X + (rq * 4 + c4) * 32 + wi, l);
    STAMP(19);
    poll_flags(flags + FLAG_X + ((rb * 2 + ((l >> 2) & 1)) * 4 + wi) * 32 + (l & 3), true);
    asm volatile("s_waitcnt lgkmcnt(0)" ::: "memory");
    STAMP(21);
#pragma unroll
    for (int j = 0; j < 4; ++j) {
      dmaN<2>(rws, vl, WS_X1 + rb * 65536 + wi * 8192 + j * 2048, r2a + j * 2048);
      dmaN<2>(rws, vl, WS_X1 + rb * 65536 + 32768 + wi * 8192 + j * 2048, r2a + 8192 + j * 2048);
    }
    dmaN<1>(rws, vl, WS_STX + rb * 8192 + wi * 1024, r2b); dmaN<1>(rws, vl, WS_STX + rb * 8192 + 4096 + wi * 1024, r2b + 1024);
    cs = colsum(wf);
  }
  if (wsel >= 4) {
    cs = colsum(wf);
    BAR(); BAR(); BAR(); BAR(); BAR();
    __builtin_amdgcn_s_setprio(1);
    f32x16 acc = gemm32<false>(wf, ba);
    STAMP(12);
    const unsigned so = TW_3 + (c8 * 7 + wv) * 32768;
    if (w != 7) sfor<0, 8>([&](auto K) { ldw<(K.v & 3) * 1024>(wf[K.v], rws, vl, so + (K.v >> 2) * 4096); });
    float mu, var; { const int l2 = lane(); lds_stats<1024>(lds0 + L_ST + (l2 >> 5) * 2048 + (l2 & 31) * 16, mu, var); }
    const float rs = __builtin_amdgcn_rsqf(var + EPS), cz = rs * __builtin_amdgcn_rsqf(var * rs * rs + EPS);
#pragma unroll
    for (int i = 0; i < 16; ++i) acc[i] = gelu((acc[i] - mu * cs[i]) * cz);
    st128s(rws, vl, WS_G + tileo, pack8(acc, 0)); st128s(rws, vl + 1024, WS_G + tileo, pack8(acc, 8));
    STAMP(13);
    set_flag(flags + FLAG_G + (rq * 4 + c4) * 32 + wi, l);
    __builtin_amdgcn_s_setprio(0);
    STAMP(14);
    if (w != 7) sfor<8, 18>([&](auto K) { ldw<(K.v & 3) * 1024>(wf[K.v], rws, vl, so + (K.v >> 2) * 4096); waitv<3>(); });
    STAMP(16);
    BAR();
    if (wsel == 7) unstage(so + 31 * 1024);
    BAR(); BAR(); BAR();
    if (w != 7) sfor<18, 21>([&](auto K) { ldw<(K.v & 3) * 1024>(wf[K.v], rws, vl, so + (K.v >> 2) * 4096); });
    BAR();
    if (w != 7) sfor<21, 32>([&](auto K) { ldw<(K.v & 3) * 1024>(wf[K.v], rws, vl, so + (K.v >> 2) * 4096); waitv<3>(); });
    waitv<0>();
    cs = colsum(wf);
    STAMP(20);
  }
  }
#define KS5(t) (8 * (((t) >> 1) & 3) + 2 * ((t) >> 3) + ((t) & 1))
#define XA5(t) (bb + (KS5(t) >> 3) * 16384 + (KS5(t) & 7) * 1024)
#define W4LD(k) ldw<(((k) % 14) & 3) * 1024>(wf[KS5(k)], rws, vl, so4 + ((k) / 14) * 114688 + (((k) % 14) >> 2) * 4096)
  u32x4 rh, rx1;
  const bool early = w < 4 || w == 7;
  {
    int l = lane();
    const int h = l >> 5, l31 = l & 31;
    const unsigned vl = l * 16;
    const unsigned so4 = TW_4 + (c8 * 2 * 112 + w * 14) * 1024;
    const unsigned bb = lds0 + vl;
    auto body = [&](auto EARLY) {
      constexpr bool E = EARLY.v;
      f32x16 a0 = {}, a1 = {};
      bf16x8 f[3][2];
      const unsigned uo = (h * 64 + l31) * 16, us = WS_U + rb * (224 * 1024) + (c8 * 28 + wv * 4) * 1024;
      if (!E) { BAR(); BAR(); BAR(); }
      f[0][0] = lds128(XA5(0)); f[0][1] = lds128(XA5(0) + 8192); f[1][0] = lds128(XA5(1)); f[1][1] = lds128(XA5(1) + 8192);
      sfor<0, 32>([&](auto T) {
        constexpr int t = T.v, nl = t < 5 ? 0 : (t - 4) - (t - 4) / 4;
        if constexpr (E && (t == 6 || t == 14 || t == 22)) { waitv<(t == 6 ? 10 : t == 14 ? 6 : 0) + nl>(); BAR(); }
        SB();
        a0 = MFMA32(wf[KS5(t)], f[t % 3][0], a0);
        TOUCH(a0);
        if constexpr (t < 30) f[(t + 2) % 3][0] = lds128(XA5(t + 2));
        SB();
        a1 = MFMA32(wf[KS5(t)], f[t % 3][1], a1);
        TOUCH(a1);
        if constexpr (t < 30) f[(t + 2) % 3][1] = lds128(XA5(t + 2) + 8192);
        if constexpr (t >= 4 && (t & 3) != 3) W4LD(nl);
        SB();
      });
      STAMP(22);
      float mu0, mu1, v0, v1;
      lds_stats<8192>(lds0 + 65536 + h * 16384 + l31 * 16, mu0, v0); lds_stats<8192>(lds0 + 65536 + 1024 + h * 16384 + l31 * 16, mu1, v1);
      const float rs0 = __builtin_amdgcn_rsqf(v0 + EPS), rs1 = __builtin_amdgcn_rsqf(v1 + EPS);
      __builtin_amdgcn_s_setprio(0);
      sfor<0, 7>([&](auto K) { W4LD(21 + K.v); });
#pragma unroll
      for (int i = 0; i < 8; ++i) a0[i] = gelu((a0[i] - mu0 * cs[i]) * rs0);
      st128s(rws, uo, us, pack8(a0, 0));
#pragma unroll
      for (int i = 0; i < 8; ++i) a1[i] = gelu((a1[i] - mu1 * cs[i]) * rs1);
      st128s(rws, uo + 512, us, pack8(a1, 0));
#pragma unroll
      for (int i = 8; i < 16; ++i) a0[i] = gelu((a0[i] - mu0 * cs[i]) * rs0);
      st128s(rws, uo + 2048, us, pack8(a0, 8));
#pragma unroll
      for (int i = 8; i < 16; ++i) a1[i] = gelu((a1[i] - mu1 * cs[i]) * rs1);
      st128s(rws, uo + 2048 + 512, us, pack8(a1, 8));
      STAMP(23);
    };
    waitv<14>(); BAR();
    STAMP(31);
    unsigned ws2 = w;
    asm volatile("" : "+s"(ws2));
    if (w < 3 || w == 7) { __builtin_amdgcn_s_setprio(2); body(IC<1>{}); }
    if (ws2 - 4 < 3) { __builtin_amdgcn_s_setprio(1); body(IC<0>{}); }
    if (wsel == 3) {
      waitv<10>(); BAR(); waitv<6>(); BAR(); waitv<0>(); BAR();
      sfor<0, 28>([&](auto K) { W4LD(K.v); waitv<1>(); });
    }
    if (!early) BAR();
    l = lane();
    set_flag(flags + FLAG_U + (rb * 8 + c8) * 32 + (w == 3 ? 7 : wv), l);
    { const unsigned ro = (rb * 2 + (w >> 2)) * 32768 + (c8 * 8 + (w & 1) * 4 + ((w >> 1) & 1) * 2) * 512;
      rh = __builtin_amdgcn_raw_buffer_load_b128(rws, l * 16, WS_H + ro, 16); rx1 = __builtin_amdgcn_raw_buffer_load_b128(rws, l * 16, WS_X1 + ro, 16); }
    STAMP(24);
  }
  {
    const int l = lane(), h = l >> 5, l31 = l & 31;
    const unsigned vl = l * 16, vu = (h * 64 + l31) * 16, ub = WS_U + rb * (224 * 1024) + w * 28672;
    f32x16 ac[4] = {};
    bf16x8 f[3][4];
    auto ldu = [&](auto R, auto P) { sfor<0, 4>([&](auto J) { ldn<(J.v >> 1) * 2048 + (J.v & 1) * 512>(f[R.v][J.v], rws, vu, ub + P.v * 4096); }); };
    auto mm = [&](auto R, auto P) {
      sfor<0, 8>([&](auto J) { constexpr int ks = J.v >> 2, tt = (J.v >> 1) & 1, nt = J.v & 1; ac[tt * 2 + nt] = MFMA32(wf[KS5(nt * 14 + P.v * 2 + ks)], f[R.v][ks * 2 + tt], ac[tt * 2 + nt]); });
      TOUCH(ac[0]); TOUCH(ac[1]); TOUCH(ac[2]); TOUCH(ac[3]);
      SB();
    };
    const unsigned* fu = flags + FLAG_U + (rb * 8 + w) * 32;
    poll_flags(fu + ((l & 3) == 3 ? 6 : (l & 3)), true);
    drain();
    STAMP(26);
    ldu(IC<0>{}, IC<0>{}); ldu(IC<1>{}, IC<1>{}); ldu(IC<2>{}, IC<2>{});
    got4<8>(f[0]);
    STAMP(35);
    mm(IC<0>{}, IC<0>{});
    ldu(IC<0>{}, IC<6>{});
    got4<8>(f[1]); mm(IC<1>{}, IC<1>{});
    got4<4>(f[2]); mm(IC<2>{}, IC<2>{});
    got4<0>(f[0]); mm(IC<0>{}, IC<6>{});
    STAMP(36);
    if (early) BAR();
    poll_flags(fu + 3 + ((l & 3) == 3 ? 0 : (l & 3)), true);
    STAMP(37);
    ldu(IC<0>{}, IC<3>{}); ldu(IC<1>{}, IC<4>{}); ldu(IC<2>{}, IC<5>{});
    const unsigned pw = lds0 + w * 16384 + vl, me = (w >> 2) * 4 + (w & 1) * 2 + ((w >> 1) & 1);
    f32x4 s0 = {bflo(rh[0]) + bflo(rx1[0]), bfhi(rh[0]) + bfhi(rx1[0]), bflo(rh[1]) + bflo(rx1[1]), bfhi(rh[1]) + bfhi(rx1[1])};
    f32x4 s1 = {bflo(rh[2]) + bflo(rx1[2]), bfhi(rh[2]) + bfhi(rx1[2]), bflo(rh[3]) + bflo(rx1[3]), bfhi(rh[3]) + bfhi(rx1[3])};
    got4<8>(f[0]); got4<4>(f[1]); got4<0>(f[2]);
    STAMP(25);
    sfor<0, 4>([&](auto Q) {
      constexpr int tt = Q.v >> 1, nt = Q.v & 1;
      sfor<0, 6>([&](auto J) { constexpr int rr = J.v >> 1, ks = J.v & 1; ac[Q.v] = MFMA32(wf[KS5(nt * 14 + (3 + rr) * 2 + ks)], f[rr][ks * 2 + tt], ac[Q.v]); });
      TOUCH(ac[Q.v]);
      SB();
      if constexpr (Q.v > 0) {
        const f32x16& v = ac[Q.v - 1];
#pragma unroll
        for (int j = 0; j < 4; ++j) *(LAS f32x4*)(uintptr_t)(pw + (Q.v - 1) * 4096 + j * 1024) = (f32x4){v[4 * j], v[4 * j + 1], v[4 * j + 2], v[4 * j + 3]};
        SB();
      }
    });
    STAMP(27);
#pragma unroll
    for (int j = 0; j < 4; ++j) *(LAS f32x4*)(uintptr_t)(pw + 3 * 4096 + j * 1024) = (f32x4){ac[3][4 * j], ac[3][4 * j + 1], ac[3][4 * j + 2], ac[3][4 * j + 3]};
    BAR();
    { const unsigned a = lds0 + me * 2048 + vl;
#pragma unroll
      for (int k = 0; k < 8; ++k) { s0 += lds128f(a + (k & 3) * 16384 + (k >> 2) * 65536); s1 += lds128f(a + 1024 + (k & 3) * 16384 + (k >> 2) * 65536); } }
    const auto e0 = __builtin_amdgcn_permlane32_swap(pk2(s0[0], s0[1]), pk2(s1[0], s1[1]), false, false);
    const auto e1 = __builtin_amdgcn_permlane32_swap(pk2(s0[2], s0[3]), pk2(s1[2], s1[3]), false, false);
    *(u32x4*)(out + (rb * 64 + (w >> 2) * 32 + l31) * D + c8 * 64 + (w & 1) * 32 + ((w >> 1) & 1) * 16 + h * 8) = (u32x4){e0[0], e1[0], e0[1], e1[1]};
  }
  STAMPC(42); STAMP(28);
  { const int l = lane(); (void)l; STDUMP(1); }
}

__global__ void __launch_bounds__(1024) k3_probe(char* ws) {
  __shared__ unsigned smn[16], smx[16];
  const unsigned* st = (const unsigned*)(ws + (64u << 20));
  unsigned mn = 0xffffffffu, mx = 0u;
  const unsigned base = st[0 * 64 + 0] - 100000u;
  for (int i = threadIdx.x; i < 2048; i += 1024) {
    unsigned a = st[(PKA * 2048 + i) * 64 + PIA], b = st[(PKB * 2048 + i) * 64 + PIB];
    if (a) { a -= base; if (PAMAX) { a = ~a; } mn = min(mn, a); }
    if (b) { b -= base; mx = max(mx, b); }
  }
  for (int o = 32; o; o >>= 1) { mn = min(mn, (unsigned)__shfl_xor((int)mn, o)); mx = max(mx, (unsigned)__shfl_xor((int)mx, o)); }
  if ((threadIdx.x & 63) == 0) { smn[threadIdx.x >> 6] = mn; smx[threadIdx.x >> 6] = mx; }
  __syncthreads();
  for (int i = 0; i < 16; ++i) { mn = min(mn, smn[i]); mx = max(mx, smx[i]); }
  if (PAMAX) mn = ~mn;
  unsigned d = (mx - mn) * PM;
  if (d > 40000u) d = 40000u;
  const unsigned t0 = (unsigned)__builtin_amdgcn_s_memrealtime();
  while ((unsigned)__builtin_amdgcn_s_memrealtime() - t0 < d) __builtin_amdgcn_s_sleep(1);
}

extern "C" void kernel_launch(void* const* d_in, const int* in_sizes, int n_in, void* d_out, int out_size, void* d_ws, size_t ws_size, hipStream_t stream) {
  const u16* x = (const u16*)d_in[0]; const u16* wqkv = (const u16*)d_in[1]; const u16* wo = (const u16*)d_in[2];
  const u16* w1 = (const u16*)d_in[4]; const u16* w2 = (const u16*)d_in[5]; const u16* w3 = (const u16*)d_in[6]; const u16* w4 = (const u16*)d_in[7];
  (void)hipFuncSetAttribute((const void*)k1_qkv, hipFuncAttributeMaxDynamicSharedMemorySize, LDS_BYTES);
  (void)hipFuncSetAttribute((const void*)k2_main, hipFuncAttributeMaxDynamicSharedMemorySize, LDS_BYTES);
  k1_qkv<<<256, 512, LDS_BYTES, stream>>>(x, wqkv, wo, w1, w2, w3, w4, (char*)d_ws);
  k2_main<<<256, 512, LDS_BYTES, stream>>>(x, (u16*)d_out, (char*)d_ws);
  k3_probe<<<1, 1024, 0, stream>>>((char*)d_ws);
}
```

```cpp
#include <hip/hip_runtime.h>
#include <stdint.h>

typedef unsigned short u16;
typedef __attribute__((ext_vector_type(8))) short bf16x8;
typedef __attribute__((ext_vector_type(4))) short s16x4;
typedef __attribute__((ext_vector_type(8))) __bf16 bf8_t;
typedef __attribute__((ext_vector_type(16))) __bf16 bf16_t;
typedef __attribute__((ext_vector_type(2))) __bf16 bf2_t;
typedef __attribute__((ext_vector_type(16))) float f32x16;
typedef __attribute__((ext_vector_type(8))) float f32x8;
typedef __attribute__((ext_vector_type(4))) float f32x4;
typedef __attribute__((ext_vector_type(2))) float f32x2;
typedef __attribute__((ext_vector_type(16))) unsigned u32x16;
typedef __attribute__((ext_vector_type(4))) unsigned u32x4;
typedef __attribute__((ext_vector_type(2))) unsigned u32x2;
typedef __amdgpu_buffer_rsrc_t rsrc_t;
#define LAS __attribute__((address_space(3)))
#define DI __device__ __forceinline__
#define RLX_AGENT __ATOMIC_RELAXED, __HIP_MEMORY_SCOPE_AGENT

constexpr int S = 2048, D = 512, F = 1792;
constexpr float EPS = 1e-5f;
constexpr unsigned WS_FLAG = 0;
constexpr unsigned WS_KMAX = 196608;
constexpr unsigned WS_STH  = 262144;
constexpr unsigned WS_STX  = WS_STH + 262144;
constexpr unsigned WS_Q    = 1u << 20;
constexpr unsigned WS_KV   = WS_Q + (2u << 20);
constexpr unsigned WS_O    = WS_KV + (4u << 20);
constexpr unsigned WS_H    = WS_O + (2u << 20);
constexpr unsigned WS_G    = WS_H + (2u << 20);
constexpr unsigned WS_X1   = WS_G + (2u << 20);
constexpr unsigned WS_U    = WS_X1 + (2u << 20);
constexpr unsigned WS_TW   = WS_U + 32u * 224 * 1024;
constexpr unsigned TW_O = WS_TW, TW_1 = TW_O + 524288, TW_2 = TW_1 + 524288, TW_3 = TW_2 + 524288, TW_4 = TW_3 + 1835008;
constexpr unsigned WS_END  = TW_4 + 1835008;
constexpr int FLAG_O = 0, FLAG_H = 8192, FLAG_G = 16384, FLAG_X = 24576, FLAG_U = 32768, FLAG_W = 40960;

constexpr int LDS_BYTES = 163840;
constexpr unsigned L_P = 96 * 1024, L_Z = 128 * 1024, L_ST = 156 * 1024;

#define STAMP(i)
#define STAMPC(i)
#define STDECL
#define STDUMP(k)

DI rsrc_t mkrsrc(const void* p, unsigned bytes) { return __builtin_amdgcn_make_buffer_rsrc((void*)p, 0, bytes, 0x00020000); }
DI unsigned rfl(unsigned v) { return __builtin_amdgcn_readfirstlane(v); }
DI int lane() { int v; asm volatile("v_mbcnt_lo_u32_b32 %0, -1, 0\n\tv_mbcnt_hi_u32_b32 %0, -1, %0" : "=v"(v)); return v; }
#define MFMA32(a, b, c) __builtin_amdgcn_mfma_f32_32x32x16_bf16((a), (b), (c), 0, 0, 0)
#define MFMA16(a, b, c) __builtin_amdgcn_mfma_f32_16x16x32_bf16((a), (b), (c), 0, 0, 0)
#define BAR() asm volatile("s_waitcnt lgkmcnt(0)\n\ts_barrier" ::: "memory")
template <int N> DI void waitv() { asm volatile("s_waitcnt vmcnt(%0)" ::"i"(N) : "memory"); }
#define TOUCH(x) asm volatile("" : "+v"(x))
#define SB() __builtin_amdgcn_sched_barrier(0)
DI void drain() { waitv<0>(); __builtin_amdgcn_s_waitcnt(0x0F70); }

template <int OFF, bool SC1> DI void dma16(rsrc_t rs, unsigned voff, unsigned soff, unsigned lds) {
  if constexpr (SC1) asm volatile("s_mov_b32 m0, %0\n\ts_nop 0\n\tbuffer_load_dwordx4 %1, %2, %3 offen offset:%4 sc1 lds" ::"s"(lds), "v"(voff), "s"(rs), "s"(soff), "i"(OFF) : "memory");
  else asm volatile("s_mov_b32 m0, %0\n\ts_nop 0\n\tbuffer_load_dwordx4 %1, %2, %3 offen offset:%4 lds" ::"s"(lds), "v"(voff), "s"(rs), "s"(soff), "i"(OFF) : "memory");
}
template <int N> DI void dmaN(rsrc_t rs, unsigned voff, unsigned soff, unsigned lds) {
  if constexpr (N == 1) dma16<0, true>(rs, voff, soff, lds);
  else if constexpr (N == 2) asm volatile("s_mov_b32 m0, %0\n\ts_nop 0\n\tbuffer_load_dwordx4 %1, %2, %3 offen sc1 lds\n\tbuffer_load_dwordx4 %1, %2, %3 offen offset:1024 sc1 lds" ::"s"(lds), "v"(voff), "s"(rs), "s"(soff) : "memory");
  else if constexpr (N == 3) asm volatile("s_mov_b32 m0, %0\n\ts_nop 0\n\tbuffer_load_dwordx4 %1, %2, %3 offen sc1 lds\n\tbuffer_load_dwordx4 %1, %2, %3 offen offset:1024 sc1 lds\n\tbuffer_load_dwordx4 %1, %2, %3 offen offset:2048 sc1 lds" ::"s"(lds), "v"(voff), "s"(rs), "s"(soff) : "memory");
  else asm volatile("s_mov_b32 m0, %0\n\ts_nop 0\n\tbuffer_load_dwordx4 %1, %2, %3 offen sc1 lds\n\tbuffer_load_dwordx4 %1, %2, %3 offen offset:1024 sc1 lds\n\tbuffer_load_dwordx4 %1, %2, %3 offen offset:2048 sc1 lds\n\tbuffer_load_dwordx4 %1, %2, %3 offen offset:3072 sc1 lds" ::"s"(lds), "v"(voff), "s"(rs), "s"(soff) : "memory");
}
DI bf16x8 lds128(unsigned a) { return *(const LAS bf16x8*)(uintptr_t)a; }
DI f32x4 lds128f(unsigned a) { return *(const LAS f32x4*)(uintptr_t)a; }
DI s16x4 ldstr(unsigned a) { return __builtin_amdgcn_ds_read_tr16_b64_v4i16((LAS s16x4*)(uintptr_t)a); }
DI bf16x8 trfrag(unsigned a0, unsigned a1) { s16x4 lo = ldstr(a0), hi = ldstr(a1); return __builtin_shufflevector(lo, hi, 0, 1, 2, 3, 4, 5, 6, 7); }
DI unsigned pk2(float lo, float hi) { f32x2 v = {lo, hi}; bf2_t r = __builtin_convertvector(v, bf2_t); return __builtin_bit_cast(unsigned, r); }
DI float bflo(unsigned u) { return __uint_as_float(u << 16); }
DI float bfhi(unsigned u) { return __uint_as_float(u & 0xffff0000u); }
DI float swapadd(float v) { auto r = __builtin_amdgcn_permlane32_swap(__float_as_uint(v), __float_as_uint(v), false, false); return __uint_as_float(r[0]) + __uint_as_float(r[1]); }
DI float swapmax(float v) { auto r = __builtin_amdgcn_permlane32_swap(__float_as_uint(v), __float_as_uint(v), false, false); return fmaxf(__uint_as_float(r[0]), __uint_as_float(r[1])); }
DI float swap16add(float v) { auto r = __builtin_amdgcn_permlane16_swap(__float_as_uint(v), __float_as_uint(v), false, false); return __uint_as_float(r[0]) + __uint_as_float(r[1]); }
DI float swap16max(float v) { auto r = __builtin_amdgcn_permlane16_swap(__float_as_uint(v), __float_as_uint(v), false, false); return fmaxf(__uint_as_float(r[0]), __uint_as_float(r[1])); }
template <int CTRL> DI float dppf(float v) { return __uint_as_float(__builtin_amdgcn_update_dpp(0, __float_as_uint(v), CTRL, 0xf, 0xf, true)); }
DI float max16(float v) { v = fmaxf(v, dppf<0xB1>(v)); v = fmaxf(v, dppf<0x4E>(v)); v = fmaxf(v, dppf<0x141>(v)); return fmaxf(v, dppf<0x140>(v)); }
DI float sum8(float v) { v += dppf<0xB1>(v); v += dppf<0x4E>(v); return v + dppf<0x141>(v); }
DI float dot2(unsigned a, unsigned b, float c) { return __builtin_amdgcn_fdot2_f32_bf16(__builtin_bit_cast(bf2_t, a), __builtin_bit_cast(bf2_t, b), c, false); }
DI float gelu(float x) {
  const float t = x * __builtin_fmaf(x * x, -0.10294324f, -2.3022082f);
  return x * __builtin_amdgcn_rcpf(1.0f + __builtin_amdgcn_exp2f(t));
}
DI void st128(rsrc_t rs, unsigned off, u32x4 v) { __builtin_amdgcn_raw_buffer_store_b128(v, rs, off, 0, 16); }
DI void st128s(rsrc_t rs, unsigned voff, unsigned soff, u32x4 v) { __builtin_amdgcn_raw_buffer_store_b128(v, rs, voff, soff, 16); }
DI bf16x8 ld128(rsrc_t rs, unsigned voff, unsigned soff) { return __builtin_bit_cast(bf16x8, __builtin_amdgcn_raw_buffer_load_b128(rs, voff, soff, 16)); }
template <int OFF> DI void ldw(bf16x8& r, rsrc_t rs, unsigned voff, unsigned soff) {
  asm volatile("buffer_load_dwordx4 %0, %1, %2, %3 offen offset:%4 sc1" : "+v"(r) : "v"(voff), "s"(rs), "s"(soff), "i"(OFF) : "memory");
}
template <int OFF> DI void ldn(bf16x8& r, rsrc_t rs, unsigned voff, unsigned soff) {
  asm volatile("buffer_load_dwordx4 %0, %1, %2, %3 offen offset:%4 sc1" : "=v"(r) : "v"(voff), "s"(rs), "s"(soff), "i"(OFF) : "memory");
}
template <int N> DI void got4(bf16x8 (&f)[4]) { asm volatile("s_waitcnt vmcnt(%4)" : "+v"(f[0]), "+v"(f[1]), "+v"(f[2]), "+v"(f[3]) : "i"(N) : "memory"); }
template <int OFF> DI void ldsw(bf16x8& r, unsigned a) { asm volatile("ds_read_b128 %0, %1 offset:%2" : "+v"(r) : "v"(a), "i"(OFF) : "memory"); }
template <int V> struct IC { static constexpr int v = V; };
template <int B, int E, class Fn> DI void sfor(Fn&& fn) { if constexpr (B < E) { fn(IC<B>{}); sfor<B + 1, E>(fn); } }
DI u32x4 pack8(const f32x16& a, int o) { u32x4 p; p[0] = pk2(a[o], a[o + 1]); p[1] = pk2(a[o + 2], a[o + 3]); p[2] = pk2(a[o + 4], a[o + 5]); p[3] = pk2(a[o + 6], a[o + 7]); return p; }

DI bf16x8 split_op(float v, bool first, bool on) {
  const unsigned hi = __float_as_uint(v) & 0xffff0000u, lo = pk2(v - __uint_as_float(hi), 0.f);
  u32x4 o = {0u, 0u, 0u, 0u};
  if (first) { o[0] = hi | (hi >> 16); o[1] = lo | (lo << 16); } else { o[0] = (hi >> 16) | (lo << 16); o[1] = o[0]; }
  if (!on) { o[0] = 0u; o[1] = 0u; }
  return __builtin_bit_cast(bf16x8, o);
}

__global__ void __launch_bounds__(512, 2) k1_qkv(const u16* __restrict__ x, const u16* __restrict__ wqkv, const u16* __restrict__ wo, const u16* __restrict__ w1,
                                                 const u16* __restrict__ w2, const u16* __restrict__ w3, const u16* __restrict__ w4, char* __restrict__ ws) {
  extern __shared__ __attribute__((aligned(16))) char smem[];
  const unsigned lds0 = (unsigned)(uintptr_t)smem;
  const int b = blockIdx.x, l = lane();
  const unsigned w = rfl(threadIdx.x >> 6);
  const unsigned xcd = b & 7, r = b >> 3;
  const unsigned rb = (xcd >> 1) * 8 + (r >> 2), cb = (xcd & 1) * 4 + (r & 3);
  constexpr unsigned SLOT = 32768, LC = 4 * SLOT, LT = LC + 20480;
  STDECL; STAMP(0); STAMPC(40);
  const rsrc_t rx = mkrsrc(x, S * D * 2), rw = mkrsrc(wqkv, D * 1536 * 2), rws = mkrsrc(ws, WS_END);
  const int h = l >> 5, l31 = l & 31, G = (l >> 4) & 1, q = (l >> 2) & 3, p4 = l & 3;
  const unsigned hw = w & 3;
  const unsigned vx0 = (l >> 3) * 1024 + (((l & 7) ^ ((l >> 3) & 6)) << 4), vx1 = (vx0 ^ 16) + 8192;
  const unsigned vsw = ((l & 7) ^ (G << 2)) << 4, vw0 = (l >> 3) * 3072 + vsw, vw1 = vw0 + 8 * 3072;
  auto issue = [&](int c) {
    const unsigned sx = (rb * 64 + hw * 16) * 1024 + c * 128, sw = (c * 64 + hw * 16) * 3072 + cb * 128, lw = lds0 + (c & 3) * SLOT + hw * 2048;
    dma16<0, false>(rx, vx0, sx, lw);
    dma16<0, false>(rw, vw0, sw, lw + 8192); dma16<0, false>(rw, vw1, sw, lw + 8192 + 1024);
    dma16<0, false>(rx, vx1, sx, lw + 1024);
    dma16<0, false>(rw, vw0, sw + 1024, lw + 16384); dma16<0, false>(rw, vw1, sw + 1024, lw + 16384 + 1024);
    dma16<0, false>(rw, vw0, sw + 2048, lw + 24576); dma16<0, false>(rw, vw1, sw + 2048, lw + 24576 + 1024);
  };
  if (w >= 4) issue(0);
  asm volatile("s_barrier" ::: "memory");
  if (w >= 4) {
    issue(1);
    STAMP(1);
    const unsigned bit = hw >> 1, hf = hw & 1, big3 = b < 128;
    const unsigned bp = big3 ? F * 2 : 1024, bso = big3 ? (b >> 2) * 16 * (F * 2) + (b & 3) * 7 * 128 : ((b - 128) >> 3) * 7 * 16384 + (b & 7) * 128, bst = big3 ? 128 : 16384;
    const unsigned bdo = big3 ? TW_3 + (((b & 3) * 14 + hf) * 32 + (b >> 2)) * 1024 : TW_4 + (((b & 7) * 2 + hf) * 112 + ((b - 128) >> 3) * 7) * 1024, bdt = big3 ? 65536 : 1024;
    const unsigned sso = ((b >> 3) * 16 + hf * 8) * 1024 + (b & 7) * 128, sdo = (((b & 7) * 2 + hf) * 32 + (b >> 3)) * 1024;
    const rsrc_t rbig = mkrsrc(big3 ? w3 : w4, D * F * 2);
    const unsigned vs = (l >> 3) * 1024 + vsw, vb = (l >> 3) * bp + vsw;
    const rsrc_t rp0 = mkrsrc(bit ? w1 : wo, D * D * 2), rp1 = mkrsrc(bit ? (big3 ? w3 : w4) : w2, bit ? D * F * 2 : D * D * 2);
    const unsigned vp1 = bit ? vb : vs, bsh = bso + hf * 8 * bp, sp1 = bit ? bsh : sso, dp1 = bit ? bdo : TW_2 + sdo;
    auto piece = [&](int i) {
      const unsigned lc = lds0 + LC + (2 * i + bit) * 2048 + hf * 1024;
      if (i == 0) dma16<0, false>(rp0, vs, sso, lc);
      else if (i == 1) dma16<0, false>(rp1, vp1, sp1, lc);
      else dma16<0, false>(rbig, vb, bsh + (2 * i - 3 + bit) * bst, lc);
    };
    float s0 = 0.f, s1 = 0.f, q0 = 0.f, q1 = 0.f;
    const unsigned xs = lds0 + hw * 2048 + l * 16;
#pragma unroll
    for (int c = 0; c < 8; ++c) {
      if (c == 0) waitv<8>(); else if (c == 1) waitv<17>(); else if (c < 6) waitv<18>(); else if (c == 6) waitv<9>(); else waitv<0>();
      BAR();
      STAMP(4 + c);
      const u32x4 a0 = __builtin_bit_cast(u32x4, lds128(xs + (c & 3) * SLOT)), a1 = __builtin_bit_cast(u32x4, lds128(xs + 1024 + (c & 3) * SLOT));
      if (c == 0) issue(2);
      if (c < 5) { issue(c + 3); piece(c); }
#pragma unroll
      for (int j = 0; j < 4; ++j) { s0 = dot2(a0[j], 0x3f803f80u, s0); q0 = dot2(a0[j], a0[j], q0); s1 = dot2(a1[j], 0x3f803f80u, s1); q1 = dot2(a1[j], a1[j], q1); }
    }
    const float m0_ = sum8(s0) * (1.f / D), m1_ = sum8(s1) * (1.f / D);
    const float r0 = __builtin_amdgcn_rsqf(fmaxf(sum8(q0) * (1.f / D) - m0_ * m0_, 0.f) + EPS), r1 = __builtin_amdgcn_rsqf(fmaxf(sum8(q1) * (1.f / D) - m1_ * m1_, 0.f) + EPS);
    if ((l & 7) == 0) {
      LAS float* t = (LAS float*)(uintptr_t)(lds0 + LT + (hw * 16 + (l >> 3)) * 4);
      t[0] = m0_; t[8] = m1_; t[64] = r0; t[72] = r1;
    }
    BAR();
    const unsigned ta = lds0 + LC + bit * 2048 + (4 * h + q) * 128 + (((4 * hf + 2 * G + (p4 >> 1)) ^ ((q >> 1) << 2)) << 4) + 8 * (p4 & 1);
#pragma unroll
    for (int i = 0; i < 5; ++i) {
      const unsigned dst = i == 0 ? TW_O + bit * 524288 + sdo : i == 1 ? dp1 : bdo + (2 * i - 3 + bit) * bdt;
      st128s(rws, l * 16, dst, __builtin_bit_cast(u32x4, trfrag(ta + i * 4096, ta + i * 4096 + 1024)));
    }
    STAMP(2);
    if (w == 4) { unsigned* fz = (unsigned*)(ws + WS_FLAG) + b * 161; fz[l] = 0u; fz[64 + l] = 0u; if (l < 33) fz[128 + l] = 0u; }
  } else {
    const unsigned tt = w & 1, hp = w >> 1;
    unsigned wm0, wm1;
    if (w < 3) {
      const unsigned vo = (l >> 1) * 3072 + (l & 1) * 64, so = (128 + (r >> 2) * 48) * 3072 + w * 1024 + cb * 128;
      asm volatile("buffer_load_dword %0, %2, %3, %4 offen\n\tbuffer_load_dword %1, %2, %3, %5 offen" : "=&v"(wm0), "=&v"(wm1) : "v"(vo), "s"(rw), "s"(so), "s"(so + 16 * 3072) : "memory");
    } else {
      const unsigned vo = (l >> 2) * 1024 + (l & 3) * 64, so = (rb * 64 + (r & 3) * 16) * 1024;
      asm volatile("buffer_load_dword %0, %2, %3, %4 offen offset:256\n\tbuffer_load_dword %1, %2, %3, %4 offen offset:512\n\tbuffer_load_dword %0, %2, %3, %4 offen offset:768"
                   : "=&v"(wm0), "=&v"(wm1) : "v"(vo), "s"(rx), "s"(so) : "memory");
    }
    STAMP(1);
    unsigned xa[2][4];
#pragma unroll
    for (int s = 0; s < 4; ++s) {
      xa[0][s] = lds0 + (tt * 4 + (l31 >> 3)) * 1024 + (l31 & 7) * 128 + (((2 * s + h) ^ ((l31 & 6) | ((l31 >> 3) & 1))) << 4); xa[1][s] = xa[0][s] + 2 * SLOT;
      TOUCH(xa[0][s]); TOUCH(xa[1][s]);
    }
    unsigned wa0 = lds0 + 8192 + (8 * h + q) * 128 + (((4 * hp + 2 * G + (p4 >> 1)) ^ ((q >> 1) << 2)) << 4) + 8 * (p4 & 1), wa1 = wa0 + 2 * SLOT;
    TOUCH(wa0); TOUCH(wa1);
    f32x16 aq = {}, ak = {}, av = {};
    float cq = 0.f, ck = 0.f, cv = 0.f, dq = 0.f, dk = 0.f, dv = 0.f;
    auto body = [&](auto TT) {
      bf16x8 f[2][4];
      auto wadr = [&](int s, int p) { return ((s & 8) ? wa1 : wa0) + ((s >> 2) & 1) * SLOT + p * 8192 + (s & 3) * 2048; };
      auto xadr = [&](int s) { return xa[(s >> 3) & 1][s & 3] + ((s >> 2) & 1) * SLOT; };
      auto cs2 = [&](const bf16x8& v, float& c, float& d) {
        const u32x4 u = __builtin_bit_cast(u32x4, v); c = dot2(u[2 * TT.v], 0x3f803f80u, c); d = dot2(u[2 * TT.v + 1], 0x3f803f80u, d); };
      BAR();
      STAMP(4);
      f[0][0] = lds128(xadr(0)); f[0][1] = trfrag(wadr(0, 0), wadr(0, 0) + 512); f[0][2] = trfrag(wadr(0, 1), wadr(0, 1) + 512); f[0][3] = trfrag(wadr(0, 2), wadr(0, 2) + 512);
#pragma unroll
      for (int s = 0; s < 32; ++s) {
        const bf16x8* o = f[s & 1];
        bf16x8* n = f[(s + 1) & 1];
        if ((s & 3) == 3 && s < 31) { BAR(); STAMP(5 + (s >> 2)); }
        SB();
        aq = MFMA32(o[1], o[0], aq); TOUCH(aq);
        if (s < 31) { n[0] = lds128(xadr(s + 1)); n[1] = trfrag(wadr(s + 1, 0), wadr(s + 1, 0) + 512); }
        cs2(o[1], cq, dq);
        SB();
        ak = MFMA32(o[2], o[0], ak); TOUCH(ak);
        if (s < 31) n[2] = trfrag(wadr(s + 1, 1), wadr(s + 1, 1) + 512);
        cs2(o[2], ck, dk);
        SB();
        av = MFMA32(o[0], o[3], av); TOUCH(av);
        if (s < 31) n[3] = trfrag(wadr(s + 1, 2), wadr(s + 1, 2) + 512);
        cs2(o[3], cv, dv);
        SB();
      }
    };
    if (tt) body(IC<1>{}); else body(IC<0>{});
    asm volatile("s_waitcnt vmcnt(0)" :: "v"(wm0), "v"(wm1) : "memory");
    STAMP(12);
    cq = swapadd(cq + dq); ck = swapadd(ck + dk); cv = swapadd(cv + dv);
    LAS float* ct = (LAS float*)(uintptr_t)(lds0 + LT + 512 + l31 * 4);
    if (h == 0) { ct[w * 96] = cq; ct[w * 96 + 32] = ck; ct[w * 96 + 64] = cv; }
    BAR();
    cq += ct[(w ^ 1) * 96]; ck += ct[(w ^ 1) * 96 + 32]; cv += ct[(w ^ 1) * 96 + 64];
    const LAS float* st = (const LAS float*)(uintptr_t)(lds0 + LT + tt * 128);
    const float mu = st[l31], rs = st[64 + l31];
    f32x4 rv[4];
#pragma unroll
    for (int j = 0; j < 4; ++j) rv[j] = *(const LAS f32x4*)(st + 64 + 8 * j + 4 * h);
    const bf16x8 mo = split_op(-mu, false, true);
    aq = MFMA32(split_op(cq, true, h == 0), mo, aq); ak = MFMA32(split_op(ck, true, h == 0), mo, ak); av = MFMA32(mo, split_op(cv, true, h == 0), av);
    const float qs = rs * 0.36067376f;
    float n0 = 0.f, n1 = 0.f;
#pragma unroll
    for (int i = 0; i < 16; ++i) { aq[i] *= qs; ak[i] *= rs; av[i] *= rv[i >> 2][i & 3]; }
    const unsigned head = cb * 4 + hp * 2, t32 = rb * 2 + tt;
    const unsigned vq = (h * 2048 + l31) * 16, sq = WS_Q + head * 65536 + t32 * 512;
    const unsigned vk = h * 512 + ((l31 & 19) | ((l31 & 4) << 1) | ((l31 & 8) >> 1)) * 16, sk = WS_KV + head * 131072 + t32 * 2048;
    const unsigned vv = G * 131072 + 1024 + h * 512 + (l & 15) * 16;
    st128s(rws, vk, sk, pack8(ak, 0)); st128s(rws, vk, sk + 131072, pack8(ak, 8));
    st128s(rws, vv, sk, pack8(av, 0)); st128s(rws, vv + 256, sk, pack8(av, 8));
    st128s(rws, vq, sq, pack8(aq, 0)); st128s(rws, vq, sq + 65536, pack8(aq, 8));
#pragma unroll
    for (int i = 0; i < 8; ++i) { n0 += ak[i] * ak[i]; n1 += ak[8 + i] * ak[8 + i]; }
    n0 = swap16max(max16(swapadd(n0))); n1 = swap16max(max16(swapadd(n1)));
    if (l == 0) { float* km = (float*)(ws + WS_KMAX) + head * 64 + t32; km[0] = n0; km[64] = n1; }
  }
  STAMP(3); STAMPC(41); STDUMP(0);
}

DI void poll_flags(const unsigned* p, bool need) {
#pragma unroll 1
  for (unsigned sp = 0; sp < (1u << 20); ++sp) {
    const unsigned v = need ? __hip_atomic_load(p, RLX_AGENT) : 1u;
    if (__all(v != 0u)) break;
    __builtin_amdgcn_s_sleep(1);
  }
  asm volatile("" ::: "memory");
}
DI void set_flag(unsigned* p, int l) { drain(); if (l == 0) __hip_atomic_store(p, 1u, RLX_AGENT); }
template <int ST> DI void lds_stats(unsigned a, float& mu, float& var) {
  float s = 0.f, q = 0.f;
#pragma unroll
  for (int i = 0; i < 4; ++i) { const f32x4 v = lds128f(a + (i & 1) * 512 + (i >> 1) * ST); s += v[0] + v[2]; q += v[1] + v[3]; }
  s = swapadd(s); q = swapadd(q);
  mu = s * (1.f / D); var = fmaxf(q * (1.f / D) - mu * mu, 0.f);
  TOUCH(mu);
}
DI void put_stats(rsrc_t rs, unsigned vl, unsigned soff, const f32x16& v) {
  float s = 0.f, q = 0.f;
#pragma unroll
  for (int i = 0; i < 16; ++i) { s += v[i]; q += v[i] * v[i]; }
  s = swapadd(s); q = swapadd(q);
  if (vl < 512) __builtin_amdgcn_raw_buffer_store_b64((u32x2){__float_as_uint(s), __float_as_uint(q)}, rs, vl, soff, 16);
}
DI f32x16 unpack16(const u32x4& p0, const u32x4& p1) {
  f32x16 o;
#pragma unroll
  for (int j = 0; j < 4; ++j) { o[2 * j] = bflo(p0[j]); o[2 * j + 1] = bfhi(p0[j]); o[8 + 2 * j] = bflo(p1[j]); o[8 + 2 * j + 1] = bfhi(p1[j]); }
  return o;
}
template <bool IO> DI f32x16 gemm32(const bf16x8 (&wf)[32], unsigned ba) {
  f32x16 acc = {};
  bf16x8 f[2][8];
  if (IO) waitv<6>();
  BAR();
#pragma unroll
  for (int k = 0; k < 8; ++k) f[0][k] = lds128(ba + (8 * (k >> 1) + (k & 1)) * 1024);
#pragma unroll
  for (int j = 0; j < 4; ++j) {
    if (IO) { if (j == 0) waitv<4>(); else if (j == 1) waitv<2>(); else if (j == 2) waitv<0>(); }
    BAR();
#pragma unroll
    for (int k = 0; k < 8; ++k) {
      SB();
      acc = MFMA32(wf[8 * (k >> 1) + 2 * j + (k & 1)], f[j & 1][k], acc);
      TOUCH(acc);
      if (j < 3) f[(j + 1) & 1][k] = lds128(ba + (8 * (k >> 1) + 2 * j + 2 + (k & 1)) * 1024);
    }
    SB();
  }
  return acc;
}
DI f32x16 colsum(const bf16x8 (&wf)[32]) {
  u32x4 o = {0x3f803f80u, 0x3f803f80u, 0x3f803f80u, 0x3f803f80u};
  f32x16 acc = {};
  TOUCH(o);
  SB();
#pragma unroll
  for (int k = 0; k < 32; ++k) acc = MFMA32(wf[k], __builtin_bit_cast(bf16x8, o), acc);
  TOUCH(acc);
  SB();
  return acc;
}
DI void tile_dma(rsrc_t rs, unsigned voff, unsigned soff, unsigned lds) { dmaN<4>(rs, voff, soff, lds); dmaN<4>(rs, voff, soff + 4096, lds + 4096); }

#define EX(B, i) "v_exp_f32 v[" #B "+" #i "], v[" #B "+" #i "]\n\t"
#define TILE(C, P, KC, KP, LC, LP, HC, HP, U, X0, X1)                                                 \
  EX(C, 0) EX(C, 1)                                                                                   \
  "v_smfmac_f32_32x32x32_bf16 v[2:17], v[" #HP ":" #HP "+3], v[" #P "+8:" #P "+15], v78\n\t"          \
  EX(C, 2) EX(C, 3) X0 EX(C, 4) EX(C, 5)                                                              \
  "v_mfma_f32_32x32x16_bf16 v[" #P ":" #P "+15], v[" #KP ":" #KP "+3], v[50:53], 0\n\t"               \
  "ds_read_b128 v[" #KC ":" #KC "+3], v79 offset:" #U "*2048+4096\n\t"                                \
  "s_mov_b64 exec, %[msk]\n\t"                                                                        \
  "ds_read_b128 v[" #LP ":" #LP "+3], v79 offset:" #U "*2048+3072\n\t"                                \
  "ds_read_b128 v[" #HC ":" #HC "+3], v79 offset:" #U "*2048+1280\n\t"                                \
  "s_mov_b64 exec, -1\n\t"                                                                            \
  EX(C, 6) EX(C, 7) EX(C, 8) EX(C, 9)                                                                 \
  "v_smfmac_f32_32x32x32_bf16 v[2:17], v[" #LC ":" #LC "+3], v[" #C ":" #C "+7], v78\n\t"             \
  EX(C, 10) EX(C, 11) EX(C, 12) X1 EX(C, 13) EX(C, 14) EX(C, 15)                                      \
  "s_waitcnt lgkmcnt(0)\n\t"
#define TILE4(A0, A1, B0, B1, C0, C1, D0, D1)                                                         \
  TILE(18, 34, 54, 58, 62, 66, 70, 74, 0, A0, A1) TILE(34, 18, 58, 54, 66, 62, 74, 70, 1, B0, B1)     \
  TILE(18, 34, 54, 58, 62, 66, 70, 74, 2, C0, C1) TILE(34, 18, 58, 54, 66, 62, 74, 70, 3, D0, D1) "v_add_u32 v79, 0x2000, v79"
#define ATT_OPS "+{v[2:17]}"(oacc), "+{v[18:33]}"(s0), "+{v[34:49]}"(s1), "+{v[54:57]}"(k0), "+{v[58:61]}"(k1), "+{v[62:65]}"(l0), "+{v[66:69]}"(l1), \
                "+{v[70:73]}"(h0), "+{v[74:77]}"(h1), "+{v79}"(ad)
#define ATT_IN "{v[50:53]}"(qf), "{v78}"(idx), [msk] "s"(msk)
#define WARM "buffer_load_dword v80, %[vl], %[rs], %[sw] offen\n\t"
#define WL(B, i) "buffer_load_dwordx4 v[" #B "+4*" #i ":" #B "+4*" #i "+3], %[vl], %[rs], %[sa] offen offset:" #i "*1024 sc1\n\t"
#define DL(i) "buffer_load_dwordx4 %[vl], %[rs], %[sa] offen offset:" #i "*1024 sc1 lds\n\t"
#define ATT_LD [vl] "v"(vl), [rs] "s"(rws)
#define ATT_1() asm volatile(TILE4("", "", "", "", "", "", "", "") : ATT_OPS : ATT_IN : "memory")
#define ATT_W(B, E, n, g) asm volatile(TILE4(WARM, WL(B, 0), "", WL(B, 1), "", WL(B, 2), "", WL(B, 3)) \
    : ATT_OPS, "+{v80}"(dum), "={v[" #B ":" #E "]}"(wt[n]) : ATT_IN, ATT_LD, [sa] "s"(wso + n * 4096), [sw] "s"(wrm + g * 8192) : "memory")
#define ATT_D0(src, dst) asm volatile("s_mov_b32 m0, %[ld]\n\t" TILE4("", DL(0), "", DL(1), "", DL(2), "", DL(3)) : ATT_OPS : ATT_IN, ATT_LD, [sa] "s"(src), [ld] "s"(dst) : "memory")
#define ATT_D(src, dst, g) asm volatile("s_mov_b32 m0, %[ld]\n\t" TILE4(WARM, DL(0), "", DL(1), "", DL(2), "", DL(3)) \
    : ATT_OPS, "+{v80}"(dum) : ATT_IN, ATT_LD, [sa] "s"(src), [ld] "s"(dst), [sw] "s"(wrm + g * 8192) : "memory")
#define ATT_DH(src, dst, g) asm volatile("s_mov_b32 m0, %[ld]\n\t" TILE4(WARM, DL(0), "", DL(1), "", "", "", "") \
    : ATT_OPS, "+{v80}"(dum) : ATT_IN, ATT_LD, [sa] "s"(src), [ld] "s"(dst), [sw] "s"(wrm + g * 8192) : "memory")

__global__ void __launch_bounds__(512, 2) k2_main(const u16* __restrict__ x, u16* __restrict__ out, char* __restrict__ ws) {
  extern __shared__ __attribute__((aligned(16))) char smem[];
  const unsigned lds0 = (unsigned)(uintptr_t)smem;
  const int b = blockIdx.x;
  const unsigned w = rfl(threadIdx.x >> 6);
  const unsigned xcd = b & 7, r = b >> 3;
  unsigned* flags = (unsigned*)(ws + WS_FLAG);
  const rsrc_t rws = mkrsrc(ws, WS_END);
  const unsigned rq = xcd * 8 + (r >> 2), c4 = r & 3;
  const unsigned rb = xcd * 4 + (r >> 3), c8 = r & 7;
  const unsigned wi = w & 3;
  const unsigned r2a = lds0 + wi * 16384, r2b = lds0 + 65536 + wi * 8192, r2c = lds0 + L_Z + wi * 7168;
  bf16x8 wf[32];
  u32x16 wt[8];
  STDECL; STAMP(0);
  {
    const int l = lane(), h = l >> 5, l31 = l & 31;
    const unsigned vl = l * 16;
    const unsigned head = xcd * 4 + (r & 3), qb = r >> 2;
    const unsigned tok = qb * 256 + w * 32 + l31;
    bf16x8 qf; float km;
    asm volatile("buffer_load_dwordx4 %0, %1, %2, %3 offen" : "={v[50:53]}"(qf) : "v"((h * 2048 + tok) * 16), "s"(rws), "s"(WS_Q + head * 65536) : "memory");
    asm volatile("buffer_load_dword %0, %1, %2, %3 offen" : "=v"(km) : "v"(l * 4), "s"(rws), "s"(WS_KMAX + head * 256) : "memory");
    const unsigned kv = WS_KV + head * 131072;
    dma16<0, true>(rws, vl, kv + w * 1024, lds0 + w * 1024); dma16<0, true>(rws, vl, kv + 8192 + w * 1024, lds0 + 8192 + w * 1024);
    dmaN<4>(rws, vl, kv + 16384 + w * 4096, lds0 + 16384 + w * 4096);
    asm volatile("s_waitcnt vmcnt(6)" : "+{v[50:53]}"(qf), "+v"(km) :: "memory");
    km = swapmax(swap16max(max16(km)));
    float qn = 0.f;
    { const u32x4 u = __builtin_bit_cast(u32x4, qf);
#pragma unroll
      for (int j = 0; j < 4; ++j) qn = dot2(u[j], u[j], qn); }
    qn = swapadd(qn);
    { const unsigned slowf = __all(qn * km * 1.05f <= 48.f * 48.f) ? 0u : 1u;
      if (l == 0) *(LAS unsigned*)(uintptr_t)(lds0 + L_ST + 1024 + w * 4) = slowf; }
    const unsigned cst = l31 == 16 ? 0x3f803f80u : 0u;
    const bf16x8 vc = __builtin_bit_cast(bf16x8, (u32x4){cst, cst, cst, cst});
    f32x16 oacc = {};
    int idx = 0xDDDD;
    unsigned ad = lds0 + h * 512 + l31 * 16;
    const unsigned wso = (w < 4 ? TW_O : TW_1) + (c4 * 4 + wi) * 32768, w2o = TW_2 + (c4 * 4 + wi) * 32768, hw = w >> 2;
    const unsigned wrm = TW_3 + r * 114688 + w * 1024;
    STAMP(1);
    waitv<4>();
    BAR();
    STAMP(2); STAMPC(40);
    const u32x4 sf0 = *(const LAS u32x4*)(uintptr_t)(lds0 + L_ST + 1024), sf1 = *(const LAS u32x4*)(uintptr_t)(lds0 + L_ST + 1040);
    if (rfl(sf0[0] | sf0[1] | sf0[2] | sf0[3] | sf1[0] | sf1[1] | sf1[2] | sf1[3]) == 0u) {
      f32x16 s0, s1 = {};
      bf16x8 k0, k1, l0 = vc, l1 = vc, h0 = vc, h1 = vc;
      const unsigned long long msk = 0x0000ffff0000ffffull;
      asm volatile("ds_read_b128 v[54:57], v79\n\tds_read_b128 v[58:61], v79 offset:2048\n\ts_mov_b64 exec, %[msk]\n\tds_read_b128 v[62:65], v79 offset:1024\n\ts_mov_b64 exec, -1\n\t"
                   "s_waitcnt lgkmcnt(0)\n\tv_mfma_f32_32x32x16_bf16 v[18:33], v[54:57], v[50:53], 0\n\ts_nop 7\n\ts_nop 4"
                   : "={v[18:33]}"(s0), "={v[54:57]}"(k0), "={v[58:61]}"(k1), "+{v[62:65]}"(l0) : "{v79}"(ad), "{v[50:53]}"(qf), [msk] "s"(msk) : "memory");
      unsigned dum = 0;
      STAMPC(44);
      ATT_D0(kv + 49152 + w * 4096, lds0 + 49152 + w * 4096);
      STAMPC(45);
      waitv<4>(); BAR();
      STAMPC(46);
      ATT_D(kv + 81920 + w * 4096, lds0 + 81920 + w * 4096, 0);
      ATT_DH(kv + 114688 + w * 2048, lds0 + 114688 + w * 2048, 1);
      STAMPC(47);
      ATT_W(128, 143, 0, 2);
      ATT_W(144, 159, 1, 3);
      STAMPC(48);
      waitv<10>(); BAR();
      STAMPC(49);
      ATT_W(160, 175, 2, 4);
      ATT_W(176, 191, 3, 5);
      STAMPC(50);
      ATT_W(192, 207, 4, 6);
      ATT_W(208, 223, 5, 7);
      STAMPC(51);
      ATT_W(224, 239, 6, 8);
      ATT_W(240, 255, 7, 9);
      STAMPC(52);
      ATT_D(w2o + 24576 + hw * 3072, r2c + hw * 3072, 10);
      STAMPC(53);
      BAR();
      STAMPC(54);
      ATT_D(w2o + hw * 8192, r2a + hw * 8192, 11);
      ATT_D(w2o + hw * 8192 + 4096, r2a + hw * 8192 + 4096, 12);
      STAMPC(55);
      ATT_D(w2o + 16384 + hw * 4096, r2b + hw * 4096, 13);
      ATT_1();
      STAMPC(56);
      asm volatile("s_waitcnt vmcnt(0)" : "+{v[128:143]}"(wt[0]), "+{v[144:159]}"(wt[1]), "+{v[160:175]}"(wt[2]), "+{v[176:191]}"(wt[3]), "+{v[192:207]}"(wt[4]),
                   "+{v[208:223]}"(wt[5]), "+{v[224:239]}"(wt[6]), "+{v[240:255]}"(wt[7]), "+{v80}"(dum) :: "memory");
      asm volatile("s_nop 1\n\tv_smfmac_f32_32x32x32_bf16 v[2:17], v[74:77], v[42:49], v78\n\ts_nop 7\n\ts_nop 4" : "+{v[2:17]}"(oacc) : "{v[34:49]}"(s1), "{v[74:77]}"(h1), "{v78}"(idx));
    } else {
      tile_dma(rws, vl, kv + 49152 + w * 10240, lds0 + 49152 + w * 10240); dmaN<2>(rws, vl, kv + 49152 + w * 10240 + 8192, lds0 + 49152 + w * 10240 + 8192);
      waitv<0>(); BAR();
      float mx = -3.0e38f;
      const f32x16 zero = {};
#pragma unroll 1
      for (int t = 0; t < 64; ++t) {
        const bf16x8 kf = lds128(ad); bf16x8 v0 = vc, v1 = vc;
        if (l31 < 16) { v0 = lds128(ad + 1024); v1 = lds128(ad + 1280); }
        f32x16 s = MFMA32(kf, qf, zero);
        float tm = s[0];
#pragma unroll
        for (int i = 1; i < 16; ++i) tm = fmaxf(tm, s[i]);
        const float mn = fmaxf(mx, swapmax(tm)), al = __builtin_amdgcn_exp2f(mx - mn);
        mx = mn;
#pragma unroll
        for (int i = 0; i < 16; ++i) { s[i] = __builtin_amdgcn_exp2f(s[i] - mn); oacc[i] *= al; }
        oacc = __builtin_amdgcn_smfmac_f32_32x32x32_bf16(__builtin_bit_cast(bf8_t, v0), __builtin_bit_cast(bf16_t, __builtin_shufflevector(s, s, 0, 1, 2, 3, 4, 5, 6, 7)), oacc, idx, 0, 0);
        oacc = __builtin_amdgcn_smfmac_f32_32x32x32_bf16(__builtin_bit_cast(bf8_t, v1), __builtin_bit_cast(bf16_t, __builtin_shufflevector(s, s, 8, 9, 10, 11, 12, 13, 14, 15)), oacc, idx, 0, 0);
        ad += 2048;
      }
      BAR();
#pragma unroll
      for (int k = 0; k < 32; ++k) { const u32x4 t = __builtin_amdgcn_raw_buffer_load_b128(rws, vl + (k & 3) * 1024, wso + (k >> 2) * 4096, 16);
        wt[k >> 2][(k & 3) * 4] = t[0]; wt[k >> 2][(k & 3) * 4 + 1] = t[1]; wt[k >> 2][(k & 3) * 4 + 2] = t[2]; wt[k >> 2][(k & 3) * 4 + 3] = t[3]; }
      tile_dma(rws, vl, w2o + hw * 8192, r2a + hw * 8192); dmaN<4>(rws, vl, w2o + 16384 + hw * 4096, r2b + hw * 4096); dmaN<4>(rws, vl, w2o + 24576 + hw * 3072, r2c + hw * 3072);
    }
    STAMP(3); STAMPC(41);
    const float inv = __builtin_amdgcn_rcpf(swapadd(h == 0 ? oacc[8] : 0.f));
#pragma unroll
    for (int i = 0; i < 8; ++i) oacc[i] *= inv;
    st128(rws, WS_O + (qb * 8 + w) * 32768 + (head * 2 + h) * 512 + l31 * 16, pack8(oacc, 0));
    set_flag(flags + FLAG_O + ((qb * 8 + w) * 4 + (head >> 3)) * 32 + (head & 7), l);
#pragma unroll
    for (int k = 0; k < 32; ++k) wf[k] = __builtin_bit_cast(bf16x8, (u32x4){wt[k >> 2][(k & 3) * 4], wt[k >> 2][(k & 3) * 4 + 1], wt[k >> 2][(k & 3) * 4 + 2], wt[k >> 2][(k & 3) * 4 + 3]});
    STAMP(4);
    BAR();
  }
  f32x16 cs;
  unsigned wsel = w;
  asm volatile("" : "+s"(wsel));
  const unsigned wv = w < 4 ? w : w - 1;
  {
  const int l = lane(), h = l >> 5, l31 = l & 31;
  const unsigned vl = l * 16;
  unsigned ba = lds0 + L_P + vl;
  TOUCH(ba);
  const unsigned tileo = rq * 32768 + (c4 * 16 + wi * 4) * 512;
  const unsigned stato = rq * 4096 + c4 * 1024 + (wi >> 1) * 512 + (wi & 1) * 8;
  const unsigned pq = lds0 + L_P + wi * 8192;
  auto unstage = [&](unsigned last) {
    const unsigned ua = r2a + vl, ub_ = r2b + vl, uc = r2c + vl;
    ldw<0>(wf[31], rws, vl, last);
    sfor<0, 16>([&](auto K) { ldsw<K.v * 1024>(wf[K.v], ua); });
    sfor<0, 8>([&](auto K) { ldsw<K.v * 1024>(wf[16 + K.v], ub_); });
    sfor<0, 7>([&](auto K) { ldsw<K.v * 1024>(wf[24 + K.v], uc); });
  };
  if (w < 4) {
    u32x2 xr[4];
#pragma unroll
    for (int g = 0; g < 4; ++g) xr[g] = *(const u32x2*)(x + (rq * 32 + l31) * D + c4 * 128 + wi * 32 + 8 * g + 4 * h);
    __builtin_amdgcn_s_setprio(1);
    poll_flags(flags + FLAG_O + (rq * 4 + wi) * 32 + (l & 7), true);
    STAMP(5);
    tile_dma(rws, vl, WS_O + rq * 32768 + wi * 8192, pq);
    STAMP(6);
    f32x16 acc = gemm32<true>(wf, ba);
    STAMP(7);
#pragma unroll
    for (int g = 0; g < 4; ++g) { acc[4 * g] += bflo(xr[g][0]); acc[4 * g + 1] += bfhi(xr[g][0]); acc[4 * g + 2] += bflo(xr[g][1]); acc[4 * g + 3] += bfhi(xr[g][1]); }
    const u32x4 p0 = pack8(acc, 0), p1 = pack8(acc, 8);
    st128s(rws, vl, WS_H + tileo, p0); st128s(rws, vl + 1024, WS_H + tileo, p1);
    f32x16 hres = unpack16(p0, p1);
    put_stats(rws, vl, WS_STH + stato, hres);
    STAMP(8);
    unstage(TW_2 + (c4 * 4 + wi) * 32768 + 31 * 1024);
    set_flag(flags + FLAG_H + (rq * 4 + c4) * 32 + wi, l);
    STAMP(9);
    const unsigned so = TW_3 + (c8 * 7 + (w < 3 ? w : 6)) * 32768;
    asm volatile("s_waitcnt lgkmcnt(0)" ::: "memory");
    tile_dma(rws, vl, so, r2a);
    poll_flags(flags + FLAG_H + (rq * 4 + wi) * 32 + (l & 3), true);
    STAMP(10);
    dmaN<1>(rws, vl, WS_STH + rq * 4096 + wi * 1024, lds0 + L_ST + wi * 1024);
    tile_dma(rws, vl, WS_H + rq * 32768 + wi * 8192, pq);
    __builtin_amdgcn_s_setprio(0);
    waitv<6>(); BAR(); waitv<4>(); BAR(); waitv<2>(); BAR(); waitv<0>(); BAR();
    sfor<8, 31>([&](auto K) {
      constexpr int k = K.v;
      dma16<0, true>(rws, vl, so + k * 1024, k < 16 ? r2a + k * 1024 : k < 24 ? r2b + (k - 16) * 1024 : r2c + (k - 24) * 1024);
      if constexpr (k == 10) BAR();
      waitv<2>();
    });
    STAMP(11);
    float mu, var; { const int l2 = lane(); lds_stats<1024>(lds0 + L_ST + (l2 >> 5) * 2048 + (l2 & 31) * 16, mu, var); }
    const float rs = __builtin_amdgcn_rsqf(var + EPS);
#pragma unroll
    for (int i = 0; i < 16; ++i) hres[i] = (hres[i] - mu) * rs;
    const int dl = (wi & 1) ? (int)(wi >> 1) + 1 : -(int)(wi >> 1) - 1;
    const int rn = (l & 4) ? (int)rq + ((wi & 1) ? 1 : -1) : (int)rq;
    __builtin_amdgcn_s_setprio(1);
    poll_flags(flags + FLAG_G + (rn * 4 + wi) * 32 + (l & 3), rn >= 0 && rn < 64);
    drain();
    STAMP(15);
    { const int l2 = lane(), src = (int)rq * 32 + (l2 & 31) + dl;
      tile_dma(mkrsrc(ws + WS_G, 2u << 20), (src >= 0 && src < S) ? (unsigned)((src >> 5) * 32768 + (src & 31) * 16 + (l2 >> 5) * 512) : 0x7ff00000u, wi * 8192, pq); }
    acc = gemm32<true>(wf, ba);
    STAMP(17);
#pragma unroll
    for (int i = 0; i < 16; ++i) acc[i] += hres[i];
    const u32x4 p2 = pack8(acc, 0), p3 = pack8(acc, 8);
    st128s(rws, vl, WS_X1 + tileo, p2); st128s(rws, vl + 1024, WS_X1 + tileo, p3);
    put_stats(rws, vl, WS_STX + stato, unpack16(p2, p3));
    STAMP(18);
    unstage(TW_3 + (c8 * 7 + wi) * 32768 + 31 * 1024);
    set_flag(flags + FLAG_X + (rq * 4 + c4) * 32 + wi, l);
    STAMP(19);
    poll_flags(flags + FLAG_X + ((rb * 2 + ((l >> 2) & 1)) * 4 + wi) * 32 + (l & 3), true);
    asm volatile("s_waitcnt lgkmcnt(0)" ::: "memory");
    STAMP(21);
#pragma unroll
    for (int j = 0; j < 4; ++j) {
      dmaN<2>(rws, vl, WS_X1 + rb * 65536 + wi * 8192 + j * 2048, r2a + j * 2048);
      dmaN<2>(rws, vl, WS_X1 + rb * 65536 + 32768 + wi * 8192 + j * 2048, r2a + 8192 + j * 2048);
    }
    dmaN<1>(rws, vl, WS_STX + rb * 8192 + wi * 1024, r2b); dmaN<1>(rws, vl, WS_STX + rb * 8192 + 4096 + wi * 1024, r2b + 1024);
    cs = colsum(wf);
  }
  if (wsel >= 4) {
    cs = colsum(wf);
    BAR(); BAR(); BAR(); BAR(); BAR();
    __builtin_amdgcn_s_setprio(1);
    f32x16 acc = gemm32<false>(wf, ba);
    STAMP(12);
    const unsigned so = TW_3 + (c8 * 7 + wv) * 32768;
    if (w != 7) sfor<0, 8>([&](auto K) { ldw<(K.v & 3) * 1024>(wf[K.v], rws, vl, so + (K.v >> 2) * 4096); });
    float mu, var; { const int l2 = lane(); lds_stats<1024>(lds0 + L_ST + (l2 >> 5) * 2048 + (l2 & 31) * 16, mu, var); }
    const float rs = __builtin_amdgcn_rsqf(var + EPS), cz = rs * __builtin_amdgcn_rsqf(var * rs * rs + EPS);
#pragma unroll
    for (int i = 0; i < 16; ++i) acc[i] = gelu((acc[i] - mu * cs[i]) * cz);
    st128s(rws, vl, WS_G + tileo, pack8(acc, 0)); st128s(rws, vl + 1024, WS_G + tileo, pack8(acc, 8));
    STAMP(13);
    set_flag(flags + FLAG_G + (rq * 4 + c4) * 32 + wi, l);
    __builtin_amdgcn_s_setprio(0);
    STAMP(14);
    if (w != 7) sfor<8, 18>([&](auto K) { ldw<(K.v & 3) * 1024>(wf[K.v], rws, vl, so + (K.v >> 2) * 4096); waitv<3>(); });
    STAMP(16);
    BAR();
    if (wsel == 7) unstage(so + 31 * 1024);
    BAR(); BAR(); BAR();
    if (w != 7) sfor<18, 21>([&](auto K) { ldw<(K.v & 3) * 1024>(wf[K.v], rws, vl, so + (K.v >> 2) * 4096); });
    BAR();
    if (w != 7) sfor<21, 32>([&](auto K) { ldw<(K.v & 3) * 1024>(wf[K.v], rws, vl, so + (K.v >> 2) * 4096); waitv<3>(); });
    waitv<0>();
    cs = colsum(wf);
    STAMP(20);
  }
  }
#define KS5(t) (8 * (((t) >> 1) & 3) + 2 * ((t) >> 3) + ((t) & 1))
#define XA5(t) (bb + (KS5(t) >> 3) * 16384 + (KS5(t) & 7) * 1024)
#define W4LD(k) ldw<(((k) % 14) & 3) * 1024>(wf[KS5(k)], rws, vl, so4 + ((k) / 14) * 114688 + (((k) % 14) >> 2) * 4096)
  u32x4 rh, rx1;
  const bool early = w < 4 || w == 7;
  {
    int l = lane();
    const int h = l >> 5, l31 = l & 31;
    const unsigned vl = l * 16;
    const unsigned so4 = TW_4 + (c8 * 2 * 112 + w * 14) * 1024;
    const unsigned bb = lds0 + vl;
    auto body = [&](auto EARLY) {
      constexpr bool E = EARLY.v;
      f32x16 a0 = {}, a1 = {};
      bf16x8 f[3][2];
      const unsigned uo = (h * 64 + l31) * 16, us = WS_U + rb * (224 * 1024) + (c8 * 28 + wv * 4) * 1024;
      if (!E) { BAR(); BAR(); BAR(); }
      f[0][0] = lds128(XA5(0)); f[0][1] = lds128(XA5(0) + 8192); f[1][0] = lds128(XA5(1)); f[1][1] = lds128(XA5(1) + 8192);
      sfor<0, 32>([&](auto T) {
        constexpr int t = T.v, nl = t < 5 ? 0 : (t - 4) - (t - 4) / 4;
        if constexpr (E && (t == 6 || t == 14 || t == 22)) { waitv<(t == 6 ? 10 : t == 14 ? 6 : 0) + nl>(); BAR(); }
        SB();
        a0 = MFMA32(wf[KS5(t)], f[t % 3][0], a0);
        TOUCH(a0);
        if constexpr (t < 30) f[(t + 2) % 3][0] = lds128(XA5(t + 2));
        SB();
        a1 = MFMA32(wf[KS5(t)], f[t % 3][1], a1);
        TOUCH(a1);
        if constexpr (t < 30) f[(t + 2) % 3][1] = lds128(XA5(t + 2) + 8192);
        if constexpr (t >= 4 && (t & 3) != 3) W4LD(nl);
        SB();
      });
      STAMP(22);
      float mu0, mu1, v0, v1;
      lds_stats<8192>(lds0 + 65536 + h * 16384 + l31 * 16, mu0, v0); lds_stats<8192>(lds0 + 65536 + 1024 + h * 16384 + l31 * 16, mu1, v1);
      const float rs0 = __builtin_amdgcn_rsqf(v0 + EPS), rs1 = __builtin_amdgcn_rsqf(v1 + EPS);
      __builtin_amdgcn_s_setprio(0);
      sfor<0, 7>([&](auto K) { W4LD(21 + K.v); });
#pragma unroll
      for (int i = 0; i < 8; ++i) a0[i] = gelu((a0[i] - mu0 * cs[i]) * rs0);
      st128s(rws, uo, us, pack8(a0, 0));
#pragma unroll
      for (int i = 0; i < 8; ++i) a1[i] = gelu((a1[i] - mu1 * cs[i]) * rs1);
      st128s(rws, uo + 512, us, pack8(a1, 0));
#pragma unroll
      for (int i = 8; i < 16; ++i) a0[i] = gelu((a0[i] - mu0 * cs[i]) * rs0);
      st128s(rws, uo + 2048, us, pack8(a0, 8));
#pragma unroll
      for (int i = 8; i < 16; ++i) a1[i] = gelu((a1[i] - mu1 * cs[i]) * rs1);
      st128s(rws, uo + 2048 + 512, us, pack8(a1, 8));
      STAMP(23);
    };
    waitv<14>(); BAR();
    STAMP(31);
    unsigned ws2 = w;
    asm volatile("" : "+s"(ws2));
    if (w < 3 || w == 7) { __builtin_amdgcn_s_setprio(2); body(IC<1>{}); }
    if (ws2 - 4 < 3) { __builtin_amdgcn_s_setprio(1); body(IC<0>{}); }
    if (wsel == 3) {
      waitv<10>(); BAR(); waitv<6>(); BAR(); waitv<0>(); BAR();
      sfor<0, 28>([&](auto K) { W4LD(K.v); waitv<1>(); });
    }
    if (!early) BAR();
    l = lane();
    set_flag(flags + FLAG_U + (rb * 8 + c8) * 32 + (w == 3 ? 7 : wv), l);
    { const unsigned ro = (rb * 2 + (w >> 2)) * 32768 + (c8 * 8 + (w & 1) * 4 + ((w >> 1) & 1) * 2) * 512;
      rh = __builtin_amdgcn_raw_buffer_load_b128(rws, l * 16, WS_H + ro, 16); rx1 = __builtin_amdgcn_raw_buffer_load_b128(rws, l * 16, WS_X1 + ro, 16); }
    STAMP(24);
  }
  {
    const int l = lane(), h = l >> 5, l31 = l & 31;
    const unsigned vl = l * 16, vu = (h * 64 + l31) * 16, ub = WS_U + rb * (224 * 1024) + w * 28672;
    f32x16 ac[4] = {};
    bf16x8 f[3][4];
    auto ldu = [&](auto R, auto P) { sfor<0, 4>([&](auto J) { ldn<(J.v >> 1) * 2048 + (J.v & 1) * 512>(f[R.v][J.v], rws, vu, ub + P.v * 4096); }); };
    auto mm = [&](auto R, auto P) {
      sfor<0, 8>([&](auto J) { constexpr int ks = J.v >> 2, tt = (J.v >> 1) & 1, nt = J.v & 1; ac[tt * 2 + nt] = MFMA32(wf[KS5(nt * 14 + P.v * 2 + ks)], f[R.v][ks * 2 + tt], ac[tt * 2 + nt]); });
      TOUCH(ac[0]); TOUCH(ac[1]); TOUCH(ac[2]); TOUCH(ac[3]);
      SB();
    };
    const unsigned* fu = flags + FLAG_U + (rb * 8 + w) * 32;
    poll_flags(fu + ((l & 3) == 3 ? 6 : (l & 3)), true);
    drain();
    STAMP(26);
    ldu(IC<0>{}, IC<0>{}); ldu(IC<1>{}, IC<1>{}); ldu(IC<2>{}, IC<2>{});
    got4<8>(f[0]);
    STAMP(35);
    mm(IC<0>{}, IC<0>{});
    ldu(IC<0>{}, IC<6>{});
    got4<8>(f[1]); mm(IC<1>{}, IC<1>{});
    got4<4>(f[2]); mm(IC<2>{}, IC<2>{});
    got4<0>(f[0]); mm(IC<0>{}, IC<6>{});
    STAMP(36);
    if (early) BAR();
    poll_flags(fu + 3 + ((l & 3) == 3 ? 0 : (l & 3)), true);
    STAMP(37);
    ldu(IC<0>{}, IC<3>{}); ldu(IC<1>{}, IC<4>{}); ldu(IC<2>{}, IC<5>{});
    const unsigned pw = lds0 + w * 16384 + vl, me = (w >> 2) * 4 + (w & 1) * 2 + ((w >> 1) & 1);
    f32x4 s0 = {bflo(rh[0]) + bflo(rx1[0]), bfhi(rh[0]) + bfhi(rx1[0]), bflo(rh[1]) + bflo(rx1[1]), bfhi(rh[1]) + bfhi(rx1[1])};
    f32x4 s1 = {bflo(rh[2]) + bflo(rx1[2]), bfhi(rh[2]) + bfhi(rx1[2]), bflo(rh[3]) + bflo(rx1[3]), bfhi(rh[3]) + bfhi(rx1[3])};
    got4<8>(f[0]); got4<4>(f[1]); got4<0>(f[2]);
    STAMP(25);
    sfor<0, 4>([&](auto Q) {
      constexpr int tt = Q.v >> 1, nt = Q.v & 1;
      sfor<0, 6>([&](auto J) { constexpr int rr = J.v >> 1, ks = J.v & 1; ac[Q.v] = MFMA32(wf[KS5(nt * 14 + (3 + rr) * 2 + ks)], f[rr][ks * 2 + tt], ac[Q.v]); });
      TOUCH(ac[Q.v]);
      SB();
      if constexpr (Q.v > 0) {
        const f32x16& v = ac[Q.v - 1];
#pragma unroll
        for (int j = 0; j < 4; ++j) *(LAS f32x4*)(uintptr_t)(pw + (Q.v - 1) * 4096 + j * 1024) = (f32x4){v[4 * j], v[4 * j + 1], v[4 * j + 2], v[4 * j + 3]};
        SB();
      }
    });
    STAMP(27);
#pragma unroll
    for (int j = 0; j < 4; ++j) *(LAS f32x4*)(uintptr_t)(pw + 3 * 4096 + j * 1024) = (f32x4){ac[3][4 * j], ac[3][4 * j + 1], ac[3][4 * j + 2], ac[3][4 * j + 3]};
    BAR();
    { const unsigned a = lds0 + me * 2048 + vl;
#pragma unroll
      for (int k = 0; k < 8; ++k) { s0 += lds128f(a + (k & 3) * 16384 + (k >> 2) * 65536); s1 += lds128f(a + 1024 + (k & 3) * 16384 + (k >> 2) * 65536); } }
    const auto e0 = __builtin_amdgcn_permlane32_swap(pk2(s0[0], s0[1]), pk2(s1[0], s1[1]), false, false);
    const auto e1 = __builtin_amdgcn_permlane32_swap(pk2(s0[2], s0[3]), pk2(s1[2], s1[3]), false, false);
    *(u32x4*)(out + (rb * 64 + (w >> 2) * 32 + l31) * D + c8 * 64 + (w & 1) * 32 + ((w >> 1) & 1) * 16 + h * 8) = (u32x4){e0[0], e1[0], e0[1], e1[1]};
  }
  STAMPC(42); STAMP(28);
  { const int l = lane(); (void)l; STDUMP(1); }
}

extern "C" void kernel_launch(void* const* d_in, const int* in_sizes, int n_in, void* d_out, int out_size, void* d_ws, size_t ws_size, hipStream_t stream) {
  const u16* x = (const u16*)d_in[0]; const u16* wqkv = (const u16*)d_in[1]; const u16* wo = (const u16*)d_in[2];
  const u16* w1 = (const u16*)d_in[4]; const u16* w2 = (const u16*)d_in[5]; const u16* w3 = (const u16*)d_in[6]; const u16* w4 = (const u16*)d_in[7];
  (void)hipFuncSetAttribute((const void*)k1_qkv, hipFuncAttributeMaxDynamicSharedMemorySize, LDS_BYTES);
  (void)hipFuncSetAttribute((const void*)k2_main, hipFuncAttributeMaxDynamicSharedMemorySize, LDS_BYTES);
  k1_qkv<<<256, 512, LDS_BYTES, stream>>>(x, wqkv, wo, w1, w2, w3, w4, (char*)d_ws);
  k2_main<<<256, 512, LDS_BYTES, stream>>>(x, (u16*)d_out, (char*)d_ws);
}
```

```cpp
#include <hip/hip_runtime.h>
#include <stdint.h>

typedef unsigned short u16;
typedef __attribute__((ext_vector_type(8))) short bf16x8;
typedef __attribute__((ext_vector_type(4))) short s16x4;
typedef __attribute__((ext_vector_type(8))) __bf16 bf8_t;
typedef __attribute__((ext_vector_type(16))) __bf16 bf16_t;
typedef __attribute__((ext_vector_type(2))) __bf16 bf2_t;
typedef __attribute__((ext_vector_type(16))) float f32x16;
typedef __attribute__((ext_vector_type(8))) float f32x8;
typedef __attribute__((ext_vector_type(4))) float f32x4;
typedef __attribute__((ext_vector_type(2))) float f32x2;
typedef __attribute__((ext_vector_type(16))) unsigned u32x16;
typedef __attribute__((ext_vector_type(4))) unsigned u32x4;
typedef __attribute__((ext_vector_type(2))) unsigned u32x2;
typedef __amdgpu_buffer_rsrc_t rsrc_t;
#define LAS __attribute__((address_space(3)))
#define DI __device__ __forceinline__
#define RLX_AGENT __ATOMIC_RELAXED, __HIP_MEMORY_SCOPE_AGENT

constexpr int S = 2048, D = 512, F = 1792;
constexpr float EPS = 1e-5f;
constexpr unsigned WS_FLAG = 0;
constexpr unsigned WS_KMAX = 196608;
constexpr unsigned WS_STH  = 262144;
constexpr unsigned WS_STX  = WS_STH + 262144;
constexpr unsigned WS_Q    = 1u << 20;
constexpr unsigned WS_KV   = WS_Q + (2u << 20);
constexpr unsigned WS_O    = WS_KV + (4u << 20);
constexpr unsigned WS_H    = WS_O + (2u << 20);
constexpr unsigned WS_G    = WS_H + (2u << 20);
constexpr unsigned WS_X1   = WS_G + (2u << 20);
constexpr unsigned WS_U    = WS_X1 + (2u << 20);
constexpr unsigned WS_TW   = WS_U + 32u * 224 * 1024;
constexpr unsigned TW_O = WS_TW, TW_1 = TW_O + 524288, TW_2 = TW_1 + 524288, TW_3 = TW_2 + 524288, TW_4 = TW_3 + 1835008;
constexpr unsigned WS_END  = TW_4 + 1835008;
constexpr unsigned FLAG_O = 0, FLAG_H = 32768, FLAG_G = 65536, FLAG_X = 98304, FLAG_U = 131072, FLAG_F = 163840;
constexpr unsigned MARK = 0xff81ff81u;

constexpr int LDS_BYTES = 163840;
constexpr unsigned L_PB = 96 * 1024, L_Z = 144 * 1024, L_M = 156 * 1024;
constexpr unsigned C_P = L_M, C_R = L_M + 4, C_X = L_M + 8, C_S7 = L_M + 24, C_U7 = L_M + 28, C_5 = L_M + 32, C_ST = L_M + 36, C_H = L_M + 40, C_G = L_M + 44, C_CS = L_M + 48;
constexpr unsigned L_SLOW = L_M + 64, L_TH = L_M + 256, L_TX = L_M + 1280, L_CS = L_M + 2048;

#define STAMP(i)
#define STAMPC(i)
#define STDECL
#define KST(i)
#define KCNT()
#define KARG
#define KPASS
#define STDUMP(k)

DI rsrc_t mkrsrc(const void* p, unsigned bytes) { return __builtin_amdgcn_make_buffer_rsrc((void*)p, 0, bytes, 0x00020000); }
DI unsigned rfl(unsigned v) { return __builtin_amdgcn_readfirstlane(v); }
DI int lane() { int v; asm volatile("v_mbcnt_lo_u32_b32 %0, -1, 0\n\tv_mbcnt_hi_u32_b32 %0, -1, %0" : "=v"(v)); return v; }
#define MFMA32(a, b, c) __builtin_amdgcn_mfma_f32_32x32x16_bf16((a), (b), (c), 0, 0, 0)
#define MFMA16(a, b, c) __builtin_amdgcn_mfma_f32_16x16x32_bf16((a), (b), (c), 0, 0, 0)
#define BAR() asm volatile("s_waitcnt lgkmcnt(0)\n\ts_barrier" ::: "memory")
template <int N> DI void waitv() { asm volatile("s_waitcnt vmcnt(%0)" ::"i"(N) : "memory"); }
#define TOUCH(x) asm volatile("" : "+v"(x))
#define SB() __builtin_amdgcn_sched_barrier(0)
DI void drain() { waitv<0>(); __builtin_amdgcn_s_waitcnt(0x0F70); }

template <int OFF, bool SC1> DI void dma16(rsrc_t rs, unsigned voff, unsigned soff, unsigned lds) {
  if constexpr (SC1) asm volatile("s_mov_b32 m0, %0\n\ts_nop 0\n\tbuffer_load_dwordx4 %1, %2, %3 offen offset:%4 sc1 lds" ::"s"(lds), "v"(voff), "s"(rs), "s"(soff), "i"(OFF) : "memory");
  else asm volatile("s_mov_b32 m0, %0\n\ts_nop 0\n\tbuffer_load_dwordx4 %1, %2, %3 offen offset:%4 lds" ::"s"(lds), "v"(voff), "s"(rs), "s"(soff), "i"(OFF) : "memory");
}
template <int N> DI void dmaN(rsrc_t rs, unsigned voff, unsigned soff, unsigned lds) {
  if constexpr (N == 1) dma16<0, true>(rs, voff, soff, lds);
  else if constexpr (N == 2) asm volatile("s_mov_b32 m0, %0\n\ts_nop 0\n\tbuffer_load_dwordx4 %1, %2, %3 offen sc1 lds\n\tbuffer_load_dwordx4 %1, %2, %3 offen offset:1024 sc1 lds" ::"s"(lds), "v"(voff), "s"(rs), "s"(soff) : "memory");
  else if constexpr (N == 3) asm volatile("s_mov_b32 m0, %0\n\ts_nop 0\n\tbuffer_load_dwordx4 %1, %2, %3 offen sc1 lds\n\tbuffer_load_dwordx4 %1, %2, %3 offen offset:1024 sc1 lds\n\tbuffer_load_dwordx4 %1, %2, %3 offen offset:2048 sc1 lds" ::"s"(lds), "v"(voff), "s"(rs), "s"(soff) : "memory");
  else asm volatile("s_mov_b32 m0, %0\n\ts_nop 0\n\tbuffer_load_dwordx4 %1, %2, %3 offen sc1 lds\n\tbuffer_load_dwordx4 %1, %2, %3 offen offset:1024 sc1 lds\n\tbuffer_load_dwordx4 %1, %2, %3 offen offset:2048 sc1 lds\n\tbuffer_load_dwordx4 %1, %2, %3 offen offset:3072 sc1 lds" ::"s"(lds), "v"(voff), "s"(rs), "s"(soff) : "memory");
}
DI bf16x8 lds128(unsigned a) { return *(const LAS bf16x8*)(uintptr_t)a; }
DI f32x4 lds128f(unsigned a) { return *(const LAS f32x4*)(uintptr_t)a; }
DI s16x4 ldstr(unsigned a) { return __builtin_amdgcn_ds_read_tr16_b64_v4i16((LAS s16x4*)(uintptr_t)a); }
DI bf16x8 trfrag(unsigned a0, unsigned a1) { s16x4 lo = ldstr(a0), hi = ldstr(a1); return __builtin_shufflevector(lo, hi, 0, 1, 2, 3, 4, 5, 6, 7); }
DI unsigned pk2(float lo, float hi) { f32x2 v = {lo, hi}; bf2_t r = __builtin_convertvector(v, bf2_t); return __builtin_bit_cast(unsigned, r); }
DI float bflo(unsigned u) { return __uint_as_float(u << 16); }
DI float bfhi(unsigned u) { return __uint_as_float(u & 0xffff0000u); }
DI float swapadd(float v) { auto r = __builtin_amdgcn_permlane32_swap(__float_as_uint(v), __float_as_uint(v), false, false); return __uint_as_float(r[0]) + __uint_as_float(r[1]); }
DI float swapmax(float v) { auto r = __builtin_amdgcn_permlane32_swap(__float_as_uint(v), __float_as_uint(v), false, false); return fmaxf(__uint_as_float(r[0]), __uint_as_float(r[1])); }
DI float swap16add(float v) { auto r = __builtin_amdgcn_permlane16_swap(__float_as_uint(v), __float_as_uint(v), false, false); return __uint_as_float(r[0]) + __uint_as_float(r[1]); }
DI float swap16max(float v) { auto r = __builtin_amdgcn_permlane16_swap(__float_as_uint(v), __float_as_uint(v), false, false); return fmaxf(__uint_as_float(r[0]), __uint_as_float(r[1])); }
template <int CTRL> DI float dppf(float v) { return __uint_as_float(__builtin_amdgcn_update_dpp(0, __float_as_uint(v), CTRL, 0xf, 0xf, true)); }
DI float max16(float v) { v = fmaxf(v, dppf<0xB1>(v)); v = fmaxf(v, dppf<0x4E>(v)); v = fmaxf(v, dppf<0x141>(v)); return fmaxf(v, dppf<0x140>(v)); }
DI float sum8(float v) { v += dppf<0xB1>(v); v += dppf<0x4E>(v); return v + dppf<0x141>(v); }
DI float dot2(unsigned a, unsigned b, float c) { return __builtin_amdgcn_fdot2_f32_bf16(__builtin_bit_cast(bf2_t, a), __builtin_bit_cast(bf2_t, b), c, false); }
DI float gelu(float x) {
  const float t = x * __builtin_fmaf(x * x, -0.10294324f, -2.3022082f);
  return x * __builtin_amdgcn_rcpf(1.0f + __builtin_amdgcn_exp2f(t));
}
DI void st128(rsrc_t rs, unsigned off, u32x4 v) { __builtin_amdgcn_raw_buffer_store_b128(v, rs, off, 0, 16); }
DI void st128s(rsrc_t rs, unsigned voff, unsigned soff, u32x4 v) { __builtin_amdgcn_raw_buffer_store_b128(v, rs, voff, soff, 16); }
DI bf16x8 ld128(rsrc_t rs, unsigned voff, unsigned soff) { return __builtin_bit_cast(bf16x8, __builtin_amdgcn_raw_buffer_load_b128(rs, voff, soff, 16)); }
template <int OFF> DI void ldw(bf16x8& r, rsrc_t rs, unsigned voff, unsigned soff) {
  asm volatile("buffer_load_dwordx4 %0, %1, %2, %3 offen offset:%4 sc1" : "+v"(r) : "v"(voff), "s"(rs), "s"(soff), "i"(OFF) : "memory");
}
template <int OFF> DI void ldn(bf16x8& r, rsrc_t rs, unsigned voff, unsigned soff) {
  asm volatile("buffer_load_dwordx4 %0, %1, %2, %3 offen offset:%4 sc1" : "=v"(r) : "v"(voff), "s"(rs), "s"(soff), "i"(OFF) : "memory");
}
template <int N> DI void got4(bf16x8 (&f)[4]) { asm volatile("s_waitcnt vmcnt(%4)" : "+v"(f[0]), "+v"(f[1]), "+v"(f[2]), "+v"(f[3]) : "i"(N) : "memory"); }
template <int OFF> DI void ldsw(bf16x8& r, unsigned a) { asm volatile("ds_read_b128 %0, %1 offset:%2" : "+v"(r) : "v"(a), "i"(OFF) : "memory"); }
template <int V> struct IC { static constexpr int v = V; };
template <int B, int E, class Fn> DI void sfor(Fn&& fn) { if constexpr (B < E) { fn(IC<B>{}); sfor<B + 1, E>(fn); } }
DI u32x4 pack8(const f32x16& a, int o) { u32x4 p; p[0] = pk2(a[o], a[o + 1]); p[1] = pk2(a[o + 2], a[o + 3]); p[2] = pk2(a[o + 4], a[o + 5]); p[3] = pk2(a[o + 6], a[o + 7]); return p; }

DI bf16x8 split_op(float v, bool first, bool on) {
  const unsigned hi = __float_as_uint(v) & 0xffff0000u, lo = pk2(v - __uint_as_float(hi), 0.f);
  u32x4 o = {0u, 0u, 0u, 0u};
  if (first) { o[0] = hi | (hi >> 16); o[1] = lo | (lo << 16); } else { o[0] = (hi >> 16) | (lo << 16); o[1] = o[0]; }
  if (!on) { o[0] = 0u; o[1] = 0u; }
  return __builtin_bit_cast(bf16x8, o);
}

__global__ void __launch_bounds__(512, 2) k1_qkv(const u16* __restrict__ x, const u16* __restrict__ wqkv, const u16* __restrict__ wo, const u16* __restrict__ w1,
                                                 const u16* __restrict__ w2, const u16* __restrict__ w3, const u16* __restrict__ w4, char* __restrict__ ws) {
  extern __shared__ __attribute__((aligned(16))) char smem[];
  const unsigned lds0 = (unsigned)(uintptr_t)smem;
  const int b = blockIdx.x, l = lane();
  const unsigned w = rfl(threadIdx.x >> 6);
  const unsigned xcd = b & 7, r = b >> 3;
  const unsigned rb = (xcd >> 1) * 8 + (r >> 2), cb = (xcd & 1) * 4 + (r & 3);
  constexpr unsigned SLOT = 32768, LC = 4 * SLOT, LT = LC + 20480;
  STDECL; STAMP(0); STAMPC(40);
  const rsrc_t rx = mkrsrc(x, S * D * 2), rw = mkrsrc(wqkv, D * 1536 * 2), rws = mkrsrc(ws, WS_END);
  const int h = l >> 5, l31 = l & 31, G = (l >> 4) & 1, q = (l >> 2) & 3, p4 = l & 3;
  const unsigned hw = w & 3;
  const unsigned vx0 = (l >> 3) * 1024 + (((l & 7) ^ ((l >> 3) & 6)) << 4), vx1 = (vx0 ^ 16) + 8192;
  const unsigned vsw = ((l & 7) ^ (G << 2)) << 4, vw0 = (l >> 3) * 3072 + vsw, vw1 = vw0 + 8 * 3072;
  auto issue = [&](int c) {
    const unsigned sx = (rb * 64 + hw * 16) * 1024 + c * 128, sw = (c * 64 + hw * 16) * 3072 + cb * 128, lw = lds0 + (c & 3) * SLOT + hw * 2048;
    dma16<0, false>(rx, vx0, sx, lw);
    dma16<0, false>(rw, vw0, sw, lw + 8192); dma16<0, false>(rw, vw1, sw, lw + 8192 + 1024);
    dma16<0, false>(rx, vx1, sx, lw + 1024);
    dma16<0, false>(rw, vw0, sw + 1024, lw + 16384); dma16<0, false>(rw, vw1, sw + 1024, lw + 16384 + 1024);
    dma16<0, false>(rw, vw0, sw + 2048, lw + 24576); dma16<0, false>(rw, vw1, sw + 2048, lw + 24576 + 1024);
  };
  if (w >= 4) issue(0);
  asm volatile("s_barrier" ::: "memory");
  if (w >= 4) {
    issue(1);
    STAMP(1);
    const unsigned bit = hw >> 1, hf = hw & 1, big3 = b < 128;
    const unsigned bp = big3 ? F * 2 : 1024, bso = big3 ? (b >> 2) * 16 * (F * 2) + (b & 3) * 7 * 128 : ((b - 128) >> 3) * 7 * 16384 + (b & 7) * 128, bst = big3 ? 128 : 16384;
    const unsigned bdo = big3 ? TW_3 + (((b & 3) * 14 + hf) * 32 + (b >> 2)) * 1024 : TW_4 + (((b & 7) * 2 + hf) * 112 + ((b - 128) >> 3) * 7) * 1024, bdt = big3 ? 65536 : 1024;
    const unsigned sso = ((b >> 3) * 16 + hf * 8) * 1024 + (b & 7) * 128, sdo = (((b & 7) * 2 + hf) * 32 + (b >> 3)) * 1024;
    const rsrc_t rbig = mkrsrc(big3 ? w3 : w4, D * F * 2);
    const unsigned vs = (l >> 3) * 1024 + vsw, vb = (l >> 3) * bp + vsw;
    const rsrc_t rp0 = mkrsrc(bit ? w1 : wo, D * D * 2), rp1 = mkrsrc(bit ? (big3 ? w3 : w4) : w2, bit ? D * F * 2 : D * D * 2);
    const unsigned vp1 = bit ? vb : vs, bsh = bso + hf * 8 * bp, sp1 = bit ? bsh : sso, dp1 = bit ? bdo : TW_2 + sdo;
    auto piece = [&](int i) {
      const unsigned lc = lds0 + LC + (2 * i + bit) * 2048 + hf * 1024;
      if (i == 0) dma16<0, false>(rp0, vs, sso, lc);
      else if (i == 1) dma16<0, false>(rp1, vp1, sp1, lc);
      else dma16<0, false>(rbig, vb, bsh + (2 * i - 3 + bit) * bst, lc);
    };
    float s0 = 0.f, s1 = 0.f, q0 = 0.f, q1 = 0.f;
    const unsigned xs = lds0 + hw * 2048 + l * 16;
#pragma unroll
    for (int c = 0; c < 8; ++c) {
      if (c == 0) waitv<8>(); else if (c == 1) waitv<17>(); else if (c < 6) waitv<18>(); else if (c == 6) waitv<9>(); else waitv<0>();
      BAR();
      STAMP(4 + c);
      const u32x4 a0 = __builtin_bit_cast(u32x4, lds128(xs + (c & 3) * SLOT)), a1 = __builtin_bit_cast(u32x4, lds128(xs + 1024 + (c & 3) * SLOT));
      if (c == 0) issue(2);
      if (c < 5) { issue(c + 3); piece(c); }
#pragma unroll
      for (int j = 0; j < 4; ++j) { s0 = dot2(a0[j], 0x3f803f80u, s0); q0 = dot2(a0[j], a0[j], q0); s1 = dot2(a1[j], 0x3f803f80u, s1); q1 = dot2(a1[j], a1[j], q1); }
    }
    const float m0_ = sum8(s0) * (1.f / D), m1_ = sum8(s1) * (1.f / D);
    const float r0 = __builtin_amdgcn_rsqf(fmaxf(sum8(q0) * (1.f / D) - m0_ * m0_, 0.f) + EPS), r1 = __builtin_amdgcn_rsqf(fmaxf(sum8(q1) * (1.f / D) - m1_ * m1_, 0.f) + EPS);
    if ((l & 7) == 0) {
      LAS float* t = (LAS float*)(uintptr_t)(lds0 + LT + (hw * 16 + (l >> 3)) * 4);
      t[0] = m0_; t[8] = m1_; t[64] = r0; t[72] = r1;
    }
    BAR();
    const unsigned ta = lds0 + LC + bit * 2048 + (4 * h + q) * 128 + (((4 * hf + 2 * G + (p4 >> 1)) ^ ((q >> 1) << 2)) << 4) + 8 * (p4 & 1);
#pragma unroll
    for (int i = 0; i < 5; ++i) {
      const unsigned dst = i == 0 ? TW_O + bit * 524288 + sdo : i == 1 ? dp1 : bdo + (2 * i - 3 + bit) * bdt;
      st128s(rws, l * 16, dst, __builtin_bit_cast(u32x4, trfrag(ta + i * 4096, ta + i * 4096 + 1024)));
    }
    STAMP(2);
    if (w == 4) { unsigned* fz = (unsigned*)(ws + WS_FLAG) + b * 161; fz[l] = 0u; fz[64 + l] = 0u; if (l < 33) fz[128 + l] = 0u; }
  } else {
    const unsigned tt = w & 1, hp = w >> 1;
    unsigned wm0, wm1;
    if (w < 3) {
      const unsigned vo = (l >> 1) * 3072 + (l & 1) * 64, so = (128 + (r >> 2) * 48) * 3072 + w * 1024 + cb * 128;
      asm volatile("buffer_load_dword %0, %2, %3, %4 offen\n\tbuffer_load_dword %1, %2, %3, %5 offen" : "=&v"(wm0), "=&v"(wm1) : "v"(vo), "s"(rw), "s"(so), "s"(so + 16 * 3072) : "memory");
    } else {
      const unsigned vo = (l >> 2) * 1024 + (l & 3) * 64, so = (rb * 64 + (r & 3) * 16) * 1024;
      asm volatile("buffer_load_dword %0, %2, %3, %4 offen offset:256\n\tbuffer_load_dword %1, %2, %3, %4 offen offset:512\n\tbuffer_load_dword %0, %2, %3, %4 offen offset:768"
                   : "=&v"(wm0), "=&v"(wm1) : "v"(vo), "s"(rx), "s"(so) : "memory");
    }
    STAMP(1);
    unsigned xa[2][4];
#pragma unroll
    for (int s = 0; s < 4; ++s) {
      xa[0][s] = lds0 + (tt * 4 + (l31 >> 3)) * 1024 + (l31 & 7) * 128 + (((2 * s + h) ^ ((l31 & 6) | ((l31 >> 3) & 1))) << 4); xa[1][s] = xa[0][s] + 2 * SLOT;
      TOUCH(xa[0][s]); TOUCH(xa[1][s]);
    }
    unsigned wa0 = lds0 + 8192 + (8 * h + q) * 128 + (((4 * hp + 2 * G + (p4 >> 1)) ^ ((q >> 1) << 2)) << 4) + 8 * (p4 & 1), wa1 = wa0 + 2 * SLOT;
    TOUCH(wa0); TOUCH(wa1);
    f32x16 aq = {}, ak = {}, av = {};
    float cq = 0.f, ck = 0.f, cv = 0.f, dq = 0.f, dk = 0.f, dv = 0.f;
    auto body = [&](auto TT) {
      bf16x8 f[2][4];
      auto wadr = [&](int s, int p) { return ((s & 8) ? wa1 : wa0) + ((s >> 2) & 1) * SLOT + p * 8192 + (s & 3) * 2048; };
      auto xadr = [&](int s) { return xa[(s >> 3) & 1][s & 3] + ((s >> 2) & 1) * SLOT; };
      auto cs2 = [&](const bf16x8& v, float& c, float& d) {
        const u32x4 u = __builtin_bit_cast(u32x4, v); c = dot2(u[2 * TT.v], 0x3f803f80u, c); d = dot2(u[2 * TT.v + 1], 0x3f803f80u, d); };
      BAR();
      STAMP(4);
      f[0][0] = lds128(xadr(0)); f[0][1] = trfrag(wadr(0, 0), wadr(0, 0) + 512); f[0][2] = trfrag(wadr(0, 1), wadr(0, 1) + 512); f[0][3] = trfrag(wadr(0, 2), wadr(0, 2) + 512);
#pragma unroll
      for (int s = 0; s < 32; ++s) {
        const bf16x8* o = f[s & 1];
        bf16x8* n = f[(s + 1) & 1];
        if ((s & 3) == 3 && s < 31) { BAR(); STAMP(5 + (s >> 2)); }
        SB();
        aq = MFMA32(o[1], o[0], aq); TOUCH(aq);
        if (s < 31) { n[0] = lds128(xadr(s + 1)); n[1] = trfrag(wadr(s + 1, 0), wadr(s + 1, 0) + 512); }
        cs2(o[1], cq, dq);
        SB();
        ak = MFMA32(o[2], o[0], ak); TOUCH(ak);
        if (s < 31) n[2] = trfrag(wadr(s + 1, 1), wadr(s + 1, 1) + 512);
        cs2(o[2], ck, dk);
        SB();
        av = MFMA32(o[0], o[3], av); TOUCH(av);
        if (s < 31) n[3] = trfrag(wadr(s + 1, 2), wadr(s + 1, 2) + 512);
        cs2(o[3], cv, dv);
        SB();
      }
    };
    if (tt) body(IC<1>{}); else body(IC<0>{});
    asm volatile("s_waitcnt vmcnt(0)" :: "v"(wm0), "v"(wm1) : "memory");
    STAMP(12);
    cq = swapadd(cq + dq); ck = swapadd(ck + dk); cv = swapadd(cv + dv);
    LAS float* ct = (LAS float*)(uintptr_t)(lds0 + LT + 512 + l31 * 4);
    if (h == 0) { ct[w * 96] = cq; ct[w * 96 + 32] = ck; ct[w * 96 + 64] = cv; }
    BAR();
    cq += ct[(w ^ 1) * 96]; ck += ct[(w ^ 1) * 96 + 32]; cv += ct[(w ^ 1) * 96 + 64];
    const LAS float* st = (const LAS float*)(uintptr_t)(lds0 + LT + tt * 128);
    const float mu = st[l31], rs = st[64 + l31];
    f32x4 rv[4];
#pragma unroll
    for (int j = 0; j < 4; ++j) rv[j] = *(const LAS f32x4*)(st + 64 + 8 * j + 4 * h);
    const bf16x8 mo = split_op(-mu, false, true);
    aq = MFMA32(split_op(cq, true, h == 0), mo, aq); ak = MFMA32(split_op(ck, true, h == 0), mo, ak); av = MFMA32(mo, split_op(cv, true, h == 0), av);
    const float qs = rs * 0.36067376f;
    float n0 = 0.f, n1 = 0.f;
#pragma unroll
    for (int i = 0; i < 16; ++i) { aq[i] *= qs; ak[i] *= rs; av[i] *= rv[i >> 2][i & 3]; }
    const unsigned head = cb * 4 + hp * 2, t32 = rb * 2 + tt;
    const unsigned vq = (h * 2048 + l31) * 16, sq = WS_Q + head * 65536 + t32 * 512;
    const unsigned vk = h * 512 + ((l31 & 19) | ((l31 & 4) << 1) | ((l31 & 8) >> 1)) * 16, sk = WS_KV + head * 131072 + t32 * 2048;
    const unsigned vv = G * 131072 + 1024 + h * 512 + (l & 15) * 16;
    st128s(rws, vk, sk, pack8(ak, 0)); st128s(rws, vk, sk + 131072, pack8(ak, 8));
    st128s(rws, vv, sk, pack8(av, 0)); st128s(rws, vv + 256, sk, pack8(av, 8));
    st128s(rws, vq, sq, pack8(aq, 0)); st128s(rws, vq, sq + 65536, pack8(aq, 8));
#pragma unroll
    for (int i = 0; i < 8; ++i) { n0 += ak[i] * ak[i]; n1 += ak[8 + i] * ak[8 + i]; }
    n0 = swap16max(max16(swapadd(n0))); n1 = swap16max(max16(swapadd(n1)));
    if (l == 0) { float* km = (float*)(ws + WS_KMAX) + head * 64 + t32; km[0] = n0; km[64] = n1; }
  }
  STAMP(3); STAMPC(41); STDUMP(0);
}

constexpr unsigned SPIN = 1u << 20;
DI void hint(rsrc_t rs, unsigned off) {
  asm volatile("s_mov_b64 exec, 1\n\tbuffer_atomic_add %0, %1, %2, 0 offen sc1\n\ts_mov_b64 exec, -1" ::"v"(1u), "v"(off), "s"(rs) : "memory");
}
DI void poll(rsrc_t rs, unsigned off, bool on, unsigned need) {
#pragma unroll 1
  for (unsigned sp = 0; sp < SPIN; ++sp) {
    unsigned v = need;
    if (on) { v = 0u; asm volatile("buffer_atomic_add %0, %1, %2, 0 offen sc0 sc1\n\ts_waitcnt vmcnt(0)" : "+v"(v) : "v"(off), "s"(rs) : "memory"); }
    if (__all(v >= need)) break;
    __builtin_amdgcn_s_sleep(1);
  }
}
DI void ladd(unsigned a) { asm volatile("s_mov_b64 exec, 1\n\tds_add_u32 %0, %1\n\ts_mov_b64 exec, -1" ::"v"(a), "v"(1u) : "memory"); }
DI unsigned lcnt(unsigned a) { return *(const volatile LAS unsigned*)(uintptr_t)a; }
DI void lwait(unsigned a, unsigned need) {
#pragma unroll 1
  for (unsigned sp = 0; sp < SPIN && rfl(lcnt(a)) < need; ++sp) __builtin_amdgcn_s_sleep(1);
  asm volatile("" ::: "memory");
}
DI unsigned mx4(unsigned m, const bf16x8& f) { const u32x4 u = __builtin_bit_cast(u32x4, f); return max(max(max(max(m, u[0]), u[1]), u[2]), u[3]); }
template <int N> DI void got1(bf16x8& f) { asm volatile("s_waitcnt vmcnt(%1)" : "+v"(f) : "i"(N) : "memory"); }
DI void fin_stats(const bf16x8 (&t)[4], float& mu, float& var) {
  float s = 0.f, q = 0.f;
#pragma unroll
  for (int i = 0; i < 4; ++i) { const f32x4 v = __builtin_bit_cast(f32x4, t[i]); s += v[0] + v[2]; q += v[1] + v[3]; }
  s = swapadd(s); q = swapadd(q);
  mu = s * (1.f / D); var = fmaxf(q * (1.f / D) - mu * mu, 0.f);
  TOUCH(mu);
}
DI void ld_stats(bf16x8 (&t)[4], rsrc_t rs, unsigned vs, unsigned so) { ldn<0>(t[0], rs, vs, so); ldn<512>(t[1], rs, vs, so); ldn<1024>(t[2], rs, vs, so); ldn<1536>(t[3], rs, vs, so); }
template <int N, int O0, int O1, int O2, int O3> DI void take4(bf16x8 (&t)[4], rsrc_t rs, unsigned voff, unsigned sa, unsigned sb) {
  got4<N>(t);
#pragma unroll 1
  for (unsigned sp = 0; sp < SPIN && __any(mx4(mx4(mx4(mx4(0u, t[0]), t[1]), t[2]), t[3]) == MARK); ++sp)
    asm volatile("buffer_load_dwordx4 %0, %4, %5, %6 offen offset:%8 sc1\n\tbuffer_load_dwordx4 %1, %4, %5, %6 offen offset:%9 sc1\n\t"
                 "buffer_load_dwordx4 %2, %4, %5, %7 offen offset:%10 sc1\n\tbuffer_load_dwordx4 %3, %4, %5, %7 offen offset:%11 sc1\n\ts_waitcnt vmcnt(0)"
                 : "=&v"(t[0]), "=&v"(t[1]), "=&v"(t[2]), "=&v"(t[3]) : "v"(voff), "s"(rs), "s"(sa), "s"(sb), "i"(O0), "i"(O1), "i"(O2), "i"(O3) : "memory");
}
DI void get_stats(bf16x8 (&t)[4], rsrc_t rs, unsigned vs, unsigned so) { take4<0, 0, 512, 1024, 1536>(t, rs, vs, so, so); }
DI void put_stats(rsrc_t rs, unsigned vl, unsigned soff, const f32x16& v) {
  f32x2 s = {v[0], v[1]}, q = s * s;
#pragma unroll
  for (int i = 2; i < 16; i += 2) { const f32x2 t = {v[i], v[i + 1]}; s += t; q += t * t; }
  float sa = s[0], sb = s[1], qa = q[0], qb = q[1];
  TOUCH(sa); TOUCH(qa);
  const float s1 = swapadd(sa + sb), q1 = swapadd(qa + qb);
  if (vl < 512) __builtin_amdgcn_raw_buffer_store_b64((u32x2){__float_as_uint(s1), __float_as_uint(q1)}, rs, vl, soff, 16);
}
template <int B, int N> DI float colsum(const bf16x8 (&wf)[32], int l) {
  const unsigned o = ((0x00100001u >> (l & 31)) & 1u) * 0x3f803f80u;
  u32x4 sel = {o, o, o, o};
  TOUCH(sel);
  f32x4 a = {}, c = {};
#pragma unroll
  for (int k = 0; k < N; k += 2) { a = MFMA16(__builtin_bit_cast(bf16x8, sel), wf[B + k], a); c = MFMA16(__builtin_bit_cast(bf16x8, sel), wf[B + k + 1], c); }
  return a[0] + c[0];
}
template <int ST, int EXTRA, class F1, class F2, class F3>
DI f32x16 kstage(const bf16x8 (&wf)[32], rsrc_t rs, unsigned voff, unsigned soff, unsigned lds0, unsigned wi, int l, F1&& mid1, F2&& mid2, F3&& last KARG) {
  f32x16 ac[4];
  bf16x8 f[8];
  const f32x16 zero = {};
  unsigned rd = ST ? lcnt(lds0 + C_R) : 0u;
#pragma unroll 1
  for (unsigned sp = 0; sp < SPIN; ++sp) {
    sfor<0, 8>([&](auto K) { ldn<(K.v & 3) * 1024>(f[K.v], rs, voff, soff + (K.v >> 2) * 4096); });
    unsigned m = 0u;
    sfor<0, 8>([&](auto K) {
      constexpr int k = K.v;
      got1<7 - k + (k ? EXTRA : 0)>(f[k]);
      if constexpr (k == 0) { mid1(); KST(0); }
      SB();
#pragma unroll
      for (int j = 0; j < 3; ++j) ac[j] = MFMA32(wf[8 * j + k], f[k], k ? ac[j] : zero);
      m = mx4(m, f[k]);
      TOUCH(ac[0]); TOUCH(ac[1]); TOUCH(ac[2]);
      SB();
    });
    if (!__any(m == MARK)) break;
    KCNT();
  }
  KST(1);
  mid2();
  if (ST && rfl(rd) < 4 * ST) lwait(lds0 + C_R, 4 * ST);
  const unsigned pw = lds0 + L_PB + l * 16, pr = pw + wi * 12288;
  auto put = [&](int j) {
    const unsigned a = pw + (((wi + 1 + j) & 3) * 3 + j) * 4096;
#pragma unroll
    for (int i = 0; i < 4; ++i) *(LAS f32x4*)(uintptr_t)(a + i * 1024) = (f32x4){ac[j][4 * i], ac[j][4 * i + 1], ac[j][4 * i + 2], ac[j][4 * i + 3]};
  };
  SB();
  ac[3] = MFMA32(wf[24], f[0], zero); TOUCH(ac[3]);
  ac[3] = MFMA32(wf[25], f[1], ac[3]); TOUCH(ac[3]);
  SB(); put(0); SB();
  ac[3] = MFMA32(wf[26], f[2], ac[3]); TOUCH(ac[3]);
  SB(); put(1); SB();
  ac[3] = MFMA32(wf[27], f[3], ac[3]); TOUCH(ac[3]);
  SB(); put(2); ladd(lds0 + C_P); SB();
  ac[3] = MFMA32(wf[28], f[4], ac[3]); TOUCH(ac[3]);
  ac[3] = MFMA32(wf[29], f[5], ac[3]); TOUCH(ac[3]);
  SB();
  f32x4 p[12];
  unsigned c = lcnt(lds0 + C_P);
  asm volatile("" ::: "memory");
#pragma unroll
  for (int i = 0; i < 12; ++i) p[i] = lds128f(pr + i * 1024);
  SB();
  ac[3] = MFMA32(wf[30], f[6], ac[3]); TOUCH(ac[3]);
  ac[3] = MFMA32(wf[31], f[7], ac[3]); TOUCH(ac[3]);
  last(ac[3]);
  SB();
  KST(2);
  if (rfl(c) < 4 * (ST + 1)) {
    lwait(lds0 + C_P, 4 * (ST + 1));
#pragma unroll
    for (int i = 0; i < 12; ++i) p[i] = lds128f(pr + i * 1024);
  }
  KST(3);
#pragma unroll
  for (int i = 0; i < 4; ++i) p[i] += p[4 + i] + p[8 + i];
  TOUCH(p[0]); TOUCH(p[1]); TOUCH(p[2]); TOUCH(p[3]);
  ladd(lds0 + C_R);
#pragma unroll
  for (int i = 0; i < 16; ++i) ac[3][i] += p[i >> 2][i & 3];
  return ac[3];
}
DI void tile_dma(rsrc_t rs, unsigned voff, unsigned soff, unsigned lds) { dmaN<4>(rs, voff, soff, lds); dmaN<4>(rs, voff, soff + 4096, lds + 4096); }
DI u32x4 gelu8(const f32x16& a, int o, float k1, float k2, float ir) {
  float t0, t1, t2, t3, t4, t5, t6, t7;
#define G8(OP) OP(0, 8) OP(1, 9) OP(2, 10) OP(3, 11) OP(4, 12) OP(5, 13) OP(6, 14) OP(7, 15)
#define G_A(t, x) "v_mul_f32 %" #t ", %" #x ", %" #x "\n\t"
#define G_B(t, x) "v_fma_f32 %" #t ", %" #t ", %16, %17\n\t"
#define G_C(t, x) "v_mul_f32 %" #t ", %" #t ", %" #x "\n\t"
#define G_D(t, x) "v_exp_f32 %" #t ", %" #t "\n\t"
#define G_E(t, x) "v_fma_f32 %" #t ", %" #t ", %18, %18\n\t"
#define G_F(t, x) "v_rcp_f32 %" #t ", %" #t "\n\t"
  asm volatile(G8(G_A) G8(G_B) G8(G_C) G8(G_D) G8(G_E) G8(G_F) G8(G_C)
               "v_cvt_pk_bf16_f32 %0, %0, %1\n\tv_cvt_pk_bf16_f32 %1, %2, %3\n\tv_cvt_pk_bf16_f32 %2, %4, %5\n\tv_cvt_pk_bf16_f32 %3, %6, %7"
               : "=&v"(t0), "=&v"(t1), "=&v"(t2), "=&v"(t3), "=&v"(t4), "=&v"(t5), "=&v"(t6), "=&v"(t7)
               : "v"(a[o]), "v"(a[o + 1]), "v"(a[o + 2]), "v"(a[o + 3]), "v"(a[o + 4]), "v"(a[o + 5]), "v"(a[o + 6]), "v"(a[o + 7]), "v"(k1), "v"(k2), "v"(ir));
  return (u32x4){__float_as_uint(t0), __float_as_uint(t1), __float_as_uint(t2), __float_as_uint(t3)};
}
constexpr float GC1 = -0.10294324f, GC2 = -2.3022082f;

#define EX(B, i) "v_exp_f32 v[" #B "+" #i "], v[" #B "+" #i "]\n\t"
#define TILE(C, P, KC, KP, LC, LP, HC, HP, U, X0, X1)                                                 \
  EX(C, 0) EX(C, 1)                                                                                   \
  "v_smfmac_f32_32x32x32_bf16 v[2:17], v[" #HP ":" #HP "+3], v[" #P "+8:" #P "+15], v78\n\t"          \
  EX(C, 2) EX(C, 3) X0 EX(C, 4) EX(C, 5)                                                              \
  "v_mfma_f32_32x32x16_bf16 v[" #P ":" #P "+15], v[" #KP ":" #KP "+3], v[50:53], 0\n\t"               \
  "ds_read_b128 v[" #KC ":" #KC "+3], v79 offset:" #U "*2048+4096\n\t"                                \
  "s_mov_b64 exec, %[msk]\n\t"                                                                        \
  "ds_read_b128 v[" #LP ":" #LP "+3], v79 offset:" #U "*2048+3072\n\t"                                \
  "ds_read_b128 v[" #HC ":" #HC "+3], v79 offset:" #U "*2048+1280\n\t"                                \
  "s_mov_b64 exec, -1\n\t"                                                                            \
  EX(C, 6) EX(C, 7) EX(C, 8) EX(C, 9)                                                                 \
  "v_smfmac_f32_32x32x32_bf16 v[2:17], v[" #LC ":" #LC "+3], v[" #C ":" #C "+7], v78\n\t"             \
  EX(C, 10) EX(C, 11) EX(C, 12) X1 EX(C, 13) EX(C, 14) EX(C, 15)                                      \
  "s_waitcnt lgkmcnt(0)\n\t"
#define TILE4(A0, A1, B0, B1, C0, C1, D0, D1)                                                         \
  TILE(18, 34, 54, 58, 62, 66, 70, 74, 0, A0, A1) TILE(34, 18, 58, 54, 66, 62, 74, 70, 1, B0, B1)     \
  TILE(18, 34, 54, 58, 62, 66, 70, 74, 2, C0, C1) TILE(34, 18, 58, 54, 66, 62, 74, 70, 3, D0, D1) "v_add_u32 v79, 0x2000, v79"
#define ATT_OPS "+{v[2:17]}"(oacc), "+{v[18:33]}"(s0), "+{v[34:49]}"(s1), "+{v[54:57]}"(k0), "+{v[58:61]}"(k1), "+{v[62:65]}"(l0), "+{v[66:69]}"(l1), \
                "+{v[70:73]}"(h0), "+{v[74:77]}"(h1), "+{v79}"(ad)
#define ATT_IN "{v[50:53]}"(qf), "{v78}"(idx), [msk] "s"(msk)
#define WARM "buffer_load_dword v80, %[vl], %[rs], %[sw] offen\n\t"
#define WL(B, i) "buffer_load_dwordx4 v[" #B "+4*" #i ":" #B "+4*" #i "+3], %[vl], %[rs], %[sa] offen offset:" #i "*1024 sc1\n\t"
#define DL(i) "buffer_load_dwordx4 %[vl], %[rs], %[sa] offen offset:" #i "*1024 sc1 lds\n\t"
#define FS(i) "buffer_store_dwordx4 %[mk], %[vl], %[rs], %[f" #i "] offen sc1\n\t"
#define ATT_LD [vl] "v"(vl), [rs] "s"(rws)
#define ATT_W(B, E, n, g) asm volatile(TILE4(WARM, WL(B, 0), "", WL(B, 1), "", WL(B, 2), "", WL(B, 3)) \
    : ATT_OPS, "+{v80}"(dum), "={v[" #B ":" #E "]}"(wt[n]) : ATT_IN, ATT_LD, [sa] "s"(wso(n)), [sw] "s"(wrm + g * 8192) : "memory")
#define ATT_D(src, dst, g) asm volatile("s_mov_b32 m0, %[ld]\n\t" TILE4(WARM, DL(0), "", DL(1), "", DL(2), "", DL(3)) \
    : ATT_OPS, "+{v80}"(dum) : ATT_IN, ATT_LD, [sa] "s"(src), [ld] "s"(dst), [sw] "s"(wrm + g * 8192) : "memory")
#define ATT_W0(B, E, n) asm volatile(TILE4("", WL(B, 0), "", WL(B, 1), "", WL(B, 2), "", WL(B, 3)) \
    : ATT_OPS, "={v[" #B ":" #E "]}"(wt[n]) : ATT_IN, ATT_LD, [sa] "s"(wso(n)) : "memory")
#define ATT_D0(src, dst) asm volatile("s_mov_b32 m0, %[ld]\n\t" TILE4("", DL(0), "", DL(1), "", DL(2), "", DL(3)) : ATT_OPS : ATT_IN, ATT_LD, [sa] "s"(src), [ld] "s"(dst) : "memory")
#define ATT_FILL(a, b, c) [mk] "v"(mk), [f0] "s"(a), [f1] "s"(b), [f2] "s"(c)

__global__ void __launch_bounds__(512, 2) k2_main(const u16* __restrict__ x, u16* __restrict__ out, char* __restrict__ ws) {
  extern __shared__ __attribute__((aligned(16))) char smem[];
  const unsigned lds0 = (unsigned)(uintptr_t)smem;
  const int b = blockIdx.x;
  const unsigned w = rfl(threadIdx.x >> 6);
  const unsigned xcd = b & 7, r = b >> 3;
  const rsrc_t rws = mkrsrc(ws, WS_END);
  const unsigned rb = (xcd >> 1) * 8 + (r >> 2), c8 = (xcd & 1) * 4 + (r & 3);
  const unsigned rq = rb * 2 + ((r >> 1) & 1), c4 = (xcd & 1) * 2 + (r & 1);
  const unsigned wi = w & 3;
  const unsigned r2a = lds0 + wi * 16384, r2b = lds0 + 65536 + wi * 8192, r2c = lds0 + L_Z + wi * 3072;
  auto kq = [&](unsigned base, int j) { return base + ((c4 * 4 + ((wi + 1 + j) & 3)) * 32 + wi * 8) * 1024; };
  bf16x8 wf[32];
  u32x16 wt[8];
  u32x2 xr[4];
  STDECL; STAMP(0);
  {
    const int l = lane(), h = l >> 5, l31 = l & 31;
    const unsigned vl = l * 16;
    const unsigned head = xcd * 4 + (r & 3), qb = r >> 2;
    const unsigned tok = qb * 256 + w * 32 + l31;
    bf16x8 qf; float km;
    asm volatile("buffer_load_dwordx4 %0, %1, %2, %3 offen" : "={v[50:53]}"(qf) : "v"((h * 2048 + tok) * 16), "s"(rws), "s"(WS_Q + head * 65536) : "memory");
    asm volatile("buffer_load_dword %0, %1, %2, %3 offen" : "=v"(km) : "v"(l * 4), "s"(rws), "s"(WS_KMAX + head * 256) : "memory");
    const unsigned kv = WS_KV + head * 131072;
    dma16<0, true>(rws, vl, kv + w * 1024, lds0 + w * 1024); dma16<0, true>(rws, vl, kv + 8192 + w * 1024, lds0 + 8192 + w * 1024);
    dmaN<4>(rws, vl, kv + 16384 + w * 4096, lds0 + 16384 + w * 4096);
    if (w == 0 && l < 16) *(LAS unsigned*)(uintptr_t)(lds0 + L_M + l * 4) = 0u;
    asm volatile("s_waitcnt vmcnt(6)" : "+{v[50:53]}"(qf), "+v"(km) :: "memory");
    km = swapmax(swap16max(max16(km)));
    float qn = 0.f;
    { const u32x4 u = __builtin_bit_cast(u32x4, qf);
#pragma unroll
      for (int j = 0; j < 4; ++j) qn = dot2(u[j], u[j], qn); }
    qn = swapadd(qn);
    { const unsigned slowf = __all(qn * km * 1.05f <= 48.f * 48.f) ? 0u : 1u;
      if (l == 0) *(LAS unsigned*)(uintptr_t)(lds0 + L_SLOW + w * 4) = slowf; }
    const unsigned cst = l31 == 16 ? 0x3f803f80u : 0u;
    const bf16x8 vc = __builtin_bit_cast(bf16x8, (u32x4){cst, cst, cst, cst});
    f32x16 oacc = {};
    int idx = 0xDDDD;
    unsigned ad = lds0 + h * 512 + l31 * 16;
    const unsigned wb = w < 4 ? TW_O : TW_1, hw = w >> 2;
    auto wso = [&](int n) { return kq(wb, n >> 1) + (n & 1) * 4096; };
    const unsigned wrm = (r < 16 ? TW_3 : TW_4) + ((xcd & 1) * 16 + (r & 15)) * 57344 + w * 1024;
    const unsigned oo = WS_O + (qb * 8 + w) * 32768 + head * 1024, to = rq * 32768 + c4 * 8192 + w * 1024;
    const unsigned uf = WS_U + rb * (224 * 1024) + (c8 * 28 + w * 4) * 1024, sf = rq * 4096 + c4 * 1024;
    const unsigned u0 = w < 7 ? uf : WS_STH + sf, u1 = w < 7 ? uf + 1024 : WS_STX + sf, u2 = w < 7 ? uf + 2048 : u0, u3 = w < 7 ? uf + 3072 : u1;
    const u32x4 mk = {MARK, MARK, MARK, MARK};
    const unsigned ho = FLAG_O + ((qb * 8 + w) * 4 + (head >> 3)) * 128;
    const int nq = (int)rq - 1 + ((l - 32) >> 2);
    const bool fon = l < 32 || (l < 44 && nq >= 0 && nq < 64);
    const unsigned fo = fon ? FLAG_F + 4 * (l < 32 ? (l >> 2) + 8 * ((rq >> 3) * 4 + (l & 3)) : (nq >> 4) * 2 + ((l >> 1) & 1) + 8 * ((nq & 15) * 2 + (l & 1))) : 0x7ff00000u;
    unsigned fv = 0u;
    const rsrc_t rx = mkrsrc(x, S * D * 2);
    const unsigned xo = ((rq * 32 + l31) * D + c4 * 128 + wi * 32 + 4 * h) * 2;
    STAMP(1);
    waitv<4>();
    BAR();
    STAMP(2); STAMPC(40);
    const u32x4 sf0 = *(const LAS u32x4*)(uintptr_t)(lds0 + L_SLOW), sf1 = *(const LAS u32x4*)(uintptr_t)(lds0 + L_SLOW + 16);
    if (rfl(sf0[0] | sf0[1] | sf0[2] | sf0[3] | sf1[0] | sf1[1] | sf1[2] | sf1[3]) == 0u) {
      f32x16 s0, s1 = {};
      bf16x8 k0, k1, l0 = vc, l1 = vc, h0 = vc, h1 = vc;
      const unsigned long long msk = 0x0000ffff0000ffffull;
      asm volatile("ds_read_b128 v[54:57], v79\n\tds_read_b128 v[58:61], v79 offset:2048\n\ts_mov_b64 exec, %[msk]\n\tds_read_b128 v[62:65], v79 offset:1024\n\ts_mov_b64 exec, -1\n\t"
                   "s_waitcnt lgkmcnt(0)\n\tv_mfma_f32_32x32x16_bf16 v[18:33], v[54:57], v[50:53], 0\n\ts_nop 7\n\ts_nop 4"
                   : "={v[18:33]}"(s0), "={v[54:57]}"(k0), "={v[58:61]}"(k1), "+{v[62:65]}"(l0) : "{v79}"(ad), "{v[50:53]}"(qf), [msk] "s"(msk) : "memory");
      unsigned dum = 0;
      asm volatile("s_mov_b32 m0, %[ld]\n\t" TILE4("", DL(0), FS(0), DL(1), FS(1), DL(2), FS(2), DL(3))
                   : ATT_OPS : ATT_IN, ATT_LD, [sa] "s"(kv + 49152 + w * 4096), [ld] "s"(lds0 + 49152 + w * 4096), ATT_FILL(oo, WS_H + to, WS_G + to) : "memory");
      waitv<7>(); BAR();
      asm volatile("s_mov_b32 m0, %[ld]\n\t" TILE4(WARM, DL(0), FS(0), DL(1), FS(1), DL(2), FS(2), DL(3))
                   : ATT_OPS, "+{v80}"(dum) : ATT_IN, ATT_LD, [sa] "s"(kv + 81920 + w * 4096), [ld] "s"(lds0 + 81920 + w * 4096), [sw] "s"(wrm), ATT_FILL(WS_X1 + to, u0, u1) : "memory");
      asm volatile("s_mov_b32 m0, %[ld]\n\t" TILE4(WARM, DL(0), FS(0), DL(1), FS(1), "", "", "")
                   : ATT_OPS, "+{v80}"(dum) : ATT_IN, ATT_LD, [sa] "s"(kv + 114688 + w * 2048), [ld] "s"(lds0 + 114688 + w * 2048), [sw] "s"(wrm + 8192), ATT_FILL(u2, u3, u3) : "memory");
      ATT_W(128, 143, 0, 2);
      ATT_W(144, 159, 1, 3);
      waitv<10>(); BAR();
      if (w == 0) hint(rws, FLAG_F + b * 4);
      ATT_W(160, 175, 2, 4);
      ATT_W(176, 191, 3, 5);
      ATT_W(192, 207, 4, 6);
      ATT_W0(208, 223, 5);
      ATT_W0(224, 239, 6);
      ATT_W0(240, 255, 7);
      asm volatile("s_mov_b32 m0, %[ld]\n\t" TILE4("", DL(0), "", DL(1), "", "", "", "")
                   : ATT_OPS : ATT_IN, ATT_LD, [sa] "s"(kq(TW_2, 3) + hw * 1024), [ld] "s"(r2c + hw * 1024) : "memory");
      BAR();
      ATT_D0(kq(TW_2, hw), r2a + hw * 8192);
      ATT_D0(kq(TW_2, hw) + 4096, r2a + hw * 8192 + 4096);
      ATT_D0(kq(TW_2, 2) + hw * 4096, r2b + hw * 4096);
      asm volatile(TILE4("buffer_load_dwordx2 %[x0], %[xo], %[rx], 0 offen\n\tbuffer_load_dwordx2 %[x1], %[xo], %[rx], 0 offen offset:16\n\t",
                         "buffer_load_dwordx2 %[x2], %[xo], %[rx], 0 offen offset:32\n\tbuffer_load_dwordx2 %[x3], %[xo], %[rx], 0 offen offset:48\n\t",
                         "buffer_load_dword %[fv], %[fo], %[rs], 0 offen sc1\n\t", "", "", "", "s_waitcnt vmcnt(0)\n\t",
                         "s_mov_b64 exec, 1\n\tbuffer_atomic_add %[one], %[ho], %[rs], 0 offen sc1\n\ts_mov_b64 exec, -1\n\t")
                   : ATT_OPS, [x0] "=&v"(xr[0]), [x1] "=&v"(xr[1]), [x2] "=&v"(xr[2]), [x3] "=&v"(xr[3]), [fv] "=&v"(fv),
                     "+{v[128:143]}"(wt[0]), "+{v[144:159]}"(wt[1]), "+{v[160:175]}"(wt[2]), "+{v[176:191]}"(wt[3]), "+{v[192:207]}"(wt[4]),
                     "+{v[208:223]}"(wt[5]), "+{v[224:239]}"(wt[6]), "+{v[240:255]}"(wt[7]), "+{v80}"(dum)
                   : ATT_IN, [rs] "s"(rws), [rx] "s"(rx), [xo] "v"(xo), [fo] "v"(fo), [ho] "v"(ho), [one] "v"(1u) : "memory");
      asm volatile("s_nop 1\n\tv_smfmac_f32_32x32x32_bf16 v[2:17], v[74:77], v[42:49], v78\n\ts_nop 7\n\ts_nop 4" : "+{v[2:17]}"(oacc) : "{v[34:49]}"(s1), "{v[74:77]}"(h1), "{v78}"(idx));
    } else {
      st128s(rws, vl, oo, mk); st128s(rws, vl, WS_H + to, mk); st128s(rws, vl, WS_G + to, mk); st128s(rws, vl, WS_X1 + to, mk);
      st128s(rws, vl, u0, mk); st128s(rws, vl, u1, mk); st128s(rws, vl, u2, mk); st128s(rws, vl, u3, mk);
      tile_dma(rws, vl, kv + 49152 + w * 10240, lds0 + 49152 + w * 10240); dmaN<2>(rws, vl, kv + 49152 + w * 10240 + 8192, lds0 + 49152 + w * 10240 + 8192);
      waitv<0>(); BAR();
      if (w == 0) hint(rws, FLAG_F + b * 4);
      float mx = -3.0e38f;
      const f32x16 zero = {};
#pragma unroll 1
      for (int t = 0; t < 64; ++t) {
        const bf16x8 kf = lds128(ad); bf16x8 v0 = vc, v1 = vc;
        if (l31 < 16) { v0 = lds128(ad + 1024); v1 = lds128(ad + 1280); }
        f32x16 s = MFMA32(kf, qf, zero);
        float tm = s[0];
#pragma unroll
        for (int i = 1; i < 16; ++i) tm = fmaxf(tm, s[i]);
        const float mn = fmaxf(mx, swapmax(tm)), al = __builtin_amdgcn_exp2f(mx - mn);
        mx = mn;
#pragma unroll
        for (int i = 0; i < 16; ++i) { s[i] = __builtin_amdgcn_exp2f(s[i] - mn); oacc[i] *= al; }
        oacc = __builtin_amdgcn_smfmac_f32_32x32x32_bf16(__builtin_bit_cast(bf8_t, v0), __builtin_bit_cast(bf16_t, __builtin_shufflevector(s, s, 0, 1, 2, 3, 4, 5, 6, 7)), oacc, idx, 0, 0);
        oacc = __builtin_amdgcn_smfmac_f32_32x32x32_bf16(__builtin_bit_cast(bf8_t, v1), __builtin_bit_cast(bf16_t, __builtin_shufflevector(s, s, 8, 9, 10, 11, 12, 13, 14, 15)), oacc, idx, 0, 0);
        ad += 2048;
      }
      BAR();
#pragma unroll
      for (int k = 0; k < 32; ++k) { const u32x4 t = __builtin_amdgcn_raw_buffer_load_b128(rws, vl + (k & 3) * 1024, wso(k >> 2), 16);
        wt[k >> 2][(k & 3) * 4] = t[0]; wt[k >> 2][(k & 3) * 4 + 1] = t[1]; wt[k >> 2][(k & 3) * 4 + 2] = t[2]; wt[k >> 2][(k & 3) * 4 + 3] = t[3]; }
      tile_dma(rws, vl, kq(TW_2, hw), r2a + hw * 8192); dmaN<4>(rws, vl, kq(TW_2, 2) + hw * 4096, r2b + hw * 4096); dmaN<2>(rws, vl, kq(TW_2, 3) + hw * 1024, r2c + hw * 1024);
#pragma unroll
      for (int g = 0; g < 4; ++g) xr[g] = __builtin_amdgcn_raw_buffer_load_b64(rx, xo + g * 16, 0, 0);
      drain();
      hint(rws, ho);
    }
    STAMP(3); STAMPC(41);
    const float inv = __builtin_amdgcn_rcpf(swapadd(h == 0 ? oacc[8] : 0.f));
#pragma unroll
    for (int i = 0; i < 8; ++i) oacc[i] *= inv;
    st128(rws, oo + vl, pack8(oacc, 0));
    if (w == 4 && !__all(!fon || fv != 0u)) poll(rws, fo, fon, 1u);
#pragma unroll
    for (int k = 0; k < 32; ++k) wf[k] = __builtin_bit_cast(bf16x8, (u32x4){wt[k >> 2][(k & 3) * 4], wt[k >> 2][(k & 3) * 4 + 1], wt[k >> 2][(k & 3) * 4 + 2], wt[k >> 2][(k & 3) * 4 + 3]});
    STAMP(4);
    BAR();
  }
  float cs = 0.f;
  unsigned wsel = w;
  asm volatile("" : "+s"(wsel));
  const unsigned wv = w < 4 ? w : w - 1;
  const unsigned so3 = TW_3 + (c8 * 7 + (w == 3 ? 6 : wv)) * 32768;
  {
  const int l = lane();
  const unsigned vl = l * 16;
  const unsigned tileo = rq * 32768 + (c4 * 16 + wi * 4) * 512;
  const unsigned stato = rq * 4096 + c4 * 1024 + (wi >> 1) * 512 + (wi & 1) * 8;
  const unsigned hq = (rq * 4 + wi) * 128, hs = (rq * 4 + c4) * 128;
  auto rest = [&](unsigned o) { ldw<3072>(wf[27], rws, vl, o); sfor<0, 4>([&](auto K) { ldw<K.v * 1024>(wf[28 + K.v], rws, vl, o + 4096); }); };
  auto unstage = [&]() {
    const unsigned ua = r2a + vl, ub_ = r2b + vl, uc = r2c + vl;
    sfor<0, 16>([&](auto K) { ldsw<K.v * 1024>(wf[K.v], ua); });
    sfor<0, 8>([&](auto K) { ldsw<K.v * 1024>(wf[16 + K.v], ub_); });
    sfor<0, 3>([&](auto K) { ldsw<K.v * 1024>(wf[24 + K.v], uc); });
    asm volatile("s_waitcnt lgkmcnt(0)" ::: "memory");
  };
  auto none = [] {};
  auto none1 = [](f32x16&) {};
  if (w < 4) {
    __builtin_amdgcn_s_setprio(1);
    poll(rws, FLAG_O + hq, l == 0, 8u);
    STAMP(5);
    f32x16 hres = kstage<0, 0>(wf, rws, vl, WS_O + rq * 32768 + wi * 8192, lds0, wi, l, none, none, none1 KPASS);
    hint(rws, FLAG_H + hs);
    STAMP(7);
#pragma unroll
    for (int g = 0; g < 4; ++g) { hres[4 * g] += bflo(xr[g][0]); hres[4 * g + 1] += bfhi(xr[g][0]); hres[4 * g + 2] += bflo(xr[g][1]); hres[4 * g + 3] += bfhi(xr[g][1]); }
    st128s(rws, vl, WS_H + tileo, pack8(hres, 0)); st128s(rws, vl + 1024, WS_H + tileo, pack8(hres, 8));
    put_stats(rws, vl, WS_STH + stato, hres);
    STAMP(8);
    __builtin_amdgcn_s_setprio(0);
    rest(kq(TW_2, 3));
    unstage();
    STAMP(9);
    dmaN<4>(rws, vl, so3, r2a);
    lwait(lds0 + C_H, 4);
    STAMP(10);
    sfor<4, 27>([&](auto K) {
      constexpr int k = K.v;
      dma16<0, true>(rws, vl, so3 + k * 1024, k < 16 ? r2a + k * 1024 : k < 24 ? r2b + (k - 16) * 1024 : r2c + (k - 24) * 1024);
      waitv<4>();
    });
    drain();
    if (w == 3) ladd(lds0 + C_S7);
    STAMP(11);
    const int dl = (wi & 1) ? (int)(wi >> 1) + 1 : -(int)(wi >> 1) - 1;
    const int rn = (int)rq + ((wi & 1) ? 1 : -1);
    __builtin_amdgcn_s_setprio(1);
    poll(rws, FLAG_G + (l ? (rn * 4 + wi) * 128 : hq), l == 0 || (l == 1 && rn >= 0 && rn < 64), 4u);
    STAMP(15);
    const int src = (int)rq * 32 + (l & 31) + dl;
    f32x16 acc = kstage<2, 0>(wf, mkrsrc(ws + WS_G, 2u << 20), (src >= 0 && src < S) ? (unsigned)((src >> 5) * 32768 + (src & 31) * 16 + (l >> 5) * 512) : 0x7ff00000u, wi * 8192, lds0, wi, l,
                              none, [&] { ladd(lds0 + C_G); }, none1 KPASS);
    hint(rws, FLAG_X + hs);
    STAMP(17);
    { const f32x2 t = *(const LAS f32x2*)(uintptr_t)(lds0 + L_TH + wi * 256 + (l & 31) * 8);
      float rs = t[0], ms = t[1];
      TOUCH(rs); TOUCH(ms);
#pragma unroll
      for (int i = 0; i < 16; ++i) acc[i] += hres[i] * rs - ms; }
    st128s(rws, vl, WS_X1 + tileo, pack8(acc, 0)); st128s(rws, vl + 1024, WS_X1 + tileo, pack8(acc, 8));
    put_stats(rws, vl, WS_STX + stato, acc);
    STAMP(18);
    if (w < 3) {
      rest(so3 + 24576);
      unstage();
      drain();
      cs = colsum<0, 32>(wf, l);
    }
    STAMP(19);
  }
  if (wsel >= 4) {
#pragma unroll
    for (int j = 0; j < 4; ++j) {
      const float c = j == 0 ? colsum<0, 8>(wf, l) : j == 1 ? colsum<8, 8>(wf, l) : j == 2 ? colsum<16, 8>(wf, l) : colsum<24, 8>(wf, l);
      if (l < 32) *(LAS float*)(uintptr_t)(lds0 + L_CS + (((wi + 1 + j) & 3) * 4 + wi) * 128 + l * 4) = c;
    }
    ladd(lds0 + C_CS);
    lwait(lds0 + C_CS, 4);
    { const LAS float* t = (const LAS float*)(uintptr_t)(lds0 + L_CS + wi * 512 + (l & 31) * 4); cs = (t[0] + t[32]) + (t[64] + t[96]); }
    bf16x8 co = split_op(cs, true, l < 32);
    TOUCH(co);
    __builtin_amdgcn_s_setprio(1);
    poll(rws, FLAG_H + hq, l == 0, 4u);
    STAMP(6);
    bf16x8 st[4];
    float cz;
    const unsigned vs = (l >> 5) * 2048 + (l & 31) * 16;
    f32x16 acc = kstage<1, 4>(wf, rws, vl, WS_H + rq * 32768 + wi * 8192, lds0, wi, l, [&] { ld_stats(st, rws, vs, WS_STH + rq * 4096); }, [&] { ladd(lds0 + C_H); },
      [&](f32x16& a) {
        get_stats(st, rws, vs, WS_STH + rq * 4096);
        float mu, var; fin_stats(st, mu, var);
        a = MFMA32(co, split_op(-mu, false, true), a);
        const float rs = __builtin_amdgcn_rsqf(var + EPS);
        cz = rs * __builtin_amdgcn_rsqf(var * rs * rs + EPS);
        if (l < 32) *(LAS f32x2*)(uintptr_t)(lds0 + L_TH + wi * 256 + l * 8) = (f32x2){rs, mu * rs};
      } KPASS);
    STAMP(12);
    { const float k1 = GC1 * cz * cz * cz, k2 = GC2 * cz, ir = __builtin_amdgcn_rcpf(cz);
      const u32x4 g0 = gelu8(acc, 0, k1, k2, ir);
      hint(rws, FLAG_G + hs);
      asm volatile("s_waitcnt lgkmcnt(0)" ::: "memory");
      st128s(rws, vl, WS_G + tileo, g0);
      st128s(rws, vl + 1024, WS_G + tileo, gelu8(acc, 8, k1, k2, ir)); }
    STAMP(13);
    __builtin_amdgcn_s_setprio(0);
    unsigned ws3 = w;
    asm volatile("" : "+s"(ws3));
    if (w != 7) {
      sfor<0, 4>([&](auto K) { ldw<K.v * 1024>(wf[K.v], rws, vl, so3); });
      lwait(lds0 + C_G, 4);
      STAMP(14);
      sfor<4, 32>([&](auto K) { ldw<(K.v & 3) * 1024>(wf[K.v], rws, vl, so3 + (K.v >> 2) * 4096); waitv<5>(); });
      drain();
    }
    if (ws3 == 7) {
      rest(so3 + 24576);
      lwait(lds0 + C_S7, 1);
      unstage();
      ladd(lds0 + C_U7);
      drain();
      cs = colsum<0, 32>(wf, l);
    }
    STAMP(20);
  }
  }
#define KS5(t) (8 * (((t) >> 1) & 3) + 2 * ((t) >> 3) + ((t) & 1))
#define XQ(q) ((q) < 3 ? L_PB + (q) * 16384 : 49152)
#define XA5(t) (bb + XQ(KS5(t) >> 3) + (KS5(t) & 7) * 1024)
#define W4LD(k) ldw<(((k) % 14) & 3) * 1024>(wf[KS5(k)], rws, vl, so4 + ((k) / 14) * 114688 + (((k) % 14) >> 2) * 4096)
  u32x4 rh, rx1;
  {
    const int l = lane();
    const int h = l >> 5, l31 = l & 31;
    const unsigned vl = l * 16;
    const unsigned so4 = TW_4 + (c8 * 2 * 112 + w * 14) * 1024;
    const unsigned bb = lds0 + vl;
    if (w - 3 < 4) {
      const unsigned q = w - 3, xs = WS_X1 + rb * 65536 + q * 8192, xd = bb + (q < 3 ? L_PB + q * 16384 : 49152);
      bf16x8 f[4][4];
      __builtin_amdgcn_s_setprio(2);
      const unsigned cw = lcnt(lds0 + (q < 3 ? C_R : C_U7));
      poll(rws, FLAG_X + ((rb * 2 + (l & 1)) * 4 + q) * 128, l < 2, 4u);
      STAMP(21);
      auto ldx = [&](auto J) { ldn<(J.v & 1) * 2048>(f[J.v][0], rws, vl, xs + (J.v >> 1) * 4096); ldn<(J.v & 1) * 2048 + 1024>(f[J.v][1], rws, vl, xs + (J.v >> 1) * 4096);
                               ldn<(J.v & 1) * 2048>(f[J.v][2], rws, vl, xs + 32768 + (J.v >> 1) * 4096); ldn<(J.v & 1) * 2048 + 1024>(f[J.v][3], rws, vl, xs + 32768 + (J.v >> 1) * 4096); };
      sfor<0, 4>(ldx);
      if (w != 3) cs = colsum<0, 32>(wf, l);
      if (rfl(cw) < (q < 3 ? 12u : 1u)) lwait(lds0 + (q < 3 ? C_R : C_U7), q < 3 ? 12u : 1u);
      sfor<0, 4>([&](auto J) {
        take4<12 - 4 * J.v, (J.v & 1) * 2048, (J.v & 1) * 2048 + 1024, (J.v & 1) * 2048, (J.v & 1) * 2048 + 1024>(f[J.v], rws, vl, xs + (J.v >> 1) * 4096, xs + 32768 + (J.v >> 1) * 4096);
        *(LAS bf16x8*)(uintptr_t)(xd + J.v * 2048) = f[J.v][0]; *(LAS bf16x8*)(uintptr_t)(xd + J.v * 2048 + 1024) = f[J.v][1];
        *(LAS bf16x8*)(uintptr_t)(xd + J.v * 2048 + 8192) = f[J.v][2]; *(LAS bf16x8*)(uintptr_t)(xd + J.v * 2048 + 9216) = f[J.v][3];
        ladd(lds0 + C_X + J.v * 4);
      });
      STAMP(31);
    }
    auto body = [&](auto EARLY) {
      constexpr bool E = EARLY.v;
      f32x16 a0 = {}, a1 = {};
      bf16x8 f[3][2];
      const unsigned uo = (h * 64 + l31) * 16, us = WS_U + rb * (224 * 1024) + (c8 * 28 + wv * 4) * 1024;
      lwait(lds0 + C_X + (E ? 0 : 12), 4);
      if (!E) { lwait(lds0 + C_X, 4); lwait(lds0 + C_X + 4, 4); lwait(lds0 + C_X + 8, 4); }
      STAMP(32);
      f[0][0] = lds128(XA5(0)); f[0][1] = lds128(XA5(0) + 8192); f[1][0] = lds128(XA5(1)); f[1][1] = lds128(XA5(1) + 8192);
      unsigned c = 0;
      f32x4 tb0, tb1;
      sfor<0, 32>([&](auto T) {
        constexpr int t = T.v, nl = t < 5 ? 0 : (t - 4) - (t - 4) / 4;
        if constexpr (E && (t == 6 || t == 14 || t == 22)) { if (rfl(c) < 4) lwait(lds0 + C_X + ((t + 2) >> 3) * 4, 4); }
        if constexpr (t == 30) { if (rfl(c) < 1) lwait(lds0 + C_ST, 1); tb0 = lds128f(lds0 + L_TX + l31 * 16); tb1 = lds128f(lds0 + L_TX + 512 + l31 * 16); }
        SB();
        a0 = MFMA32(wf[KS5(t)], f[t % 3][0], a0);
        TOUCH(a0);
        if constexpr (E && (t == 4 || t == 12 || t == 20)) c = lcnt(lds0 + C_X + ((t + 4) >> 3) * 4);
        if constexpr (t == 28) c = lcnt(lds0 + C_ST);
        if constexpr (t < 30) f[(t + 2) % 3][0] = lds128(XA5(t + 2));
        SB();
        a1 = MFMA32(wf[KS5(t)], f[t % 3][1], a1);
        TOUCH(a1);
        if constexpr (t < 30) f[(t + 2) % 3][1] = lds128(XA5(t + 2) + 8192);
        if constexpr (t >= 4 && (t & 3) != 3) W4LD(nl);
        SB();
      });
      { const bf16x8 co = split_op(cs, true, l < 32);
        a0 = MFMA32(co, split_op(tb0[0], false, true), a0); a1 = MFMA32(co, split_op(tb1[0], false, true), a1); }
      TOUCH(a0); TOUCH(a1);
      STAMP(22);
      __builtin_amdgcn_s_setprio(0);
      sfor<0, 7>([&](auto K) { W4LD(21 + K.v); });
      asm volatile("s_nop 7\n\ts_nop 4" : "+v"(a0), "+v"(a1));
      st128s(rws, uo, us, gelu8(a0, 0, tb0[1], tb0[2], tb0[3]));
      st128s(rws, uo + 512, us, gelu8(a1, 0, tb1[1], tb1[2], tb1[3]));
      st128s(rws, uo + 2048, us, gelu8(a0, 8, tb0[1], tb0[2], tb0[3]));
      hint(rws, FLAG_U + (rb * 8 + c8) * 128 + (E ? 0 : 4));
      st128s(rws, uo + 2048 + 512, us, gelu8(a1, 8, tb1[1], tb1[2], tb1[3]));
      STAMP(23);
    };
    unsigned ws2 = w;
    asm volatile("" : "+s"(ws2));
    if (w < 3 || w == 7) { __builtin_amdgcn_s_setprio(2); body(IC<1>{}); }
    if (ws2 - 4 < 3) { __builtin_amdgcn_s_setprio(1); body(IC<0>{}); }
    if (wsel == 3) {
      bf16x8 s0[4], s1[4];
      const unsigned vs = h * 2048 + l31 * 16;
      ld_stats(s0, rws, vs, WS_STX + rb * 8192); ld_stats(s1, rws, vs, WS_STX + rb * 8192 + 4096);
      get_stats(s0, rws, vs, WS_STX + rb * 8192); get_stats(s1, rws, vs, WS_STX + rb * 8192 + 4096);
      float m0, v0, m1, v1;
      fin_stats(s0, m0, v0); fin_stats(s1, m1, v1);
      const float mu = h ? m1 : m0, ve = (h ? v1 : v0) + EPS, rs = __builtin_amdgcn_rsqf(ve);
      *(LAS f32x4*)(uintptr_t)(lds0 + L_TX + vl) = (f32x4){-mu, GC1 * rs * rs * rs, GC2 * rs, ve * rs};
      ladd(lds0 + C_ST);
      __builtin_amdgcn_s_setprio(0);
      sfor<0, 28>([&](auto K) { W4LD(K.v); waitv<6>(); });
      lwait(lds0 + C_X, 4); lwait(lds0 + C_X + 4, 4); lwait(lds0 + C_X + 8, 4); lwait(lds0 + C_X + 12, 4);
    }
    { const int l2 = lane();
      const unsigned ch = (c8 & 1) * 8 + (w & 1) * 4 + ((w >> 1) & 1) * 2, q = c8 >> 1;
      rx1 = __builtin_bit_cast(u32x4, lds128(lds0 + (q < 3 ? L_PB + q * 16384 : 49152) + (w >> 2) * 8192 + ch * 512 + l2 * 16));
      bf16x8 t; ldn<0>(t, rws, l2 * 16, WS_H + (rb * 2 + (w >> 2)) * 32768 + (q * 16 + ch) * 512);
      rh = __builtin_bit_cast(u32x4, t);
      TOUCH(rx1);
      ladd(lds0 + C_5); }
    STAMP(24);
  }
  {
    const int l = lane(), h = l >> 5, l31 = l & 31;
    const unsigned vl = l * 16, vu = (h * 64 + l31) * 16, ub = WS_U + rb * (224 * 1024) + w * 28672;
    f32x16 ac[4] = {};
    bf16x8 f[3][4];
    auto ldu = [&](auto R, auto P) { sfor<0, 4>([&](auto J) { ldn<(J.v >> 1) * 2048 + (J.v & 1) * 512>(f[R.v][J.v], rws, vu, ub + P.v * 4096); }); };
    auto chk = [&](auto R, auto P, auto N) { take4<N.v, 0, 512, 2048, 2560>(f[R.v], rws, vu, ub + P.v * 4096, ub + P.v * 4096); };
    auto mm = [&](auto R, auto P) {
      SB();
      sfor<0, 8>([&](auto J) { constexpr int ks = J.v >> 2, tt = (J.v >> 1) & 1, nt = J.v & 1; ac[tt * 2 + nt] = MFMA32(wf[KS5(nt * 14 + P.v * 2 + ks)], f[R.v][ks * 2 + tt], ac[tt * 2 + nt]); });
      TOUCH(ac[0]); TOUCH(ac[1]); TOUCH(ac[2]); TOUCH(ac[3]);
      SB();
    };
    const unsigned fu = FLAG_U + (rb * 8 + w) * 128;
    poll(rws, fu, l == 0, 4u);
    asm volatile("" : "+v"(rh));
    STAMP(26);
    ldu(IC<0>{}, IC<0>{}); ldu(IC<1>{}, IC<1>{}); ldu(IC<2>{}, IC<2>{});
    chk(IC<0>{}, IC<0>{}, IC<8>{});
    STAMP(35);
    mm(IC<0>{}, IC<0>{});
    ldu(IC<0>{}, IC<6>{});
    chk(IC<1>{}, IC<1>{}, IC<8>{}); mm(IC<1>{}, IC<1>{});
    chk(IC<2>{}, IC<2>{}, IC<4>{}); mm(IC<2>{}, IC<2>{});
    chk(IC<0>{}, IC<6>{}, IC<0>{}); mm(IC<0>{}, IC<6>{});
    STAMP(36);
    poll(rws, fu + 4, l == 0, 3u);
    STAMP(37);
    ldu(IC<0>{}, IC<3>{}); ldu(IC<1>{}, IC<4>{}); ldu(IC<2>{}, IC<5>{});
    const unsigned pw = lds0 + w * 16384 + vl, me = (w >> 2) * 4 + (w & 1) * 2 + ((w >> 1) & 1);
    f32x4 s0 = {bflo(rh[0]) + bflo(rx1[0]), bfhi(rh[0]) + bfhi(rx1[0]), bflo(rh[1]) + bflo(rx1[1]), bfhi(rh[1]) + bfhi(rx1[1])};
    f32x4 s1 = {bflo(rh[2]) + bflo(rx1[2]), bfhi(rh[2]) + bfhi(rx1[2]), bflo(rh[3]) + bflo(rx1[3]), bfhi(rh[3]) + bfhi(rx1[3])};
    lwait(lds0 + C_5, 8);
    chk(IC<0>{}, IC<3>{}, IC<8>{}); chk(IC<1>{}, IC<4>{}, IC<4>{}); chk(IC<2>{}, IC<5>{}, IC<0>{});
    STAMP(25);
    sfor<0, 4>([&](auto Q) {
      constexpr int tt = Q.v >> 1, nt = Q.v & 1;
      sfor<0, 6>([&](auto J) { constexpr int rr = J.v >> 1, ks = J.v & 1; ac[Q.v] = MFMA32(wf[KS5(nt * 14 + (3 + rr) * 2 + ks)], f[rr][ks * 2 + tt], ac[Q.v]); });
      TOUCH(ac[Q.v]);
      SB();
      if constexpr (Q.v > 0) {
        const f32x16& v = ac[Q.v - 1];
#pragma unroll
        for (int j = 0; j < 4; ++j) *(LAS f32x4*)(uintptr_t)(pw + (Q.v - 1) * 4096 + j * 1024) = (f32x4){v[4 * j], v[4 * j + 1], v[4 * j + 2], v[4 * j + 3]};
        SB();
      }
    });
    STAMP(27);
#pragma unroll
    for (int j = 0; j < 4; ++j) *(LAS f32x4*)(uintptr_t)(pw + 3 * 4096 + j * 1024) = (f32x4){ac[3][4 * j], ac[3][4 * j + 1], ac[3][4 * j + 2], ac[3][4 * j + 3]};
    BAR();
    { const unsigned a = lds0 + me * 2048 + vl;
#pragma unroll
      for (int k = 0; k < 8; ++k) { s0 += lds128f(a + (k & 3) * 16384 + (k >> 2) * 65536); s1 += lds128f(a + 1024 + (k & 3) * 16384 + (k >> 2) * 65536); } }
    const auto e0 = __builtin_amdgcn_permlane32_swap(pk2(s0[0], s0[1]), pk2(s1[0], s1[1]), false, false);
    const auto e1 = __builtin_amdgcn_permlane32_swap(pk2(s0[2], s0[3]), pk2(s1[2], s1[3]), false, false);
    *(u32x4*)(out + (rb * 64 + (w >> 2) * 32 + l31) * D + c8 * 64 + (w & 1) * 32 + ((w >> 1) & 1) * 16 + h * 8) = (u32x4){e0[0], e1[0], e0[1], e1[1]};
  }
  STAMPC(42); STAMP(28);
  { const int l = lane(); (void)l; STDUMP(1); }
}

extern "C" void kernel_launch(void* const* d_in, const int* in_sizes, int n_in, void* d_out, int out_size, void* d_ws, size_t ws_size, hipStream_t stream) {
  const u16* x = (const u16*)d_in[0]; const u16* wqkv = (const u16*)d_in[1]; const u16* wo = (const u16*)d_in[2];
  const u16* w1 = (const u16*)d_in[4]; const u16* w2 = (const u16*)d_in[5]; const u16* w3 = (const u16*)d_in[6]; const u16* w4 = (const u16*)d_in[7];
  (void)hipFuncSetAttribute((const void*)k1_qkv, hipFuncAttributeMaxDynamicSharedMemorySize, LDS_BYTES);
  (void)hipFuncSetAttribute((const void*)k2_main, hipFuncAttributeMaxDynamicSharedMemorySize, LDS_BYTES);
  k1_qkv<<<256, 512, LDS_BYTES, stream>>>(x, wqkv, wo, w1, w2, w3, w4, (char*)d_ws);
  k2_main<<<256, 512, LDS_BYTES, stream>>>(x, (u16*)d_out, (char*)d_ws);
}
```

```cpp
#include <hip/hip_runtime.h>
#include <stdint.h>

typedef unsigned short u16;
typedef __attribute__((ext_vector_type(8))) short bf16x8;
typedef __attribute__((ext_vector_type(4))) short s16x4;
typedef __attribute__((ext_vector_type(8))) __bf16 bfv8_t;
typedef __attribute__((ext_vector_type(16))) __bf16 bf16_t;
typedef __attribute__((ext_vector_type(2))) __bf16 bf2_t;
typedef __attribute__((ext_vector_type(16))) float f32x16;
typedef __attribute__((ext_vector_type(8))) float f32x8;
typedef __attribute__((ext_vector_type(4))) float f32x4;
typedef __attribute__((ext_vector_type(2))) float f32x2;
typedef __attribute__((ext_vector_type(16))) unsigned u32x16;
typedef __attribute__((ext_vector_type(8))) unsigned u32x8;
typedef __attribute__((ext_vector_type(4))) unsigned u32x4;
typedef __attribute__((ext_vector_type(2))) unsigned u32x2;
typedef __amdgpu_buffer_rsrc_t rsrc_t;
#define LAS __attribute__((address_space(3)))
#define DI __device__ __forceinline__
#define RLX_AGENT __ATOMIC_RELAXED, __HIP_MEMORY_SCOPE_AGENT

constexpr int S = 2048, D = 512, F = 1792;
constexpr float EPS = 1e-5f;
constexpr unsigned WS_FLAG = 0;
constexpr unsigned WS_EP   = 200704;
constexpr unsigned WS_STH  = 262144;
constexpr unsigned WS_STX  = WS_STH + 262144;
constexpr unsigned WS_Q    = 1u << 20;
constexpr unsigned WS_KV   = WS_Q + (2u << 20);
constexpr unsigned WS_O    = WS_KV + (4u << 20);
constexpr unsigned WS_H    = WS_O + (2u << 20);
constexpr unsigned WS_G    = WS_H + (2u << 20);
constexpr unsigned WS_X1   = WS_G + (2u << 20);
constexpr unsigned WS_CS   = WS_X1 + (2u << 20);
#define GRS 128
constexpr unsigned WS_GR   = WS_CS + 524288;
constexpr unsigned WS_TW   = WS_CS + (1u << 20);
constexpr unsigned TW_O = WS_TW, TW_1 = TW_O + 524288, TW_2 = TW_1 + 524288, TW_3 = TW_2 + 524288, TW_4 = TW_3 + 1835008;
constexpr unsigned WS_U    = TW_4 + 1835008;
constexpr unsigned WS_END  = WS_U + 256u * 28672;
constexpr unsigned FLAG_O = 0, FLAG_H = 32768, FLAG_G = 65536, FLAG_X = 98304, FLAG_U = 131072, FLAG_F = 163840, FLAG_W = 167936, FLAG_B = 172032;
constexpr unsigned MARK = 0xff81ff81u;

constexpr int LDS_BYTES = 163840;
constexpr unsigned L_PB = 96 * 1024, L_Z = 144 * 1024, L_M = 156 * 1024;
constexpr unsigned C_S = L_M, C_R = L_M + 4, C_X = L_M + 8, C_PA = L_M + 16, C_PB = L_M + 20, C_S7 = L_M + 32, C_U7 = L_M + 36, C_ST = L_M + 40, C_H = L_M + 44, C_G = L_M + 48, C_D = L_M + 52, C_E = L_M + 56, C_Q = L_M + 96, C_Y = L_M + 112;
constexpr unsigned L_SP = L_PB + 32768, L_SLOW = L_M + 64, L_TH = L_M + 256, L_TX = L_M + 1280, L_CS = L_M + 2048 + 256;

#define STAMP(i)
#define STAMPC(i)
#define STDECL
#define AP(i)
#define KST(i)
#define AQ(i)
#define AQD()
#define KCNT()
#define KARG
#define KPASS
#define STDUMP(k)
#define STSWAP()

DI rsrc_t mkrsrc(const void* p, unsigned bytes) { return __builtin_amdgcn_make_buffer_rsrc((void*)p, 0, bytes, 0x00020000); }
DI unsigned rfl(unsigned v) { return __builtin_amdgcn_readfirstlane(v); }
DI int lane() { int v; asm volatile("v_mbcnt_lo_u32_b32 %0, -1, 0\n\tv_mbcnt_hi_u32_b32 %0, -1, %0" : "=v"(v)); return v; }
#define MFMA32(a, b, c) __builtin_amdgcn_mfma_f32_32x32x16_bf16((a), (b), (c), 0, 0, 0)
#define MFMA16(a, b, c) __builtin_amdgcn_mfma_f32_16x16x32_bf16((a), (b), (c), 0, 0, 0)
#define MFMA4(a, b, c) __builtin_amdgcn_mfma_f32_4x4x4bf16_1k((a), (b), (c), 0, 0, 0)
#define BAR() asm volatile("s_waitcnt lgkmcnt(0)\n\ts_barrier" ::: "memory")
template <int N> DI void waitv() { asm volatile("s_waitcnt vmcnt(%0)" ::"i"(N) : "memory"); }
#define TOUCH(x) asm volatile("" : "+v"(x))
#define SB() __builtin_amdgcn_sched_barrier(0)
DI void drain() { waitv<0>(); __builtin_amdgcn_s_waitcnt(0x0F70); }

template <int OFF, bool SC1> DI void dma16(rsrc_t rs, unsigned voff, unsigned soff, unsigned lds) {
  if constexpr (SC1) asm volatile("s_mov_b32 m0, %0\n\ts_nop 0\n\tbuffer_load_dwordx4 %1, %2, %3 offen offset:%4 sc1 lds" ::"s"(lds), "v"(voff), "s"(rs), "s"(soff), "i"(OFF) : "memory");
  else asm volatile("s_mov_b32 m0, %0\n\ts_nop 0\n\tbuffer_load_dwordx4 %1, %2, %3 offen offset:%4 lds" ::"s"(lds), "v"(voff), "s"(rs), "s"(soff), "i"(OFF) : "memory");
}
#define P1_PAIR(v0, v1, rs, so) "buffer_load_dwordx4 %[" #v0 "], " rs ", %[" #so "] offen lds\n\tbuffer_load_dwordx4 %[" #v1 "], " rs ", %[" #so "] offen offset:1024 lds\n\t"
#define P1_CHUNK(sx, sw, lo) "s_add_u32 m0, %[lw], " #lo "\n\ts_add_u32 %[t], %[" #sw "], 0x400\n\t" P1_PAIR(x0, x1, "s[40:43]", sx)                          \
  "s_add_u32 m0, %[lw], " #lo "+0x2000\n\ts_add_u32 %[u], %[" #sw "], 0x800\n\t" P1_PAIR(w0, w1, "s[44:47]", sw)                                             \
  "s_add_u32 m0, %[lw], " #lo "+0x4000\n\ts_nop 0\n\t" P1_PAIR(w0, w1, "s[44:47]", t) "s_add_u32 m0, %[lw], " #lo "+0x6000\n\ts_nop 0\n\t" P1_PAIR(w0, w1, "s[44:47]", u)
typedef unsigned long long u64;
template <unsigned LO> DI void chunk1(u64 px, u64 cx, u64 pw, u64 cw, unsigned x0, unsigned x1, unsigned w0, unsigned w1, unsigned sx, unsigned sw, unsigned lw) {
  unsigned t, u;
  asm volatile(P1_CHUNK(sx, sw, %[lo]) : [t] "=&s"(t), [u] "=&s"(u) : "{s[40:41]}"(px), "{s[42:43]}"(cx), "{s[44:45]}"(pw), "{s[46:47]}"(cw), [x0] "v"(x0), [x1] "v"(x1), [w0] "v"(w0), [w1] "v"(w1),
               [sx] "s"(sx), [sw] "s"(sw), [lw] "s"(lw), [lo] "i"(LO) : "memory", "scc");
}
template <int N> DI void dmaN(rsrc_t rs, unsigned voff, unsigned soff, unsigned lds) {
  if constexpr (N == 1) dma16<0, true>(rs, voff, soff, lds);
  else if constexpr (N == 2) asm volatile("s_mov_b32 m0, %0\n\ts_nop 0\n\tbuffer_load_dwordx4 %1, %2, %3 offen sc1 lds\n\tbuffer_load_dwordx4 %1, %2, %3 offen offset:1024 sc1 lds" ::"s"(lds), "v"(voff), "s"(rs), "s"(soff) : "memory");
  else if constexpr (N == 3) asm volatile("s_mov_b32 m0, %0\n\ts_nop 0\n\tbuffer_load_dwordx4 %1, %2, %3 offen sc1 lds\n\tbuffer_load_dwordx4 %1, %2, %3 offen offset:1024 sc1 lds\n\tbuffer_load_dwordx4 %1, %2, %3 offen offset:2048 sc1 lds" ::"s"(lds), "v"(voff), "s"(rs), "s"(soff) : "memory");
  else asm volatile("s_mov_b32 m0, %0\n\ts_nop 0\n\tbuffer_load_dwordx4 %1, %2, %3 offen sc1 lds\n\tbuffer_load_dwordx4 %1, %2, %3 offen offset:1024 sc1 lds\n\tbuffer_load_dwordx4 %1, %2, %3 offen offset:2048 sc1 lds\n\tbuffer_load_dwordx4 %1, %2, %3 offen offset:3072 sc1 lds" ::"s"(lds), "v"(voff), "s"(rs), "s"(soff) : "memory");
}
DI bf16x8 lds128(unsigned a) { return *(const LAS bf16x8*)(uintptr_t)a; }
DI f32x4 lds128f(unsigned a) { return *(const LAS f32x4*)(uintptr_t)a; }
DI s16x4 ldstr(unsigned a) { return __builtin_amdgcn_ds_read_tr16_b64_v4i16((LAS s16x4*)(uintptr_t)a); }
DI bf16x8 trfrag(unsigned a0, unsigned a1) { s16x4 lo = ldstr(a0), hi = ldstr(a1); return __builtin_shufflevector(lo, hi, 0, 1, 2, 3, 4, 5, 6, 7); }
DI unsigned pk2(float lo, float hi) { f32x2 v = {lo, hi}; bf2_t r = __builtin_convertvector(v, bf2_t); return __builtin_bit_cast(unsigned, r); }
DI float bflo(unsigned u) { return __uint_as_float(u << 16); }
DI float bfhi(unsigned u) { return __uint_as_float(u & 0xffff0000u); }
DI float swapadd(float v) { auto r = __builtin_amdgcn_permlane32_swap(__float_as_uint(v), __float_as_uint(v), false, false); return __uint_as_float(r[0]) + __uint_as_float(r[1]); }
DI float swapmax(float v) { auto r = __builtin_amdgcn_permlane32_swap(__float_as_uint(v), __float_as_uint(v), false, false); return fmaxf(__uint_as_float(r[0]), __uint_as_float(r[1])); }
DI float swap16add(float v) { auto r = __builtin_amdgcn_permlane16_swap(__float_as_uint(v), __float_as_uint(v), false, false); return __uint_as_float(r[0]) + __uint_as_float(r[1]); }
DI float swap16max(float v) { auto r = __builtin_amdgcn_permlane16_swap(__float_as_uint(v), __float_as_uint(v), false, false); return fmaxf(__uint_as_float(r[0]), __uint_as_float(r[1])); }
template <int CTRL> DI float dppf(float v) { return __uint_as_float(__builtin_amdgcn_update_dpp(0, __float_as_uint(v), CTRL, 0xf, 0xf, true)); }
DI float max16(float v) { v = fmaxf(v, dppf<0xB1>(v)); v = fmaxf(v, dppf<0x4E>(v)); v = fmaxf(v, dppf<0x141>(v)); return fmaxf(v, dppf<0x140>(v)); }
DI float sum8(float v) { v += dppf<0xB1>(v); v += dppf<0x4E>(v); return v + dppf<0x141>(v); }
DI float dot2(unsigned a, unsigned b, float c) { return __builtin_amdgcn_fdot2_f32_bf16(__builtin_bit_cast(bf2_t, a), __builtin_bit_cast(bf2_t, b), c, false); }
DI float gelu(float x) {
  const float t = x * __builtin_fmaf(x * x, -0.10294324f, -2.3022082f);
  return x * __builtin_amdgcn_rcpf(1.0f + __builtin_amdgcn_exp2f(t));
}
DI void st128(rsrc_t rs, unsigned off, u32x4 v) { __builtin_amdgcn_raw_buffer_store_b128(v, rs, off, 0, 16); }
DI void st128s(rsrc_t rs, unsigned voff, unsigned soff, u32x4 v) { __builtin_amdgcn_raw_buffer_store_b128(v, rs, voff + soff, 0, 16); }
DI bf16x8 ld128(rsrc_t rs, unsigned voff, unsigned soff) { return __builtin_bit_cast(bf16x8, __builtin_amdgcn_raw_buffer_load_b128(rs, voff, soff, 16)); }
template <int OFF> DI void ldw(bf16x8& r, rsrc_t rs, unsigned voff, unsigned soff) {
  asm volatile("buffer_load_dwordx4 %0, %1, %2, %3 offen offset:%4 sc1" : "+v"(r) : "v"(voff), "s"(rs), "s"(soff), "i"(OFF) : "memory");
}
template <int OFF> DI void ldn(bf16x8& r, rsrc_t rs, unsigned voff, unsigned soff) {
  asm volatile("buffer_load_dwordx4 %0, %1, %2, %3 offen offset:%4 sc1" : "=v"(r) : "v"(voff), "s"(rs), "s"(soff), "i"(OFF) : "memory");
}
template <int N> DI void got4(bf16x8 (&f)[4]) { asm volatile("s_waitcnt vmcnt(%4)" : "+v"(f[0]), "+v"(f[1]), "+v"(f[2]), "+v"(f[3]) : "i"(N) : "memory"); }
template <int OFF> DI void ldsw(bf16x8& r, unsigned a) { asm volatile("ds_read_b128 %0, %1 offset:%2" : "+v"(r) : "v"(a), "i"(OFF) : "memory"); }
template <int V> struct IC { static constexpr int v = V; };
template <int B, int E, class Fn> DI void sfor(Fn&& fn) { if constexpr (B < E) { fn(IC<B>{}); sfor<B + 1, E>(fn); } }
DI u32x4 pack8(const f32x16& a, int o) { u32x4 p; p[0] = pk2(a[o], a[o + 1]); p[1] = pk2(a[o + 2], a[o + 3]); p[2] = pk2(a[o + 4], a[o + 5]); p[3] = pk2(a[o + 6], a[o + 7]); return p; }

DI bf16x8 split_op(float v, bool first, bool on) {
  const unsigned hi = __float_as_uint(v) & 0xffff0000u, lo = pk2(v - __uint_as_float(hi), 0.f);
  u32x4 o = {0u, 0u, 0u, 0u};
  if (first) { o[0] = hi | (hi >> 16); o[1] = lo | (lo << 16); } else { o[0] = (hi >> 16) | (lo << 16); o[1] = o[0]; }
  if (!on) { o[0] = 0u; o[1] = 0u; }
  return __builtin_bit_cast(bf16x8, o);
}

constexpr unsigned SPIN = 1u << 20;
DI void ladd(unsigned a) { asm volatile("s_mov_b64 exec, 1\n\tds_add_u32 %0, %1\n\ts_mov_b64 exec, -1" ::"v"(a), "v"(1u) : "memory"); }
DI unsigned lcnt(unsigned a) { return *(const volatile LAS unsigned*)(uintptr_t)a; }
DI void lwait(unsigned a, unsigned need) {
#pragma unroll 1
  for (unsigned sp = 0; sp < SPIN && rfl(lcnt(a)) < need; ++sp) __builtin_amdgcn_s_sleep(1);
  asm volatile("" ::: "memory");
}
DI u32x2 gettag(rsrc_t rs) {
  unsigned long long e;
  asm volatile("s_buffer_load_dwordx2 %0, %1, %2\n\ts_waitcnt lgkmcnt(0)" : "=s"(e) : "s"(rs), "s"(WS_EP) : "memory");
  e += 0x9e3779b97f4a7c15ull;
  return (u32x2){(unsigned)e, (unsigned)(e >> 32)};
}
DI void phase1(u64 px, u64 pw, const u16* wo, const u16* w1, const u16* w2, const u16* w3, const u16* w4, char*& ws, unsigned lds0, int b, unsigned w, u32x2& tag KARG) {
  const unsigned long long cx = (0x00020000ull << 32) | (S * D * 2), cw = (0x00020000ull << 32) | (D * 1536 * 2);
  const int l = lane();
#define EPF 0
  u64 pc; unsigned len, wm0 = 0;
  asm volatile("s_getpc_b64 %0\n9:\n\ts_mov_b32 %1, kernel_end - 9b" : "=s"(pc), "=s"(len));
  if (EPF) asm volatile("s_mov_b64 exec, 0xffff\n\tglobal_load_dword %0, %1, %2\n\ts_mov_b64 exec, -1" : "=&{v200}"(wm0) : "v"(w * 2048 + l * 128), "s"(pc) : "memory");
  const unsigned xcd = b & 7, r = b >> 3;
  const unsigned rb = (xcd >> 1) * 8 + (r >> 2), cb = (xcd & 1) * 4 + (r & 3);
  constexpr unsigned SLOT = 32768, LC = 4 * SLOT, LT = LC + 20480;
  const int h = l >> 5, l31 = l & 31, G = (l >> 4) & 1, q = (l >> 2) & 3, p4 = l & 3;
  const unsigned hw = w & 3;
  const unsigned vx0 = (l >> 3) * 1024 + (((l & 7) ^ ((l >> 3) & 6)) << 4), vx1 = (vx0 ^ 16) + 8192 - 1024;
  const unsigned vsw = ((l & 7) ^ (G << 2)) << 4, vw0 = (l >> 3) * 3072 + vsw, vw1 = vw0 + 8 * 3072 - 1024;
#define NOLD 0
#define LA 2
#define RMASK 6
#define WU 0
  const unsigned rot = ((r >> 2) + 2 * (r & 3)) & RMASK;
  const unsigned sx0 = (rb * 64 + hw * 16) * 1024, sw0 = hw * 16 * 3072 + cb * 128, lw = lds0 + hw * 2048;
  auto issue = [&](auto C) { const unsigned cr = (C.v + rot) & 7; chunk1<(C.v & 3) * SLOT>(px, cx, pw, cw, vx0, vx1, vw0, vw1, sx0 + cr * 128, sw0 + cr * 64 * 3072, lw); };
  { unsigned t, u;
    const unsigned c0 = rot & 7, c1 = (rot + 1) & 7;
    asm volatile("s_cmp_lt_u32 %[w], 4\n\ts_waitcnt lgkmcnt(0)\n\ts_cbranch_scc1 1f\n\t" P1_CHUNK(sx, sw, 0) P1_CHUNK(sy, sz, 0x8000) "\n1:"
                 : [t] "=&s"(t), [u] "=&s"(u), "+{s[40:41]}"(px), "+{s[44:45]}"(pw), "+{s[38:39]}"(ws) : "{s[42:43]}"(cx), "{s[46:47]}"(cw), [x0] "v"(vx0), [x1] "v"(vx1), [w0] "v"(vw0), [w1] "v"(vw1),
                   [sx] "s"(sx0 + c0 * 128), [sw] "s"(sw0 + c0 * 64 * 3072), [sy] "s"(sx0 + c1 * 128), [sz] "s"(sw0 + c1 * 64 * 3072), [lw] "s"(lw), [w] "s"(w) : "memory", "scc"); }
  STAMP(34);
  const rsrc_t rws = mkrsrc(ws, WS_END);
  const unsigned so_ = ((0x10100101u >> l31) & 1u) * 0x3f803f80u;
  u32x4 sel4 = {so_, so_, so_, so_};
  if (w >= 4) {
    if (LA == 3) issue(IC<2>{});
    STAMP(32);
    tag = gettag(rws);
    STAMP(1);
    TOUCH(sel4);
    const bf16x8 sel = __builtin_bit_cast(bf16x8, sel4);
    const unsigned bit = hw >> 1, hf = hw & 1;
    const unsigned sso = (b >> 3) * 16384 + (b & 7) * 128, dst = TW_O + bit * 524288 + (((b & 7) * 2 + hf) * 32 + (b >> 3)) * 1024;
    const unsigned vs = (l >> 3) * 1024 + vsw, lc = lds0 + LC + hw * 2048;
    const rsrc_t rp0 = mkrsrc(bit ? w1 : wo, D * D * 2);
    unsigned ta = lc + (4 * h + q) * 128 + (((4 * hf + 2 * G + (p4 >> 1)) ^ ((q >> 1) << 2)) << 4) + 8 * (p4 & 1);
    unsigned dq = dst + l * 16, dc = bit && h == 0 ? WS_CS + ((dst - TW_O) >> 15) * 4096 + ((dst >> 10) & 31) * 128 + l31 * 4 : 0x7ff00000u;
    const unsigned rq = rb * 2 + ((r >> 1) & 1), c4 = (xcd & 1) * 2 + (r & 1);
    unsigned zo = w != 4 ? 0x7ff00000u : l < 3 ? FLAG_H + l * 32768 + (rq * 4 + c4) * 128 : l < 7 ? FLAG_U + (rb * 8 + cb) * 128 + (l - 3) * 4 : 0x7ff00000u;
    TOUCH(ta); TOUCH(dq); TOUCH(dc); TOUCH(zo);
#define PAT 5
    sfor<0, 8>([&](auto C) {
      constexpr int c = C.v;
      waitv<LA == 2 ? (c < 7 ? 8 : 0) + (c == PAT + 1 || (c == PAT + 2 && PAT < 6) ? 2 : 0) : (c < 6 ? 16 : c < 7 ? 8 : 0) + (c > PAT ? 2 : 0)>();
      STAMP(16 + c);
      BAR();
      STAMP(4 + c); AP(c);
      if constexpr (c + LA < 8) issue(IC<c + LA>{});
      if constexpr (c == PAT) { dma16<0, false>(rp0, vs, sso, lc); dma16<0, false>(rp0, vs, sso + 8192, lc + 1024); }
      STAMP(24 + c);
    });
    BAR(); AP(8);
    asm volatile("s_waitcnt vmcnt(0)" :: "{v200}"(wm0) : "memory");
    STAMP(14);
    { const bf16x8 fr = trfrag(ta, ta + 1024);
      st128(rws, dq, __builtin_bit_cast(u32x4, fr));
      const f32x4 z4 = {};
      const f32x4 c = MFMA16(sel, fr, z4);
      __builtin_amdgcn_raw_buffer_store_b32(0u, rws, zo, 0, 16);
      __builtin_amdgcn_raw_buffer_store_b32(__float_as_uint(c[0]), rws, dc, 0, 16); }
    STAMP(2);
  } else {
    STAMP(32);
    if (w == 0) *(LAS unsigned*)(uintptr_t)(lds0 + L_M + (l & 31) * 4) = 0u;
    const unsigned tt = w & 1, hp = w >> 1;
    unsigned wm1 = 0;
    if (w == 0) {
      const unsigned o = EPF * 16384 + (r * 64 + l) * 64;
      if (o + 4 <= len) asm volatile("global_load_dword %0, %1, %2" : "=&v"(wm1) : "v"(o), "s"(pc) : "memory");
    }
    tag = gettag(rws);
    STAMP(1);
    TOUCH(sel4);
    const bf16x8 sel = __builtin_bit_cast(bf16x8, sel4);
    const unsigned flip = tt ^ hp;
    unsigned xa[2][4], wa[2][2];
#pragma unroll
    for (int s = 0; s < 4; ++s) {
      xa[0][s] = lds0 + (tt * 4 + (l31 >> 3)) * 1024 + (l31 & 7) * 128 + (((2 * (s ^ flip) + h) ^ ((l31 & 6) | ((l31 >> 3) & 1))) << 4); xa[1][s] = xa[0][s] + 2 * SLOT;
      TOUCH(xa[0][s]); TOUCH(xa[1][s]);
    }
    wa[0][0] = lds0 + 8192 + (8 * h + q) * 128 + (((4 * hp + 2 * G + (p4 >> 1)) ^ ((q >> 1) << 2)) << 4) + 8 * (p4 & 1) + flip * 2048; wa[0][1] = wa[0][0] - flip * 4096;
    wa[1][0] = wa[0][0] + 2 * SLOT; wa[1][1] = wa[0][1] + 2 * SLOT;
    TOUCH(wa[0][0]); TOUCH(wa[0][1]); TOUCH(wa[1][0]); TOUCH(wa[1][1]);
    f32x16 aq = {}, ak = {}, av = {};
    f32x4 cq4 = {}, ck4 = {}, cv4 = {}, sx4 = {}, sq4 = {}, sr4 = {};
    u32x4 m4 = {}, k4 = {}, v4 = {}, q4 = {};
    unsigned zero = 0u;
    unsigned long long m1 = 0xaaaaaaaaaaaaaaaaull, m2 = 0xccccccccccccccccull, mhi = 0xffffffff00000000ull;
    TOUCH(m4); TOUCH(k4); TOUCH(v4); TOUCH(q4); TOUCH(zero);
    asm volatile("" : "+s"(m1), "+s"(m2), "+s"(mhi));
    auto hilo = [&](u32x4& o, float v) { const unsigned hi = __float_as_uint(v) & 0xffff0000u, lo = pk2(v - __uint_as_float(hi), 0.f); o[0] = hi | (hi >> 16); o[1] = lo | (lo << 16); TOUCH(o); };
    auto stat = [&](const bf16x8& f) {
      const s16x4 lo = __builtin_shufflevector(f, f, 0, 1, 2, 3), hi = __builtin_shufflevector(f, f, 4, 5, 6, 7);
      sx4 = MFMA16(sel, f, sx4); TOUCH(sx4); sq4 = MFMA4(lo, lo, sq4); TOUCH(sq4); sr4 = MFMA4(hi, hi, sr4); TOUCH(sr4); };
    const unsigned head = cb * 4 + hp * 2, t32 = rb * 2 + tt;
    const unsigned sk = WS_KV + head * 131072 + t32 * 2048;
    unsigned vq = (h * 2048 + l31) * 16 + WS_Q + head * 65536 + t32 * 512, vq1 = vq + 65536;
    unsigned vk = sk + h * 512 + ((l31 & 19) | ((l31 & 4) << 1) | ((l31 & 8) >> 1)) * 16, vk1 = vk + 131072;
    unsigned vv = sk + G * 131072 + 1024 + h * 512 + (l & 15) * 16;
    unsigned cta = lds0 + LT + l31 * 16 + w * 1024, ctb = cta ^ 1024, ctc = cta ^ 2048;
    unsigned rta = lds0 + LT + 4096 + w * 128 + l31 * 4, rtb = lds0 + LT + 4096 + w * 128 + h * 16;
    TOUCH(vq); TOUCH(vq1); TOUCH(vk); TOUCH(vk1); TOUCH(vv); TOUCH(cta); TOUCH(ctb); TOUCH(ctc); TOUCH(rta); TOUCH(rtb);
    bf16x8 e[4][4];
    auto ldx = [&](int s) { e[s & 3][0] = lds128(xa[(s >> 3) & 1][s & 3] + ((s >> 2) & 1) * SLOT); };
    auto ldw_ = [&](int s, int p) { const unsigned a = wa[(s >> 3) & 1][s & 1] + ((s >> 2) & 1) * SLOT + p * 8192 + (s & 3) * 2048; e[s & 3][1 + p] = trfrag(a, a + 512); };
    BAR();
    STAMP(4);
#pragma unroll
    for (int j = 0; j < 2; ++j) { SB(); ldx(j); ldw_(j, 0); SB(); ldw_(j, 1); SB(); ldw_(j, 2); SB(); }
#pragma unroll
    for (int s = 0; s < 26; ++s) {
      const int j = s & 3;
      if (j == 2) { if (LA == 3) BAR(); else asm volatile("s_barrier" ::: "memory"); }
      SB();
      aq = MFMA32(e[j][1], e[j][0], aq); TOUCH(aq);
      ldx(s + 2); ldw_(s + 2, 0);
      if (!(j & 1)) { cq4 = MFMA16(sel, e[j][1], cq4); TOUCH(cq4); }
      SB();
      ak = MFMA32(e[j][2], e[j][0], ak); TOUCH(ak);
      ldw_(s + 2, 1);
      if (!(j & 1)) { ck4 = MFMA16(sel, e[j][2], ck4); TOUCH(ck4); } else stat(e[j][0]);
      SB();
      av = MFMA32(e[j][0], e[j][3], av); TOUCH(av);
      ldw_(s + 2, 2);
      if (!(j & 1)) { cv4 = MFMA16(sel, e[j][3], cv4); TOUCH(cv4); }
      SB();
    }
    unsigned long long aq_[10]; (void)aq_;
    bf16x8 t[4][4];
    auto lx = [&](int i) { t[i][0] = lds128(xa[1][i] + SLOT); };
    auto lw = [&](int i, int p) { const unsigned a = wa[1][i & 1] + SLOT + p * 8192 + i * 2048; t[i][1 + p] = trfrag(a, a + 512); };
    asm volatile("s_barrier" ::: "memory");
    AQ(0);
    SB();
    aq = MFMA32(e[2][1], e[2][0], aq); TOUCH(aq); lx(0); lx(1); lx(2); lx(3); cq4 = MFMA16(sel, e[2][1], cq4); TOUCH(cq4);
    SB();
    ak = MFMA32(e[2][2], e[2][0], ak); TOUCH(ak); lw(0, 1); lw(1, 1); ck4 = MFMA16(sel, e[2][2], ck4); TOUCH(ck4);
    SB();
    av = MFMA32(e[2][0], e[2][3], av); TOUCH(av); lw(2, 1); lw(3, 1); cv4 = MFMA16(sel, e[2][3], cv4); TOUCH(cv4);
    SB();
    aq = MFMA32(e[3][1], e[3][0], aq); TOUCH(aq); lw(0, 2); lw(1, 2);
    SB();
    ak = MFMA32(e[3][2], e[3][0], ak); TOUCH(ak); lw(2, 2); lw(3, 2); stat(e[3][0]);
    SB();
#define M32(acc, a, b) do { acc = MFMA32(a, b, acc); TOUCH(acc); SB(); } while (0)
#define M16(acc, f) do { acc = MFMA16(sel, f, acc); TOUCH(acc); SB(); } while (0)
#define M4(acc, f, i) do { const s16x4 h_ = __builtin_shufflevector(f, f, 4 * i, 4 * i + 1, 4 * i + 2, 4 * i + 3); acc = MFMA4(h_, h_, acc); TOUCH(acc); SB(); } while (0)
#define SEL(d, a, m) asm volatile("v_cndmask_b32 %0, %0, %1, %2" : "+v"(d) : "v"(a), "s"(m))
    f32x4 sx5 = {}, sq5 = {}, sr5 = {}, pb;
    float g0, g1, g2, g3, d0, d1;
    M32(av, e[3][0], e[3][3]);
    pb[0] = cq4[0]; pb[1] = ck4[0]; pb[2] = cv4[0]; TOUCH(pb); SB();
    g0 = sq4[0] + sr4[0]; g1 = sq4[1] + sr4[1]; g2 = sq4[2] + sr4[2]; g3 = sq4[3] + sr4[3]; TOUCH(g0); TOUCH(g1); TOUCH(g2); TOUCH(g3); SB();
    STAMP(11); AQ(1);
    M16(sx5, t[0][0]); pb[3] = sx4[0]; *(LAS f32x4*)(uintptr_t)cta = pb; SEL(g0, g1, m1); SB();
    M4(sq5, t[0][0], 0); SEL(g2, g3, m1); SB();
    M4(sr5, t[0][0], 1); SEL(g0, g2, m2); SB();
    M16(sx5, t[1][0]); d0 = swapadd(g0); TOUCH(d0); SB();
    M4(sq5, t[1][0], 0); *(LAS float*)(uintptr_t)(cta + 512) = d0; SB();
    M4(sr5, t[1][0], 1);
    M16(sx5, t[2][0]); M4(sq5, t[2][0], 0); M4(sr5, t[2][0], 1);
    M16(sx5, t[3][0]); M4(sq5, t[3][0], 0); M4(sr5, t[3][0], 1);
    AQ(2);
    BAR();
    AQ(3);
    const f32x4 pc = lds128f(ctb);
    const float px = *(const LAS float*)(uintptr_t)(ctc + 12), pd = *(const LAS float*)(uintptr_t)(ctc + 512);
    SB();
    M32(ak, t[0][2], t[0][0]); lw(0, 0); lw(1, 0); SB();
    g0 = sq5[0] + sr5[0]; g1 = sq5[1] + sr5[1]; g2 = sq5[2] + sr5[2]; TOUCH(g0); TOUCH(g1); TOUCH(g2); SB();
    M16(ck4, t[0][2]); g3 = sq5[3] + sr5[3]; SEL(g0, g1, m1); SEL(g2, g3, m1); SB();
    M16(ck4, t[1][2]); SEL(g0, g2, m2); float mu = pb[3] + sx5[0]; TOUCH(mu); SB();
    M32(ak, t[1][2], t[1][0]); d1 = swapadd(g0) + d0; TOUCH(d1); SB(); lw(2, 0); SB();
    M16(ck4, t[2][2]); lw(3, 0); SB();
    M16(ck4, t[3][2]); mu += px; d1 += pd; mu *= 1.f / D; TOUCH(mu); TOUCH(d1); SB();
    M32(ak, t[2][2], t[2][0]);
    float rs;
    asm volatile("v_mul_f32 %0, %1, -%1\n\tv_fmac_f32 %0, 0x3b000000, %2\n\tv_max_f32 %0, 0, %0\n\tv_add_f32 %0, 0x3727c5ac, %0\n\tv_rsq_f32 %0, %0" : "=&v"(rs) : "v"(mu), "v"(d1));
    unsigned mh = __float_as_uint(-mu) & 0xffff0000u; float ml = -mu - __uint_as_float(mh); TOUCH(mh); TOUCH(ml); SB();
    M16(cv4, t[0][3]); { unsigned o = (mh >> 16) | (pk2(ml, 0.f) << 16); SEL(o, zero, mhi); m4[0] = o; TOUCH(m4); } SB();
    M16(cv4, t[1][3]); m4[1] = m4[0]; float ck = ck4[0] + pc[1]; TOUCH(m4); TOUCH(ck); SB();
    const bf16x8 mo = __builtin_bit_cast(bf16x8, m4);
    f32x4 rv[4];
    M16(cv4, t[2][3]); *(LAS float*)(uintptr_t)rta = rs; rv[0] = lds128f(rtb); SB();
    M16(cv4, t[3][3]); rv[1] = lds128f(rtb + 32); rv[2] = lds128f(rtb + 64); SB();
    M32(ak, t[3][2], t[3][0]); hilo(k4, ck); rv[3] = lds128f(rtb + 96); SB();
    M32(ak, __builtin_bit_cast(bf16x8, k4), mo); AQ(4);
    hilo(v4, cv4[0] + pc[2]); SB();
    float kk[16], vs_[16];
    u32x4 o0, o1, o2, o3;
#define KM(i) do { kk[i] = ak[i] * rs; TOUCH(kk[i]); } while (0)
#define VM(i) do { vs_[i] = av[i] * rv[(i) >> 2][(i) & 3]; TOUCH(vs_[i]); } while (0)
#define CV(o, j, a, i) do { o[j] = pk2(a[i], a[i + 1]); } while (0)
    M32(av, t[0][0], t[0][3]);
    float qs = rs * 0.36067376f;
    TOUCH(qs); SB();
    KM(0); KM(1); KM(2); SB();
    M16(cq4, t[0][1]); KM(3); KM(4); SB();
    M16(cq4, t[1][1]); KM(5); KM(6); SB();
    M32(av, t[1][0], t[1][3]); KM(7); CV(o0, 0, kk, 0); CV(o0, 1, kk, 2); CV(o0, 2, kk, 4); CV(o0, 3, kk, 6); st128(rws, vk, o0); SB();
    M16(cq4, t[2][1]); KM(8); KM(9); SB();
    M16(cq4, t[3][1]); KM(10); KM(11); SB();
    M32(av, t[2][0], t[2][3]); KM(12); KM(13); KM(14); KM(15); CV(o1, 0, kk, 8); CV(o1, 1, kk, 10); TOUCH(o1); SB();
    M32(av, t[3][0], t[3][3]); CV(o1, 2, kk, 12); CV(o1, 3, kk, 14); st128(rws, vk1, o1); SB();
    M32(av, mo, __builtin_bit_cast(bf16x8, v4)); STAMP(12); AQ(5);
    hilo(q4, cq4[0] + pc[0]); SB();
    M32(aq, t[0][1], t[0][0]); VM(0); VM(1); VM(2); SB();
    M32(aq, t[1][1], t[1][0]); VM(3); VM(4); VM(5); VM(6); VM(7); CV(o2, 0, vs_, 0); TOUCH(o2); SB();
    M32(aq, t[2][1], t[2][0]); CV(o2, 1, vs_, 2); CV(o2, 2, vs_, 4); CV(o2, 3, vs_, 6); st128(rws, vv, o2); SB(); VM(8); VM(9); SB();
    M32(aq, t[3][1], t[3][0]); VM(10); VM(11); VM(12); VM(13); VM(14); VM(15); SB();
    M32(aq, __builtin_bit_cast(bf16x8, q4), mo); AQ(6);
    CV(o3, 0, vs_, 8); CV(o3, 1, vs_, 10); CV(o3, 2, vs_, 12); CV(o3, 3, vs_, 14); st128(rws, vv + 256, o3); SB();
    { f32x16 qq;
#pragma unroll
      for (int i = 0; i < 8; ++i) qq[i] = aq[i] * qs;
      const u32x4 o4 = pack8(qq, 0); st128(rws, vq, o4);
      SB();
#pragma unroll
      for (int i = 8; i < 16; ++i) qq[i] = aq[i] * qs;
      const u32x4 o5 = pack8(qq, 8); st128(rws, vq1, o5);
      asm volatile("" :: "v"(o0), "v"(o1), "v"(o2), "v"(o3), "v"(o4), "v"(o5));
#pragma unroll
      for (int i = 0; i < 4; ++i) asm volatile("" :: "v"(t[i][0]), "v"(t[i][1]), "v"(t[i][2]), "v"(t[i][3]));
      asm volatile("" :: "v"(cq4), "v"(ck4), "v"(cv4), "v"(sel), "v"(k4), "v"(v4), "v"(q4), "v"(m4), "v"(sx5), "v"(sq5), "v"(sr5));
      SB(); }
    STAMP(13); AQ(7);
    float n0 = 0.f, n1 = 0.f;
#pragma unroll
    for (int i = 0; i < 8; ++i) { n0 += kk[i] * kk[i]; n1 += kk[8 + i] * kk[8 + i]; }
    n0 = swap16max(max16(swapadd(n0))); n1 = swap16max(max16(swapadd(n1)));
    asm volatile("s_waitcnt vmcnt(0)" :: "{v200}"(wm0), "v"(wm1) : "memory");
    STAMP(14); AQ(8); AQ(9); AQD();
    st128(rws, l == 0 ? WS_GR + ((cb * 2 + hp) * 64 + t32) * GRS : 0x7ff00000u, (u32x4){tag[0], tag[1], __float_as_uint(n0), __float_as_uint(n1)});
  }
  STAMP(3); STAMPC(41);
}

#define XW4 1
#define XGE 1
#define HU0 52
#define HU1 114
DI void hint(rsrc_t rs, unsigned off) {
  asm volatile("s_mov_b64 exec, 1\n\tbuffer_atomic_add %0, %1, %2, 0 offen sc1\n\ts_mov_b64 exec, -1" ::"v"(1u), "v"(off), "s"(rs) : "memory");
}
DI void polltag(rsrc_t rs, unsigned off, u32x2 tag) {
#pragma unroll 1
  for (unsigned sp = 0; sp < SPIN; ++sp) {
    u32x2 v;
    asm volatile("buffer_load_dwordx2 %0, %1, %2, 0 offen sc1\n\ts_waitcnt vmcnt(0)" : "=v"(v) : "v"(off), "s"(rs) : "memory");
    if (__all(off >= 0x7ff00000u || (v[0] == tag[0] && v[1] == tag[1]))) break;
    __builtin_amdgcn_s_sleep(1);
  }
}
DI void spoll8(rsrc_t rs, unsigned o, unsigned t) {
#pragma unroll 1
  for (unsigned sp = 0; sp < SPIN; ++sp) {
    u32x8 a;
    asm volatile("s_buffer_load_dwordx8 %0, %1, %2 glc\n\ts_waitcnt lgkmcnt(0)" : "=s"(a) : "s"(rs), "s"(o) : "memory");
    if ((((a[0] ^ t) | (a[1] ^ t)) | ((a[2] ^ t) | (a[3] ^ t)) | ((a[4] ^ t) | (a[5] ^ t)) | ((a[6] ^ t) | (a[7] ^ t))) == 0u) break;
  }
}
DI unsigned sld(rsrc_t rs, unsigned off) { unsigned v; asm volatile("s_buffer_load_dword %0, %1, %2 glc" : "=s"(v) : "s"(rs), "s"(off) : "memory"); return v; }
DI void sgot(unsigned& v) { asm volatile("s_waitcnt lgkmcnt(0)" : "+s"(v) :: "memory"); }
DI void spoll(rsrc_t rs, unsigned o0, unsigned o1, unsigned need) {
#pragma unroll 1
  for (unsigned sp = 0; sp < SPIN; ++sp) {
    unsigned a, c;
    asm volatile("s_buffer_load_dword %0, %2, %3 glc\n\ts_buffer_load_dword %1, %2, %4 glc\n\ts_waitcnt lgkmcnt(0)" : "=&s"(a), "=&s"(c) : "s"(rs), "s"(o0), "s"(o1) : "memory");
    if (a >= need && c >= need) break;
  }
}
DI unsigned mx4(unsigned m, const bf16x8& f) { const u32x4 u = __builtin_bit_cast(u32x4, f); return max(max(max(max(m, u[0]), u[1]), u[2]), u[3]); }
template <int N> DI void got1(bf16x8& f) { asm volatile("s_waitcnt vmcnt(%1)" : "+v"(f) : "i"(N) : "memory"); }
DI void fin_stats(const bf16x8 (&t)[4], float& mu, float& var) {
  float s = 0.f, q = 0.f;
#pragma unroll
  for (int i = 0; i < 4; ++i) { const f32x4 v = __builtin_bit_cast(f32x4, t[i]); s += v[0] + v[2]; q += v[1] + v[3]; }
  s = swapadd(s); q = swapadd(q);
  mu = s * (1.f / D); var = fmaxf(q * (1.f / D) - mu * mu, 0.f);
  TOUCH(mu);
}
DI void ld_stats(bf16x8 (&t)[4], rsrc_t rs, unsigned vs, unsigned so) { ldn<0>(t[0], rs, vs, so); ldn<512>(t[1], rs, vs, so); ldn<1024>(t[2], rs, vs, so); ldn<1536>(t[3], rs, vs, so); }
template <int N, int O0, int O1, int O2, int O3> DI void take4(bf16x8 (&t)[4], rsrc_t rs, unsigned voff, unsigned sa, unsigned sb) {
  got4<N>(t);
#pragma unroll 1
  for (unsigned sp = 0; sp < SPIN && __any(mx4(mx4(mx4(mx4(0u, t[0]), t[1]), t[2]), t[3]) == MARK); ++sp)
    asm volatile("buffer_load_dwordx4 %0, %4, %5, %6 offen offset:%8 sc1\n\tbuffer_load_dwordx4 %1, %4, %5, %6 offen offset:%9 sc1\n\t"
                 "buffer_load_dwordx4 %2, %4, %5, %7 offen offset:%10 sc1\n\tbuffer_load_dwordx4 %3, %4, %5, %7 offen offset:%11 sc1\n\ts_waitcnt vmcnt(0)"
                 : "=&v"(t[0]), "=&v"(t[1]), "=&v"(t[2]), "=&v"(t[3]) : "v"(voff), "s"(rs), "s"(sa), "s"(sb), "i"(O0), "i"(O1), "i"(O2), "i"(O3) : "memory");
}
template <int N, int O> DI void take2(bf16x8 (&t)[2], rsrc_t rs, unsigned voff, unsigned sa) {
  asm volatile("s_waitcnt vmcnt(%2)" : "+v"(t[0]), "+v"(t[1]) : "i"(N) : "memory");
#pragma unroll 1
  for (unsigned sp = 0; sp < SPIN && __any(mx4(mx4(0u, t[0]), t[1]) == MARK); ++sp)
    asm volatile("buffer_load_dwordx4 %0, %2, %3, %4 offen offset:%5 sc1\n\tbuffer_load_dwordx4 %1, %2, %3, %4 offen offset:%6 sc1\n\ts_waitcnt vmcnt(0)"
                 : "=&v"(t[0]), "=&v"(t[1]) : "v"(voff), "s"(rs), "s"(sa), "i"(O), "i"(O + 1024) : "memory");
}
DI void undef2(bf16x8 (&t)[2]) { asm volatile("" : "=v"(t[0]), "=v"(t[1])); }
DI void ld4s(bf16x8 (&t)[4], rsrc_t rs, unsigned vo, unsigned so, unsigned on) {
  asm volatile("s_cmp_eq_u32 %7, 0\n\ts_cbranch_scc1 1f\n\tbuffer_load_dwordx4 %0, %4, %5, %6 offen sc1\n\tbuffer_load_dwordx4 %1, %4, %5, %6 offen offset:512 sc1\n\t"
               "buffer_load_dwordx4 %2, %4, %5, %6 offen offset:1024 sc1\n\tbuffer_load_dwordx4 %3, %4, %5, %6 offen offset:1536 sc1\n1:"
               : "+v"(t[0]), "+v"(t[1]), "+v"(t[2]), "+v"(t[3]) : "v"(vo), "s"(rs), "s"(so), "s"(on) : "memory", "scc");
}
DI void touch4(unsigned& d, rsrc_t rs, unsigned vo, unsigned so) {
  asm volatile("buffer_load_dword %0, %1, %2, %3 offen sc1\n\tbuffer_load_dword %0, %1, %2, %4 offen sc1\n\tbuffer_load_dword %0, %1, %2, %5 offen sc1\n\tbuffer_load_dword %0, %1, %2, %6 offen sc1"
               : "+v"(d) : "v"(vo), "s"(rs), "s"(so + 28672), "s"(so + 2 * 28672), "s"(so + 3 * 28672), "s"(so) : "memory");
}
DI void keep4(u32x2 (&r)[4]) { asm volatile("" : "+v"(r[0]), "+v"(r[1]), "+v"(r[2]), "+v"(r[3])); }
DI void ld4x2(u32x2 (&r)[4], rsrc_t rs, unsigned vo, unsigned a, unsigned b, unsigned c, unsigned d) {
  asm volatile("buffer_load_dwordx2 %0, %4, %5, %6 offen sc1\n\tbuffer_load_dwordx2 %1, %4, %5, %7 offen sc1\n\tbuffer_load_dwordx2 %2, %4, %5, %8 offen sc1\n\tbuffer_load_dwordx2 %3, %4, %5, %9 offen sc1"
               : "=&{v[232:233]}"(r[0]), "=&{v[234:235]}"(r[1]), "=&{v[236:237]}"(r[2]), "=&{v[238:239]}"(r[3]) : "v"(vo), "s"(rs), "s"(a), "s"(b), "s"(c), "s"(d) : "memory");
}
DI unsigned lwaitv(unsigned a, unsigned need) {
  unsigned v;
#pragma unroll 1
  for (unsigned sp = 0; sp < SPIN; ++sp) { v = rfl(lcnt(a)); if (v >= need) break; __builtin_amdgcn_s_sleep(1); }
  asm volatile("" ::: "memory");
  return v;
}
template <unsigned N, unsigned A> DI void waity(unsigned a) {
#pragma unroll 1
  for (unsigned sp = 0; sp < SPIN; ++sp) {
    const u32x4 v = *(const volatile LAS u32x4*)(uintptr_t)a;
    if (rfl(min(min(v[0], v[1]), min(v[2], v[3] + A))) >= N) break;
    __builtin_amdgcn_s_sleep(1);
  }
  asm volatile("" ::: "memory");
}
DI unsigned spollq1(rsrc_t rs, unsigned o, unsigned need) {
  unsigned a, b, c, d;
  asm volatile("s_buffer_load_dword %0, %4, %5 glc\n\ts_buffer_load_dword %1, %4, %6 glc\n\ts_buffer_load_dword %2, %4, %7 glc\n\ts_buffer_load_dword %3, %4, %8 glc\n\ts_waitcnt lgkmcnt(0)\n\t"
               "s_min_u32 %0, %0, %1\n\ts_min_u32 %2, %2, %3\n\ts_min_u32 %0, %0, %2\n\ts_cmp_lt_u32 %0, %9\n\ts_cselect_b32 %0, 1, 0"
               : "=&s"(a), "=&s"(b), "=&s"(c), "=&s"(d) : "s"(rs), "s"(o), "s"(o + 128), "s"(o + 256), "s"(o + 384), "s"(need) : "memory", "scc");
  return a;
}
DI void sq_ld(unsigned (&q)[4], rsrc_t rs, unsigned o) {
  asm volatile("s_buffer_load_dword %0, %4, %5 glc\n\ts_buffer_load_dword %1, %4, %6 glc\n\ts_buffer_load_dword %2, %4, %7 glc\n\ts_buffer_load_dword %3, %4, %8 glc"
               : "=&s"(q[0]), "=&s"(q[1]), "=&s"(q[2]), "=&s"(q[3]) : "s"(rs), "s"(o), "s"(o + 128), "s"(o + 256), "s"(o + 384) : "memory");
}
DI unsigned sq_min(unsigned (&q)[4]) { asm volatile("s_waitcnt lgkmcnt(0)" : "+s"(q[0]), "+s"(q[1]), "+s"(q[2]), "+s"(q[3]) :: "memory"); return min(min(q[0], q[1]), min(q[2], q[3])); }
DI void spollq(rsrc_t rs, unsigned o, unsigned need) {
#pragma unroll 1
  for (unsigned sp = 0; sp < SPIN && spollq1(rs, o, need); ++sp) {}
}
DI void keep7(bf16x8 (&t)[7]) { asm volatile("" : "+v"(t[0]), "+v"(t[1]), "+v"(t[2]), "+v"(t[3]), "+v"(t[4]), "+v"(t[5]), "+v"(t[6])); }
DI void keep2(u32x2 (&r)[2]) { asm volatile("" : "+v"(r[0]), "+v"(r[1])); }
DI void get_stats(bf16x8 (&t)[4], rsrc_t rs, unsigned vs, unsigned so) { take4<0, 0, 512, 1024, 1536>(t, rs, vs, so, so); }
DI void put_stats(rsrc_t rs, unsigned vl, unsigned soff, const f32x16& v) {
  f32x2 s = {v[0], v[1]}, q = s * s;
#pragma unroll
  for (int i = 2; i < 16; i += 2) { const f32x2 t = {v[i], v[i + 1]}; s += t; q += t * t; }
  float sa = s[0], sb = s[1], qa = q[0], qb = q[1];
  TOUCH(sa); TOUCH(qa);
  const float s1 = swapadd(sa + sb), q1 = swapadd(qa + qb);
  if (vl < 512) __builtin_amdgcn_raw_buffer_store_b64((u32x2){__float_as_uint(s1), __float_as_uint(q1)}, rs, vl, soff, 16);
}
DI u32x16 cat4(const bf16x8 (&w)[32], int i) {
  const u32x4 a = __builtin_bit_cast(u32x4, w[i]), b = __builtin_bit_cast(u32x4, w[i + 1]), c = __builtin_bit_cast(u32x4, w[i + 2]), d = __builtin_bit_cast(u32x4, w[i + 3]);
  return __builtin_shufflevector(__builtin_shufflevector(a, b, 0, 1, 2, 3, 4, 5, 6, 7), __builtin_shufflevector(c, d, 0, 1, 2, 3, 4, 5, 6, 7), 0, 1, 2, 3, 4, 5, 6, 7, 8, 9, 10, 11, 12, 13, 14, 15);
}
struct UPart { u32x16 a, b, c; u32x8 d; };
#define U_E(g) "+{v[176:191]}"(g.a), "+{v[192:207]}"(g.b)
#define U_O(g) "+{v[208:223]}"(g.c), "+{v[224:231]}"(g.d)
#define U_LD(r, so, o) "buffer_load_dwordx4 v[" #r ":" #r "+3], %[vo], %[rs], %[" #so "] offen offset:" #o "*1024 sc1\n\t"
#define U_LD8(so) U_LD(176, so, 0) U_LD(180, so, 1) U_LD(184, so, 2) U_LD(188, so, 3) "s_add_u32 %[t], %[" #so "], 0x1000\n\t" U_LD(192, t, 0) U_LD(196, t, 1) U_LD(200, t, 2) U_LD(204, t, 3)
#define U_LD6(so) U_LD(208, so, 0) U_LD(212, so, 1) U_LD(216, so, 2) U_LD(220, so, 3) "s_add_u32 %[t], %[" #so "], 0x1000\n\t" U_LD(224, t, 0) U_LD(228, t, 1)
#define U_IF(on, L) "s_cmp_eq_u32 %[" #on "], 0\n\ts_cbranch_scc1 " #L "f\n"
#define U_WT(n) "s_waitcnt vmcnt(%[" #n "])\n\ts_cmp_ge_u32 %[nw], %[" #n "]\n\ts_cbranch_scc1 5f\n\t"
#define U_M(c, w, g, C) "v_mfma_f32_32x32x16_bf16 v[" #c ":" #c "+15], v[" #w ":" #w "+3], v[" #g ":" #g "+3], " C "\n\t"
#define U_X(g) "v_max3_u32 v240, v240, v[" #g "], v[" #g "+1]\n\tv_max3_u32 v241, v241, v[" #g "+2], v[" #g "+3]\n\t"
#define U_PAIR(w, g, C0, C1, X0, X1) U_M(0, w, g, C0) X0 U_M(16, w+4, g, C1) U_M(0, w+8, g+4, "v[0:15]") X1 U_M(16, w+12, g+4, "v[16:31]")
#define U_AGAIN "v_max_u32 v240, v240, v241\n\ts_sub_u32 %[tr], %[tr], 1\n\ts_cselect_b64 %[m], 0, -1\n\tv_cmp_eq_u32 vcc, 0xff81ff81, v240\n\ts_mov_b32 %[nx], 0\n\ts_mov_b32 %[nw], 0\n\t" \
                "s_and_b64 %[m], %[m], vcc\n\ts_cbranch_scc1 2b\n\t"
#define U_FL "s_buffer_load_dwordx4 %[fl], %[rs], %[fw] glc\n\t"
#define U_S [fl] "=&s"(fl), [t] "=&s"(t), [m] "=&s"(m), [tr] "+s"(tr), [nx] "+s"(nx), [nw] "+s"(nw)
#define U_I [fw] "s"(fw), [vo] "v"(vo), [rs] "s"(rs), [so] "s"(so), [sn] "s"(sn), [on] "s"(on), [w1] "i"(W1), [w2] "i"(W2), [w3] "i"(W3)
DI void undefu(UPart& g) { asm volatile("" : "={v[176:191]}"(g.a), "={v[192:207]}"(g.b), "={v[208:223]}"(g.c), "={v[224:231]}"(g.d)); }
DI void ld8if(UPart& g, rsrc_t rs, unsigned vo, unsigned so, unsigned on) { unsigned t; asm volatile(U_IF(on, 1) U_LD8(so) "1:" : U_E(g), [t] "=&s"(t) : [vo] "v"(vo), [rs] "s"(rs), [so] "s"(so), [on] "s"(on) : "memory", "scc"); }
DI void ld6if(UPart& g, rsrc_t rs, unsigned vo, unsigned so, unsigned on) { unsigned t; asm volatile(U_IF(on, 1) U_LD6(so) "1:" : U_O(g), [t] "=&s"(t) : [vo] "v"(vo), [rs] "s"(rs), [so] "s"(so), [on] "s"(on) : "memory", "scc"); }
#define U_RD(r, a, o) "ds_read_b128 v[" #r ":" #r "+3], %[" #a "] offset:" #o "*1024\n\t"
DI void rd8if(UPart& g, unsigned a, unsigned on) {
  asm volatile(U_IF(on, 1) U_RD(176, a, 0) U_RD(180, a, 1) U_RD(184, a, 2) U_RD(188, a, 3) U_RD(192, a, 4) U_RD(196, a, 5) U_RD(200, a, 6) U_RD(204, a, 7) "1:" : U_E(g) : [a] "v"(a), [on] "s"(on) : "memory", "scc");
}
DI void rd6if(UPart& g, unsigned a, unsigned on) {
  asm volatile(U_IF(on, 1) U_RD(208, a, 0) U_RD(212, a, 1) U_RD(216, a, 2) U_RD(220, a, 3) U_RD(224, a, 4) U_RD(228, a, 5) "1:" : U_O(g) : [a] "v"(a), [on] "s"(on) : "memory", "scc");
}
#define U_L(r, o) "buffer_load_dwordx4 v[" #r ":" #r "+3], %[vo], %[rs], %[t] offen offset:" #o "*1024 sc1\n\t"
#define U_T(o) "s_add_u32 %[t], %[ub], " #o "\n\t"
#define U_G0 U_T(0) U_L(176, 0) U_L(180, 1) U_L(184, 2) U_L(188, 3) U_T(4096) U_L(192, 0) U_L(196, 1) U_L(200, 2) U_L(204, 3)
#define U_G1 U_T(8192) U_L(208, 0) U_L(212, 1) U_L(216, 2) U_L(220, 3) U_T(12288) U_L(224, 0) U_L(228, 1)
#define U_G2 U_T(14336) U_L(176, 0) U_L(180, 1) U_L(184, 2) U_L(188, 3) U_T(18432) U_L(192, 0) U_L(196, 1) U_L(200, 2) U_L(204, 3)
#define U_F(o) "s_add_u32 %[f], %[fw], " #o "\n\ts_buffer_load_dword %[f], %[rs], %[f] glc\n\t"
#define U_TRY "s_max_i32 %[tr], %[tr], 0\n\ts_sub_u32 %[tr], %[tr], 1\n\t"
#define U_POLL(L, E, o, n) #L ":\n\t" U_F(o) U_TRY "s_waitcnt lgkmcnt(0)\n\ts_cbranch_scc1 " #E "f\n\ts_cmp_ge_u32 %[f], " #n "\n\ts_cbranch_scc0 " #L "b\n" #E ":\n\t"
#define U_Q(w, g, C0, C1, X0, X1, X2, X3) U_M(0, w, g, C0) X0 U_M(16, w+4, g, C1) X1 U_M(0, w+8, g+4, "v[0:15]") X2 U_M(16, w+12, g+4, "v[16:31]") X3
#define U_C(w, g, X0, X1, X2, X3) U_Q(w, g, "v[0:15]", "v[16:31]", X0, X1, X2, X3)
#define U_BAD(L) "v_max_u32 v240, v240, v241\n\t" U_TRY "s_cselect_b64 %[m], 0, -1\n\tv_cmp_eq_u32 vcc, 0xff81ff81, v240\n\ts_and_b64 %[m], %[m], vcc\n\ts_cbranch_scc1 " #L "\n\t"
#define U_Z "v_mov_b32 v240, 0\n\tv_mov_b32 v241, 0\n\t"
#define U_PUT(i) "v_xor_b32 v[240+(" #i "&1)], 32*" #i ", %[pa]\n\tds_write_b128 v[240+(" #i "&1)], v[4*" #i ":4*" #i "+3] offset:" #i "*1024\n\t"
#define U_SECOND(Y0, Y1, Y2, Y3, Y4, Y5, Y6) U_Q(32, 176, "0", "0", U_X(176), Y0, U_X(180), Y1) U_C(48, 184, U_X(184), Y2, U_X(188), Y3) U_C(64, 192, U_X(192), Y4, U_X(196), Y5) U_C(80, 200, U_X(200), Y6, U_X(204), "")
DI void uthree(f32x16& c0, f32x16& c1, UPart& g, const u32x16 (&wq)[4], const bf16x8 (&wf)[32], rsrc_t rs, unsigned vo, unsigned ub, unsigned fw, unsigned md, unsigned la, unsigned need,
               unsigned ua, unsigned uc, unsigned pa, unsigned cpa, unsigned& f, unsigned& m3) {
  unsigned t, tr = SPIN; unsigned long long m;
  asm volatile("s_bitcmp1_b32 %[md], 0\n\ts_cbranch_scc1 60f\n"
               "10:\n\tds_read_b32 v240, %[la]\n\ts_waitcnt lgkmcnt(0)\n\tv_readfirstlane_b32 %[t], v240\n\ts_cmp_ge_u32 %[t], %[nd]\n\ts_cbranch_scc0 61f\n"
               "11:\n\ts_bitcmp1_b32 %[md], 1\n\ts_cbranch_scc0 20f\n\t"
               U_RD(176, ua, 0) U_RD(180, ua, 1) U_RD(184, ua, 2) U_RD(188, ua, 3) U_RD(192, ua, 4) U_RD(196, ua, 5) U_RD(200, ua, 6) U_RD(204, ua, 7)
               U_RD(208, uc, 0) U_RD(212, uc, 1) U_RD(216, uc, 2) U_RD(220, uc, 3) U_RD(224, uc, 4) U_RD(228, uc, 5)
               "20:\n\t" U_Z "s_waitcnt vmcnt(6) lgkmcnt(0)\n\t" U_F(8)
               U_Q(32, 176, "0", "0", U_X(176), U_X(200), U_X(180), "") U_C(48, 184, U_X(184), U_X(204), U_X(188), "") U_C(64, 192, U_X(192), "", U_X(196), "s_waitcnt vmcnt(0)\n\t")
               U_C(80, 200, U_X(208) U_X(212), U_X(216) U_X(220), U_X(224) U_X(228), "") U_BAD(62f)
               "s_waitcnt lgkmcnt(0)\n\ts_cmp_ge_u32 %[f], 4\n\ts_cselect_b32 %[m3], 1, 0\n\ts_cbranch_scc0 63f\n\t"
               U_C(128, 208, U_T(14336) U_L(176, 0), U_L(180, 1), U_L(184, 2), U_L(188, 3)) U_C(144, 216, U_T(18432) U_L(192, 0), U_L(196, 1) U_F(12), U_L(200, 2), U_L(204, 3)) U_C(160, 224, "", "", "", "")
               "30:\n\ts_nop 7\n\ts_nop 2\n\t" U_PUT(0) U_PUT(1) U_PUT(2) U_PUT(3) U_PUT(4) U_PUT(5) U_PUT(6) U_PUT(7)
               "v_mov_b32 v241, 1\n\ts_mov_b64 exec, 1\n\tds_add_u32 %[cpa], v241\n\ts_mov_b64 exec, -1\n\ts_cmp_eq_u32 %[m3], 0\n\ts_cbranch_scc1 64f\n"
               "40:\n\ts_waitcnt vmcnt(0) lgkmcnt(0)\n\t" U_Z "s_cmp_ge_u32 %[f], 3\n\ts_cselect_b32 %[m3], 0, 1\n\ts_cbranch_scc0 65f\n\t"
               U_SECOND(U_T(22528) U_L(208, 0), U_L(212, 1), U_L(216, 2), U_L(220, 3), U_T(26624) U_L(224, 0), U_L(228, 1), "") U_BAD(66f) "s_branch 99f\n"
               "60:\n\ts_bitcmp1_b32 %[md], 2\n\ts_cbranch_scc0 72f\n\ts_bitcmp1_b32 %[md], 3\n\ts_cbranch_scc1 71f\n" U_POLL(70, 71, 0, 4) U_G0
               "72:\n\ts_bitcmp1_b32 %[md], 4\n\ts_cbranch_scc1 74f\n" U_POLL(73, 74, 4, 3) U_G1 "s_branch 10b\n"
               "61:\n\t" U_TRY "s_sleep 1\n\ts_cbranch_scc0 10b\n\ts_branch 11b\n"
               "62:\n\t" U_G0 U_G1 "s_waitcnt vmcnt(0)\n\ts_branch 20b\n"
               "63:\n\t" U_C(128, 208, "", "", "", "") U_C(144, 216, "", "", "", "") U_C(160, 224, "", "", "", "") "s_branch 30b\n"
               "64:\n" U_POLL(75, 76, 8, 4) U_G2 U_F(12) "s_branch 40b\n"
               "66:\n\t" U_G2 "s_branch 40b\n"
               "65:\n\t" U_F(12) U_SECOND("", "", "", "", "", "", "") U_BAD(66b)
               "99:"
               : "=&{v[0:15]}"(c0), "=&{v[16:31]}"(c1), U_E(g), U_O(g), [f] "=&s"(f), [m3] "=&s"(m3), [t] "=&s"(t), [m] "=&s"(m), [tr] "+s"(tr)
               : "{v[32:47]}"(wq[0]), "{v[48:63]}"(wq[1]), "{v[64:79]}"(wq[2]), "{v[80:95]}"(wq[3]), "{v[128:143]}"(cat4(wf, 0)), "{v[144:159]}"(cat4(wf, 4)), "{v[160:175]}"(cat4(wf, 8)),
                 [vo] "v"(vo), [rs] "s"(rs), [ub] "s"(ub), [fw] "s"(fw), [md] "s"(md), [la] "v"(la), [nd] "s"(need), [ua] "v"(ua), [uc] "v"(uc), [pa] "v"(pa), [cpa] "v"(cpa)
               : "memory", "vcc", "scc", "v240", "v241");
}
#define U_W(n) "s_waitcnt vmcnt(" #n "+%[ex])\n\t"
#define U_PB(i) "v_xor_b32 v[240+(" #i "&1)], 32*" #i ", %[pb]\n\tds_write_b128 v[240+(" #i "&1)], v[32+4*" #i ":32+4*" #i "+3] offset:" #i "*1024\n\t"
#define U_FIN(P0, P1, P2, P3, P4) "s_branch 3f\n2:\n\t" U_LD6(so) "s_waitcnt vmcnt(0)\n3:\n\t" U_Z                                                                        \
  U_W(5) U_M(32, 128, 208, "v[0:15]") U_X(208) U_W(4) U_M(32, 136, 212, "v[32:47]") U_X(212) U_W(3) U_M(32, 144, 216, "v[32:47]") U_X(216)                                \
  U_W(2) U_M(32, 152, 220, "v[32:47]") U_X(220) U_W(1) U_M(32, 160, 224, "v[32:47]") U_X(224) U_W(0) U_M(32, 168, 228, "v[32:47]") U_X(228)                               \
  U_M(48, 132, 208, "v[16:31]") U_BAD(2b) U_M(48, 140, 212, "v[48:63]") U_M(48, 148, 216, "v[48:63]") P0 U_M(48, 156, 220, "v[48:63]") P1                                 \
  U_M(48, 164, 224, "v[48:63]") P2 U_M(48, 172, 228, "v[48:63]") P3 "s_nop 7\n\ts_nop 3\n\t" P4
#define U_FO U_O(g), "=&{v[32:47]}"(d0), "=&{v[48:63]}"(d1), [t] "=&s"(t), [m] "=&s"(m), [tr] "+s"(tr)
#define U_FI "{v[0:15]}"(c0), "{v[16:31]}"(c1), "{v[128:143]}"(cat4(wf, 0)), "{v[144:159]}"(cat4(wf, 4)), "{v[160:175]}"(cat4(wf, 8)), [vo] "v"(vo), [rs] "s"(rs), [so] "s"(so), [pb] "v"(pb), [ex] "i"(PUT)
template <int PUT> DI void ufin(const f32x16& c0, const f32x16& c1, f32x16& d0, f32x16& d1, UPart& g, const bf16x8 (&wf)[32], rsrc_t rs, unsigned vo, unsigned so, unsigned pb) {
  unsigned t, tr = SPIN; unsigned long long m;
  if constexpr (PUT) asm volatile(U_FIN(U_PB(0), U_PB(1), U_PB(2), U_PB(3), U_PB(4) U_PB(5) U_PB(6) U_PB(7)) : U_FO : U_FI : "memory", "vcc", "scc", "v240", "v241");
  else asm volatile(U_FIN("", "", "", "", "") : U_FO : U_FI : "memory", "vcc", "scc", "v240", "v241");
}
DI void uput(const f32x16& d0, const f32x16& d1, unsigned pb) { asm volatile(U_PB(0) U_PB(1) U_PB(2) U_PB(3) U_PB(4) U_PB(5) U_PB(6) U_PB(7) :: "{v[32:47]}"(d0), "{v[48:63]}"(d1), [pb] "v"(pb) : "memory", "v240", "v241"); }
#define X_P(i, n) "s_waitcnt vmcnt(" #n ")\n\tds_write_b128 %[a], %[f" #i "] offset:" #i "*1024\n\t"
template <int N> DI void stage4(bf16x8 (&t)[4], rsrc_t rs, unsigned voff, unsigned so, unsigned a) {
  asm volatile(X_P(0, 3+%[n]) X_P(1, 2+%[n]) X_P(2, 1+%[n]) X_P(3, %[n]) : [f0] "+v"(t[0]), [f1] "+v"(t[1]), [f2] "+v"(t[2]), [f3] "+v"(t[3]) : [a] "v"(a), [n] "i"(N) : "memory");
#pragma unroll 1
  for (unsigned sp = 0; sp < SPIN && __any(mx4(mx4(mx4(mx4(0u, t[0]), t[1]), t[2]), t[3]) == MARK); ++sp)
    asm volatile("s_waitcnt lgkmcnt(0)\n\tbuffer_load_dwordx4 %[f0], %[vo], %[rs], %[so] offen sc1\n\tbuffer_load_dwordx4 %[f1], %[vo], %[rs], %[so] offen offset:1024 sc1\n\t"
                 "buffer_load_dwordx4 %[f2], %[vo], %[rs], %[so] offen offset:2048 sc1\n\tbuffer_load_dwordx4 %[f3], %[vo], %[rs], %[so] offen offset:3072 sc1\n\t" X_P(0, 3) X_P(1, 2) X_P(2, 1) X_P(3, 0)
                 : [f0] "=&v"(t[0]), [f1] "=&v"(t[1]), [f2] "=&v"(t[2]), [f3] "=&v"(t[3]) : [vo] "v"(voff), [rs] "s"(rs), [so] "s"(so), [a] "v"(a) : "memory");
}
#define W_LD(r, s, o) "buffer_load_dwordx4 v[" #r ":" #r "+3], %[vo], %[rs], %[" #s "] offen offset:%[" #o "] sc1\n\t"
#define W_Q(K, R, E) if constexpr (Q == K) asm volatile(U_IF(on, 1) W_LD(R, s0, o0) W_LD(R+4, s1, o0) W_LD(R+8, s2, o1) W_LD(R+12, s3, o1) "1:" : "+{v[" #R ":" #E "]}"(q) \
    : [vo] "v"(vo), [rs] "s"(rs), [s0] "s"(s0), [s1] "s"(s1), [s2] "s"(s2), [s3] "s"(s3), [on] "s"(on), [o0] "i"(O0), [o1] "i"(O1) : "memory", "scc")
template <int Q, int O0, int O1> DI void ldq(u32x16& q, rsrc_t rs, unsigned vo, unsigned s0, unsigned s1, unsigned s2, unsigned s3, unsigned on) { W_Q(0, 32, 47); W_Q(1, 48, 63); W_Q(2, 64, 79); W_Q(3, 80, 95); }
DI void keepq(u32x16 (&q)[4]) { asm volatile("" : "+{v[32:47]}"(q[0]), "+{v[48:63]}"(q[1]), "+{v[64:79]}"(q[2]), "+{v[80:95]}"(q[3])); }
DI void undefq(u32x16 (&q)[4]) { asm volatile("" : "={v[32:47]}"(q[0]), "={v[48:63]}"(q[1]), "={v[64:79]}"(q[2]), "={v[80:95]}"(q[3])); }
#define K_CNT "ds_read_b32 v48, %[qa]\n\t"
#define K_RD(r, k, A) "ds_read_b128 v[32+4*" #r ":32+4*" #r "+3], %[" #A "] offset:" #k "*1024\n\t"
#define K_RD4(A) K_RD(0, 0, A) K_RD(1, 1, A) K_RD(2, 2, A) K_RD(3, 3, A)
#define K_WAIT(L, j, A, R) #L ":\n\t" K_CNT "s_max_i32 %[tr], %[tr], 0\n\ts_sub_u32 %[tr], %[tr], 1\n\ts_cselect_b32 %[m], -1, 0\n\ts_waitcnt lgkmcnt(0)\n\tv_cmp_le_u32 vcc, %[nd], v48\n\t" \
                           "s_or_b32 %[m], %[m], vcc_lo\n\ts_bitcmp1_b32 %[m], " #j "\n\ts_cbranch_scc0 " #L "b\n\t" K_RD4(A) "s_branch " #R "f\n"
#define K_M(W, k, r) "v_mfma_f32_32x32x16_bf16 v[16:31], v[" #W "+4*" #k ":" #W "+4*" #k "+3], v[32+4*" #r ":32+4*" #r "+3], v[16:31]\n\t"
#define K_S(W, k, r, n, nk, A) "s_waitcnt lgkmcnt(" #n ")\n\t" K_M(W, k, r) K_RD(r, nk, A)
#define K_Z(W, k, r, n) "s_waitcnt lgkmcnt(" #n ")\n\t" K_M(W, k, r)
#define K_BLK(W, A, N, j, L) K_S(W, 0, 0, 3, 4, A) K_S(W, 1, 1, 3, 5, A) K_S(W, 2, 2, 3, 6, A) K_S(W, 3, 3, 3, 7, A)                                             \
  K_CNT K_S(W, 4, 0, 4, 0, N) K_S(W, 5, 1, 4, 1, N) K_S(W, 6, 2, 4, 2, N) K_S(W, 7, 3, 4, 3, N)                                                                 \
  "s_waitcnt lgkmcnt(4)\n\tv_cmp_le_u32 vcc, %[nd], v48\n\ts_bitcmp1_b32 vcc_lo, " #j "\n\ts_cbranch_scc0 " #L "b\n"
#define K_LD(k, so, o) "buffer_load_dwordx4 v[64+4*" #k ":64+4*" #k "+3], %[vo], %[rs], " so " offen offset:" #o "*1024 sc1\n\t"
#define K_OWN(k, C) "s_waitcnt vmcnt(7-" #k "+%[ex])\n\tds_write_b128 %[a0], v[64+4*" #k ":64+4*" #k "+3] offset:" #k "*1024\n\t"                                  \
  "v_mfma_f32_32x32x16_bf16 v[0:15], v[128+4*" #k ":128+4*" #k "+3], v[64+4*" #k ":64+4*" #k "+3], " C "\n\t"                                                   \
  "v_max3_u32 v50, v50, v[64+4*" #k "], v[65+4*" #k "]\n\tv_max3_u32 v50, v50, v[66+4*" #k "], v[67+4*" #k "]\n\t"
#define K_ADD(lanes, a, o) "s_mov_b64 exec, " #lanes "\n\tds_add_u32 " a ", %[one] offset:" o "\n\ts_mov_b64 exec, -1\n\t"
#define K_BODY(MORE, MID, FIRST, LATE, H0, H1, H2, H3, END)                                                                                                     \
  "10:\n\t" K_LD(0, "%[so]", 0) K_LD(1, "%[so]", 1) K_LD(2, "%[so]", 2) K_LD(3, "%[so]", 3) "s_add_u32 %[m], %[so], 0x1000\n\t"                                  \
  K_LD(4, "%[m]", 0) K_LD(5, "%[m]", 1) K_LD(6, "%[m]", 2) K_LD(7, "%[m]", 3) MORE "v_mov_b32 v50, 0\n\ts_branch 20f\n"                                          \
  K_WAIT(11, 0, a1, 23) K_WAIT(12, 1, a2, 31) K_WAIT(13, 2, a3, 32) LATE                                                                                        \
  "20:\n\t" K_OWN(0, "v[16:31]") K_OWN(1, "v[0:15]") K_OWN(2, "v[0:15]") K_OWN(3, "v[0:15]") K_OWN(4, "v[0:15]") K_OWN(5, "v[0:15]") K_OWN(6, "v[0:15]")           \
  "s_waitcnt vmcnt(%[ex])\n\tds_write_b128 %[a0], v[92:95] offset:7168\n\tv_max3_u32 v50, v50, v92, v93\n\tv_max3_u32 v50, v50, v94, v95\n\t"                    \
  "v_cmp_eq_u32 vcc, 0xff81ff81, v50\n\ts_sub_u32 %[tr], %[tr], 1\n\ts_cbranch_scc1 21f\n\ts_cbranch_vccnz 10b\n21:\n\t"                                         \
  K_ADD(8, "%[qa]", "0") K_CNT K_RD4(a1) "v_mfma_f32_32x32x16_bf16 v[16:31], v[156:159], v[92:95], v[0:15]\n\t"                                                 \
  "s_waitcnt lgkmcnt(4)\n\tv_cmp_le_u32 vcc, %[nd], v48\n\ts_bitcmp1_b32 vcc_lo, 0\n\ts_cbranch_scc0 11b\n"                                                      \
  "23:\n\t" MID FIRST                                                                                                                                           \
  "30:\n\t" K_BLK(160, a1, a2, 1, 12)                                                                                                                           \
  "31:\n\t" K_BLK(192, a2, a3, 2, 13)                                                                                                                           \
  "32:\n\t" K_S(224, 0, 0, 3, 4, a3) H0 K_S(224, 1, 1, 3, 5, a3) K_S(224, 2, 2, 3, 6, a3) H1 K_S(224, 3, 3, 3, 7, a3)                                            \
  K_Z(224, 4, 0, 3) H2 K_Z(224, 5, 1, 2) K_Z(224, 6, 2, 1) H3 K_Z(224, 7, 3, 0) END
#define K_OUT "+{v[16:31]}"(ac), [m] "=&s"(m), [tr] "+s"(tr)
#define K_IN0 "{v[128:143]}"(cat4(wf, 0)), "{v[144:159]}"(cat4(wf, 4))
#define K_IN "{v[160:175]}"(cat4(wf, 8)), "{v[176:191]}"(cat4(wf, 12)), "{v[192:207]}"(cat4(wf, 16)), "{v[208:223]}"(cat4(wf, 20)), "{v[224:239]}"(cat4(wf, 24)), "{v[240:255]}"(cat4(wf, 28)), \
             [rs] "s"(rs), [vo] "v"(voff), [so] "s"(soff), [a0] "v"(qd[0]), [a1] "v"(qd[1]), [a2] "v"(qd[2]), [a3] "v"(qd[3]), [qa] "v"(qa), [one] "v"(one), [nd] "i"(ST + 1), [ex] "i"(ST == 1)
#define K_V8(a, b, c, d, e, f, g, h) "v" #a, "v" #b, "v" #c, "v" #d, "v" #e, "v" #f, "v" #g, "v" #h
#define K_CLOB "memory", "vcc", "scc", K_V8(0, 1, 2, 3, 4, 5, 6, 7), K_V8(8, 9, 10, 11, 12, 13, 14, 15), K_V8(32, 33, 34, 35, 36, 37, 38, 39), K_V8(40, 41, 42, 43, 44, 45, 46, 47),      \
               K_V8(48, 49, 50, 51, 56, 57, 58, 59), K_V8(64, 65, 66, 67, 68, 69, 70, 71), K_V8(72, 73, 74, 75, 76, 77, 78, 79), K_V8(80, 81, 82, 83, 84, 85, 86, 87), K_V8(88, 89, 90, 91, 92, 93, 94, 95)
#define K_HINT "s_mov_b64 exec, 1\n\tbuffer_atomic_add %[one], %[ho], %[rh], 0 offen sc1\n\ts_mov_b64 exec, -1\n\t"
#define EW 0
#define HP 0
#define K_HS K_HINT, "", "", ""
#define K_W(k, s, o) ""
#define K_BODY_(...) K_BODY(__VA_ARGS__)
template <int ST>
DI void kstage(f32x16& ac, bf16x8 (&wf)[32], rsrc_t rs, unsigned voff, unsigned soff, const unsigned (&qd)[4], unsigned qa, unsigned lm, unsigned one,
               bf16x8& st, unsigned ss, unsigned spa, unsigned long long& late, rsrc_t rh, unsigned ho) {
  unsigned m, tr = SPIN;
  if constexpr (ST == 0) asm volatile(K_BODY_("", "", "", "", K_HS, "s_nop 7\n\ts_nop 3") : K_OUT : K_IN0, K_IN, [rh] "s"(rh), [ho] "v"(ho) : K_CLOB);
  if constexpr (ST == 2) asm volatile(K_BODY_("", K_ADD(1, "%[lm]", "%[cg]"), "", "", K_HS, "s_nop 7\n\ts_nop 3") : K_OUT : K_IN0, K_IN, [lm] "v"(lm), [cg] "i"(C_G - L_M), [rh] "s"(rh), [ho] "v"(ho) : K_CLOB);
  if constexpr (ST == 1) {
    u32x16 wa = cat4(wf, 0), wb = cat4(wf, 4);
    asm volatile(K_BODY("buffer_load_dwordx4 v[52:55], %[vo], %[rs], %[ss] offen sc1\n\t", K_ADD(1, "%[lm]", "%[cg]"),
                        "s_waitcnt vmcnt(0)\n\tv_max_u32 v50, v52, v53\n\tv_max3_u32 v50, v50, v54, v55\n\tv_cmp_eq_u32 vcc, 0xff81ff81, v50\n\ts_mov_b64 %[lt], vcc\n\ts_cbranch_vccnz 14b\n\t"
                        "v_add_f32 v56, v52, v54\n\tv_add_f32 v57, v53, v55\n\tv_mov_b32 v58, v56\n\tv_mov_b32 v59, v57\n\ts_nop 1\n\t"
                        "v_permlane32_swap_b32 v56, v58\n\tv_permlane32_swap_b32 v57, v59\n\ts_nop 1\n\tv_add_f32 v56, v56, v58\n\tv_add_f32 v57, v57, v59\n\t"
                        "ds_write_b64 %[spa], v[56:57]\n\t" K_ADD(1, "%[lm]", "0"),
                        "14:\n\tbuffer_load_dwordx4 v[52:55], %[vo], %[rs], %[ss] offen sc1\n\ts_branch 30f\n",
                        K_W(0, s3, 0) K_W(1, s3, 1), K_W(2, s3, 2) K_W(3, s3, 3) "s_add_u32 %[m], %[s3], 0x1000\n\t", K_W(4, m, 0) K_W(5, m, 1), K_W(6, m, 2) K_W(7, m, 3), "")
                 : K_OUT, "=&{v[52:55]}"(st), [lt] "=&s"(late), "+{v[128:143]}"(wa), "+{v[144:159]}"(wb)
                 : K_IN, [lm] "v"(lm), [cg] "i"(C_H - L_M), [ss] "s"(ss), [spa] "v"(spa), [s3] "s"(ho) : K_CLOB);
#pragma unroll
    for (int i = 0; i < 4; ++i) {
      wf[i] = __builtin_bit_cast(bf16x8, (u32x4){wa[4 * i], wa[4 * i + 1], wa[4 * i + 2], wa[4 * i + 3]});
      wf[4 + i] = __builtin_bit_cast(bf16x8, (u32x4){wb[4 * i], wb[4 * i + 1], wb[4 * i + 2], wb[4 * i + 3]});
    }
  }
}
template <int OFF> DI void ld2if(bf16x8 (&r)[2], rsrc_t rs, unsigned voff, unsigned soff, unsigned on) {
  asm volatile("s_cmp_eq_u32 %5, 0\n\ts_cbranch_scc1 1f\n\tbuffer_load_dwordx4 %0, %2, %3, %4 offen offset:%6 sc1\n\tbuffer_load_dwordx4 %1, %2, %3, %4 offen offset:%7 sc1\n1:"
               : "+v"(r[0]), "+v"(r[1]) : "v"(voff), "s"(rs), "s"(soff), "s"(on), "i"(OFF), "i"(OFF + 1024) : "memory", "scc");
}
template <int A, int B, int C> DI void waitsel(unsigned md) {
  asm volatile("s_waitcnt vmcnt(%3)\n\ts_cmp_ge_u32 %0, 2\n\ts_cbranch_scc1 1f\n\ts_waitcnt vmcnt(%2)\n\ts_cmp_ge_u32 %0, 1\n\ts_cbranch_scc1 1f\n\ts_waitcnt vmcnt(%1)\n1:" :: "s"(md), "i"(A), "i"(B), "i"(C) : "memory", "scc");
}
template <int A, int B, int C, int D> DI void waitdyn(unsigned n) {
  asm volatile("s_waitcnt vmcnt(%4)\n\ts_cmp_ge_u32 %0, %4\n\ts_cbranch_scc1 1f\n\ts_waitcnt vmcnt(%3)\n\ts_cmp_ge_u32 %0, %3\n\ts_cbranch_scc1 1f\n\t"
               "s_waitcnt vmcnt(%2)\n\ts_cmp_ge_u32 %0, %2\n\ts_cbranch_scc1 1f\n\ts_waitcnt vmcnt(%1)\n1:" :: "s"(n), "i"(A), "i"(B), "i"(C), "i"(D) : "memory", "scc");
}
template <int A, int B> DI void waitdyn2(unsigned n) { asm volatile("s_waitcnt vmcnt(%2)\n\ts_cmp_ge_u32 %0, %2\n\ts_cbranch_scc1 1f\n\ts_waitcnt vmcnt(%1)\n1:" :: "s"(n), "i"(A), "i"(B) : "memory", "scc"); }
template <unsigned M> DI unsigned sbit(unsigned i) { unsigned r; asm volatile("s_lshr_b32 %0, %2, %1\n\ts_and_b32 %0, %0, 1" : "=s"(r) : "s"(i), "i"(M) : "scc"); return r; }
DI unsigned long long smask(unsigned on) { unsigned long long r; asm volatile("s_cmp_lg_u32 %1, 0\n\ts_cselect_b64 %0, -1, 0" : "=s"(r) : "s"(on) : "scc"); return r; }
template <unsigned N> DI unsigned sge(unsigned a) { unsigned r; asm volatile("s_cmp_ge_u32 %1, %2\n\ts_cselect_b32 %0, 1, 0" : "=s"(r) : "s"(a), "i"(N) : "scc"); return r; }
DI u32x4 sld4(rsrc_t rs, unsigned off) { u32x4 v; asm volatile("s_buffer_load_dwordx4 %0, %1, %2 glc" : "=s"(v) : "s"(rs), "s"(off) : "memory"); return v; }
DI void sld4if(u32x4& v, rsrc_t rs, unsigned off, unsigned on) { asm volatile("s_cmp_eq_u32 %3, 0\n\ts_cbranch_scc1 1f\n\ts_buffer_load_dwordx4 %0, %1, %2 glc\n1:" : "+s"(v) : "s"(rs), "s"(off), "s"(on) : "memory", "scc"); }
DI void sgot4(u32x4& v) { asm volatile("s_waitcnt lgkmcnt(0)" : "+s"(v) :: "memory"); }
DI u32x4 spoll4(rsrc_t rs, unsigned off, int i, unsigned need) {
  u32x4 v;
#pragma unroll 1
  for (unsigned sp = 0; sp < SPIN; ++sp) { v = sld4(rs, off); sgot4(v); if (v[i] >= need) break; }
  return v;
}
DI unsigned spoll1(rsrc_t rs, unsigned o0, unsigned o1, unsigned need) {
  unsigned a, c;
#pragma unroll 1
  for (unsigned sp = 0; sp < SPIN; ++sp) {
    asm volatile("s_buffer_load_dword %0, %2, %3 glc\n\ts_buffer_load_dword %1, %2, %4 glc\n\ts_waitcnt lgkmcnt(0)" : "=&s"(a), "=&s"(c) : "s"(rs), "s"(o0), "s"(o1) : "memory");
    if (a >= need) break;
  }
  return c;
}
template <int OFF> DI void ldif(bf16x8& r, rsrc_t rs, unsigned voff, unsigned soff, unsigned on) {
  asm volatile("s_cmp_eq_u32 %4, 0\n\ts_cbranch_scc1 1f\n\tbuffer_load_dwordx4 %0, %1, %2, %3 offen offset:%5 sc1\n1:" : "+v"(r) : "v"(voff), "s"(rs), "s"(soff), "s"(on), "i"(OFF) : "memory", "scc");
}
template <int O0, int O1, int O2> DI void ld3if(bf16x8& a, bf16x8& b, bf16x8& c, rsrc_t rs, unsigned voff, unsigned s0, unsigned s1, unsigned s2, unsigned on) {
  asm volatile("s_cmp_eq_u32 %8, 0\n\ts_cbranch_scc1 1f\n\tbuffer_load_dwordx4 %0, %3, %4, %5 offen offset:%9 sc1\n\tbuffer_load_dwordx4 %1, %3, %4, %6 offen offset:%10 sc1\n\t"
               "buffer_load_dwordx4 %2, %3, %4, %7 offen offset:%11 sc1\n1:" : "+v"(a), "+v"(b), "+v"(c) : "v"(voff), "s"(rs), "s"(s0), "s"(s1), "s"(s2), "s"(on), "i"(O0), "i"(O1), "i"(O2) : "memory", "scc");
}
DI void dmaif(rsrc_t rs, unsigned voff, unsigned soff, unsigned lds, unsigned on) {
  asm volatile("s_cmp_eq_u32 %4, 0\n\ts_cbranch_scc1 1f\n\ts_mov_b32 m0, %0\n\ts_nop 0\n\tbuffer_load_dwordx4 %1, %2, %3 offen sc1 lds\n1:" :: "s"(lds), "v"(voff), "s"(rs), "s"(soff), "s"(on) : "memory", "scc");
}
#define D_L(o) "buffer_load_dwordx4 %1, %2, %3 offen offset:" #o " sc1 lds\n\t"
template <int N> DI void dmaNif(rsrc_t rs, unsigned voff, unsigned soff, unsigned lds, unsigned on) {
  if constexpr (N == 2) asm volatile("s_cmp_eq_u32 %4, 0\n\ts_cbranch_scc1 1f\n\ts_mov_b32 m0, %0\n\ts_nop 0\n\t" D_L(0) D_L(1024) "1:" :: "s"(lds), "v"(voff), "s"(rs), "s"(soff), "s"(on) : "memory", "scc");
  else asm volatile("s_cmp_eq_u32 %4, 0\n\ts_cbranch_scc1 1f\n\ts_mov_b32 m0, %0\n\ts_nop 0\n\t" D_L(0) D_L(1024) D_L(2048) D_L(3072) "1:" :: "s"(lds), "v"(voff), "s"(rs), "s"(soff), "s"(on) : "memory", "scc");
}
DI void tile_dma(rsrc_t rs, unsigned voff, unsigned soff, unsigned lds) { dmaN<4>(rs, voff, soff, lds); dmaN<4>(rs, voff, soff + 4096, lds + 4096); }
DI u32x4 gelu8(const f32x16& a, int o, float k1, float k2, float ir) {
  float t0, t1, t2, t3, t4, t5, t6, t7;
#define G8(OP) OP(0, 8) OP(1, 9) OP(2, 10) OP(3, 11) OP(4, 12) OP(5, 13) OP(6, 14) OP(7, 15)
#define G_A(t, x) "v_mul_f32 %" #t ", %" #x ", %" #x "\n\t"
#define G_B(t, x) "v_fma_f32 %" #t ", %" #t ", %16, %17\n\t"
#define G_C(t, x) "v_mul_f32 %" #t ", %" #t ", %" #x "\n\t"
#define G_D(t, x) "v_exp_f32 %" #t ", %" #t "\n\t"
#define G_E(t, x) "v_fma_f32 %" #t ", %" #t ", %18, %18\n\t"
#define G_F(t, x) "v_rcp_f32 %" #t ", %" #t "\n\t"
  asm volatile(G8(G_A) G8(G_B) G8(G_C) G8(G_D) G8(G_E) G8(G_F) G8(G_C)
               "v_cvt_pk_bf16_f32 %0, %0, %1\n\tv_cvt_pk_bf16_f32 %1, %2, %3\n\tv_cvt_pk_bf16_f32 %2, %4, %5\n\tv_cvt_pk_bf16_f32 %3, %6, %7"
               : "=&v"(t0), "=&v"(t1), "=&v"(t2), "=&v"(t3), "=&v"(t4), "=&v"(t5), "=&v"(t6), "=&v"(t7)
               : "v"(a[o]), "v"(a[o + 1]), "v"(a[o + 2]), "v"(a[o + 3]), "v"(a[o + 4]), "v"(a[o + 5]), "v"(a[o + 6]), "v"(a[o + 7]), "v"(k1), "v"(k2), "v"(ir));
  return (u32x4){__float_as_uint(t0), __float_as_uint(t1), __float_as_uint(t2), __float_as_uint(t3)};
}
#define H8(OP, B) OP(0, B) OP(1, B) OP(2, B) OP(3, B) OP(4, B) OP(5, B) OP(6, B) OP(7, B)
#define H_A(i, B) "v_mul_f32 v[16+" #i "], v[" #B "+" #i "], v[" #B "+" #i "]\n\t"
#define H_B(i, B) "v_fma_f32 v[16+" #i "], v[16+" #i "], %[k1], %[k2]\n\t"
#define H_C(i, B) "v_mul_f32 v[16+" #i "], v[16+" #i "], v[" #B "+" #i "]\n\t"
#define H_D(i) "v_exp_f32 v[16+" #i "], v[16+" #i "]\n\t"
#define H_E(i, B) "v_fma_f32 v[16+" #i "], v[16+" #i "], %[ir], %[ir]\n\t"
#define H_F(i) "v_rcp_f32 v[16+" #i "], v[16+" #i "]\n\t"
#define H_BODY(B, X0, X1, X2, X3, X4, X5, X6, X7)                                                                                                              \
  H8(H_A, B) X0 H8(H_B, B) H8(H_C, B) X1 H_D(0) H_D(1) H_D(2) X2 H_D(3) H_D(4) H_D(5) X3 H_D(6) H_D(7) H8(H_E, B) X4 H_F(0) H_F(1) H_F(2) X5 H_F(3) H_F(4) H_F(5) X6 \
  H_F(6) H_F(7) H8(H_C, B) X7 "v_cvt_pk_bf16_f32 v16, v16, v17\n\tv_cvt_pk_bf16_f32 v17, v18, v19\n\tv_cvt_pk_bf16_f32 v18, v20, v21\n\tv_cvt_pk_bf16_f32 v19, v22, v23"
#define H_W(r, s, o) "buffer_load_dwordx4 v[" #r ":" #r "+3], %[vo], %[rs], %[" #s "] offen offset:" #o "*1024 sc1\n\t"
#define H_U(r, s, o) "s_mov_b64 exec, %[mk]\n\t" H_W(r, s, o) "s_mov_b64 exec, -1\n\t"
#define H_V(r, s, o) H_W(r, s, o) H_W(r+4, s, (o+1))
#define H_IN "{v[0:15]}"(a), [k1] "v"(t[1]), [k2] "v"(t[2]), [ir] "v"(t[3])
#define H_CL "v20", "v21", "v22", "v23", "memory"
DI u32x4 gelu_u(const f32x16& a, const f32x4& t, UPart& g, rsrc_t rs, unsigned vo, unsigned so, unsigned on) {
  u32x4 r;
  asm volatile("s_cmp_eq_u32 %[on], 0\n\ts_cbranch_scc1 2f\n\t" H_BODY(0, H_V(176, a0, 0), "", H_V(184, a0, 2), "", H_V(192, a1, 0), "", H_V(200, a1, 2), "") "\n\ts_branch 3f\n2:\n\t"
               H_BODY(0, "", "", "", "", "", "", "", "") "\n3:"
               : "=&{v[16:19]}"(r), U_E(g) : H_IN, [vo] "v"(vo), [rs] "s"(rs), [a0] "s"(so), [a1] "s"(so + 4096), [on] "s"(on) : H_CL, "scc");
  return r;
}
template <int HALF> DI u32x4 gelu_0(const f32x16& a, const f32x4& t) {
  u32x4 r;
  if constexpr (HALF == 0) asm volatile(H_BODY(0, "", "", "", "", "", "", "", "") : "=&{v[16:19]}"(r) : H_IN : H_CL); else asm volatile(H_BODY(8, "", "", "", "", "", "", "", "") : "=&{v[16:19]}"(r) : H_IN : H_CL);
  return r;
}
DI void ld12if(bf16x8 (&wf)[32], rsrc_t rs, unsigned vo, unsigned so, unsigned on) {
  u32x16 x[3] = {cat4(wf, 0), cat4(wf, 4), cat4(wf, 8)};
  asm volatile(U_IF(on, 1) H_W(128, a1, 2) H_W(132, b1, 2) H_W(136, a2, 0) H_W(140, b2, 0) H_W(144, a2, 2) H_W(148, b2, 2) H_W(152, a1, 3) H_W(156, b1, 3) H_W(160, a2, 1) H_W(164, b2, 1) H_W(168, a2, 3) H_W(172, b2, 3) "1:"
               : "+{v[128:143]}"(x[0]), "+{v[144:159]}"(x[1]), "+{v[160:175]}"(x[2])
               : [vo] "v"(vo), [rs] "s"(rs), [a1] "s"(so + 4096), [b1] "s"(so + 114688 + 4096), [a2] "s"(so + 8192), [b2] "s"(so + 114688 + 8192), [on] "s"(on) : "memory", "scc");
#pragma unroll
  for (int i = 0; i < 12; ++i) wf[i] = __builtin_bit_cast(bf16x8, (u32x4){x[i >> 2][4 * (i & 3)], x[i >> 2][4 * (i & 3) + 1], x[i >> 2][4 * (i & 3) + 2], x[i >> 2][4 * (i & 3) + 3]});
}
constexpr float GC1 = -0.10294324f, GC2 = -2.3022082f;

#define EX(B, i) "v_exp_f32 v[" #B "+" #i "], v[" #B "+" #i "]\n\t"
#define TILE(C, P, KC, KP, LC, LP, HC, HP, U, X0, X1)                                                 \
  EX(C, 0) EX(C, 1)                                                                                   \
  "v_smfmac_f32_32x32x32_bf16 v[2:17], v[" #HP ":" #HP "+3], v[" #P "+8:" #P "+15], v78\n\t"          \
  EX(C, 2) EX(C, 3) X0 EX(C, 4) EX(C, 5)                                                              \
  "v_mfma_f32_32x32x16_bf16 v[" #P ":" #P "+15], v[" #KP ":" #KP "+3], v[50:53], 0\n\t"               \
  "ds_read_b128 v[" #KC ":" #KC "+3], v79 offset:" #U "*2048+4096\n\t"                                \
  "s_mov_b64 exec, %[msk]\n\t"                                                                        \
  "ds_read_b128 v[" #LP ":" #LP "+3], v79 offset:" #U "*2048+3072\n\t"                                \
  "ds_read_b128 v[" #HC ":" #HC "+3], v79 offset:" #U "*2048+1280\n\t"                                \
  "s_mov_b64 exec, -1\n\t"                                                                            \
  EX(C, 6) EX(C, 7) EX(C, 8) EX(C, 9)                                                                 \
  "v_smfmac_f32_32x32x32_bf16 v[2:17], v[" #LC ":" #LC "+3], v[" #C ":" #C "+7], v78\n\t"             \
  EX(C, 10) EX(C, 11) EX(C, 12) X1 EX(C, 13) EX(C, 14) EX(C, 15)                                      \
  "s_waitcnt lgkmcnt(0)\n\t"
#define P1_3 DN
#define P5_2 DN
#define P10_0 DN
#define P14_2 DN
#define P0_0 ""
#define P0_1 ""
#define P0_2 ""
#define P0_3 ""
#define P1_0 ""
#define P1_1 ""
#define P1_2 ""
#define P2_0 ""
#define P2_1 ""
#define P2_2 ""
#define P2_3 ""
#define P3_0 ""
#define P3_1 ""
#define P3_2 ""
#define P3_3 ""
#define P4_0 ""
#define P4_1 ""
#define P4_2 ""
#define P4_3 ""
#define P5_0 ""
#define P5_1 ""
#define P5_3 ""
#define P6_0 ""
#define P6_1 ""
#define P6_2 ""
#define P6_3 ""
#define P7_0 ""
#define P7_1 ""
#define P7_2 ""
#define P7_3 ""
#define P8_0 ""
#define P8_1 ""
#define P8_2 ""
#define P8_3 ""
#define P9_0 ""
#define P9_1 ""
#define P9_2 ""
#define P9_3 ""
#define P10_1 ""
#define P10_2 ""
#define P10_3 ""
#define P11_0 ""
#define P11_1 ""
#define P11_2 ""
#define P11_3 ""
#define P12_0 ""
#define P12_1 ""
#define P12_2 ""
#define P12_3 ""
#define P13_0 ""
#define P13_1 ""
#define P13_2 ""
#define P13_3 ""
#define P14_0 ""
#define P14_1 ""
#define P14_3 ""
#define P15_0 ""
#define P15_1 ""
#define P15_2 ""
#define P15_3 ""
#define TILE4(g, A0, A1, B0, B1, C0, C1, D0, D1)                                                         \
  P##g##_0 TILE(18, 34, 54, 58, 62, 66, 70, 74, 0, A0, A1) P##g##_1 TILE(34, 18, 58, 54, 66, 62, 74, 70, 1, B0, B1)     \
  P##g##_2 TILE(18, 34, 54, 58, 62, 66, 70, 74, 2, C0, C1) P##g##_3 TILE(34, 18, 58, 54, 66, 62, 74, 70, 3, D0, D1) "v_add_u32 v79, 0x2000, v79"
#define ATT_OPS "+{v[2:17]}"(oacc), "+{v[18:33]}"(s0), "+{v[34:49]}"(s1), "+{v[54:57]}"(k0), "+{v[58:61]}"(k1), "+{v[62:65]}"(l0), "+{v[66:69]}"(l1), \
                "+{v[70:73]}"(h0), "+{v[74:77]}"(h1), "+{v79}"(ad)
#define ATT_IN "{v[50:53]}"(qf), "{v78}"(idx), [msk] "s"(msk)
#define WM 0
#define WARM "buffer_load_dword v80, %[vl], %[rs], %[sw] offen\n\t"
#define WARM2 "buffer_load_dword v80, %[vl], %[rs], %[sx] offen\n\t"
#define WN 6
#define WL(B, i) "buffer_load_dwordx4 v[" #B "+4*" #i ":" #B "+4*" #i "+3], %[vl], %[rs], %[sa] offen offset:" #i "*1024 sc1\n\t"
#define DL(i) "buffer_load_dwordx4 %[vl], %[rs], %[sb] offen offset:" #i "*1024 sc1 lds\n\t"
#define FS(i) "buffer_store_dwordx4 %[mk], %[vl], %[rs], %[f" #i "] offen sc1\n\t"
#define CL(B, i) "buffer_load_dwordx4 v[" #B "+4*" #i ":" #B "+4*" #i "+3], %[vl], %[rs], %[sc] offen offset:" #i "*1024 sc1\n\t"
#define CM(B, i) "buffer_load_dwordx4 v[" #B "+4*" #i ":" #B "+4*" #i "+3], %[vl], %[rs], %[sd] offen offset:" #i "*1024 sc1\n\t"
#define PL "s_mov_b32 m0, %[pl]\n\ts_nop 0\n\tbuffer_load_dwordx4 %[pv], %[pr], %[p0] offen lds\n\ts_add_u32 m0, m0, 0x400\n\ts_nop 0\n\tbuffer_load_dwordx4 %[pv], %[pr], %[p1] offen lds\n\t"
#define PR "ds_read_b64_tr_b16 v[96:97], %[ta]\n\tds_read_b64_tr_b16 v[98:99], %[ta] offset:1024\n\tds_read_b64_tr_b16 v[100:101], %[tb]\n\tds_read_b64_tr_b16 v[102:103], %[tb] offset:1024\n\t"
#define PS(f, c, i) "buffer_store_dwordx4 v[" #f ":" #f "+3], %[vl], %[rs], %[d" #i "] offen sc1\n\tv_mfma_f32_16x16x32_bf16 v[" #c ":" #c "+3], %[se], v[" #f ":" #f "+3], 0\n\t"
#define PC "buffer_store_dword v104, %[cv], %[rs], %[c0] offen sc1\n\tbuffer_store_dword v108, %[cv], %[rs], %[c1] offen sc1\n\t"
#define D2 "s_mov_b32 m0, %[l2]\n\ts_nop 0\n\tbuffer_load_dwordx4 %[vl], %[rs], %[s2] offen sc1 lds\n\tbuffer_load_dwordx4 %[vl], %[rs], %[s2] offen offset:1024 sc1 lds\n\ts_mov_b32 m0, %[ld]\n\ts_nop 0\n\t"
#define UP "s_cmp_lt_u32 %[wn], 4\n\ts_cbranch_scc1 3f\n\ts_setprio 1\n3:\n\t"
#define DN "s_setprio 0\n\t"
#define UPP asm volatile(UP :: [wn] "s"(w) : "scc")
#define XL(i) "buffer_load_dwordx2 v[82+2*" #i ":83+2*" #i "], %[xo], %[rx], 0 offen offset:16*" #i "\n\t"
#define HINT "s_mov_b64 exec, 1\n\tv_mov_b32 v80, %[t0]\n\tbuffer_store_dword v80, off, %[rs], %[ho] sc1\n\ts_mov_b64 exec, -1\n\t"
#define TAGLD "buffer_load_dwordx4 v[92:95], %[po], %[rs], %[ps] offen sc1\n\t"
#define TAGLD2 "buffer_load_dwordx2 v[92:93], %[po], %[rs], %[ps] offen sc1\n\t"
#define TAGCHK(n, LD) "\n\ts_waitcnt vmcnt(" #n ")\n\ts_mov_b32 %[cn], 0x100000\n"                                              \
  "1:\n\tv_cmp_ne_u32 vcc, %[t0], v92\n\tv_cmp_ne_u32 %[tm], %[t1], v93\n\ts_nop 3\n\ts_or_b64 %[tm], %[tm], vcc\n\ts_cbranch_scc0 2f\n\t"   \
  "s_sleep 1\n\t" LD "s_waitcnt vmcnt(0)\n\ts_sub_u32 %[cn], %[cn], 1\n\ts_cbranch_scc0 1b\n2:"
#define TAGCHK_(n, LD) TAGCHK(n, LD)
#define TAG_OUT [cn] "=&s"(cn), [tm] "=&s"(tm)
#define TAG_IN(o) [po] "v"(o), [ps] "s"(0), [t0] "s"(tag[0]), [t1] "s"(tag[1])
#define TAG_LO(f) [po] "v"(alo), [ps] "s"(f), [t0] "s"(al0), [t1] "s"(al1)
#define ATT_LD [vl] "v"(vl), [rs] "s"(rws)
#define ATT_DMA(src, dst) [sb] "s"(src), [ld] "s"(dst)
#define ATT_FILL(a, b) [mk] "v"(mk), [f0] "s"(a), [f1] "s"(b)

__global__ void __launch_bounds__(512, 2) k_main(const u16* x, const u16* __restrict__ wqkv, const u16* __restrict__ wo, const u16* __restrict__ w1, const u16* w2,
                                                 const u16* w3, const u16* w4, u16* out, char* ws) {
  STDECL; STAMP(0);
  u64 px, pw;
  asm volatile("s_load_dwordx2 s[40:41], %3, 0x0\n\ts_load_dwordx2 s[44:45], %3, 0x8\n\ts_load_dwordx2 s[38:39], %3, 0x40"
               : "=&{s[40:41]}"(px), "=&{s[44:45]}"(pw), "=&{s[38:39]}"(ws) : "s"(__builtin_amdgcn_kernarg_segment_ptr()));
  extern __shared__ __attribute__((aligned(16))) char smem[];
  const unsigned lds0 = (unsigned)(uintptr_t)smem;
  const int b = blockIdx.x;
  const unsigned w = rfl(threadIdx.x >> 6);
  STAMPC(40);
  u32x2 tag;
  phase1(px, pw, wo, w1, w2, w3, w4, ws, lds0, b, w, tag KPASS);
  STSWAP();
  asm volatile("" : "+s"(out), "+s"(w2), "+s"(w3), "+s"(w4), "+s"(x));
  const unsigned xcd = b & 7, r = b >> 3;
  const rsrc_t rws = mkrsrc(ws, WS_END);
  const unsigned rb = (xcd >> 1) * 8 + (r >> 2), c8 = (xcd & 1) * 4 + (r & 3);
  const unsigned rq = rb * 2 + ((r >> 1) & 1), c4 = (xcd & 1) * 2 + (r & 1);
  const unsigned wi = w & 3;
  const unsigned r2a = lds0 + wi * 16384, r2b = lds0 + 65536 + wi * 8192, r2c = lds0 + L_Z + wi * 3072;
  auto kq = [&](unsigned base, int j) { return base + ((c4 * 4 + wi) * 32 + ((wi + j) & 3) * 8) * 1024; };
  bf16x8 wf[32];
  u32x16 wt[8];
  u32x2 xr[4];
  float cs1;
  const unsigned wv = w < 3 ? w : w == 3 ? 6 : w - 1;
  STAMP(0);
  {
    const int l = lane(), h = l >> 5, l31 = l & 31;
    unsigned vl = l * 16;
    const unsigned head = xcd * 4 + (r & 3), qb = r >> 2;
    bf16x8 qf;
    const unsigned kv = WS_KV + head * 131072, gr = WS_GR + (head >> 1) * 64 * GRS;
    auto kvt = [&](unsigned i) { return kv + ((qb * 8 + i) & 63) * 2048; };
#define PINS(x) asm volatile("" : "+s"(x))
    u32x16 c3, c1;
    unsigned sc3 = WS_CS + (48 + c8 * 7 + wv) * 4096, sc1 = WS_CS + (16 + c4 * 4 + wi) * 4096;
    const unsigned cst = l31 == 16 ? 0x3f803f80u : 0u;
    const bf16x8 vc = __builtin_bit_cast(bf16x8, (u32x4){cst, cst, cst, cst});
    f32x16 oacc = {}, s0, s1 = {};
    bf16x8 k0, k1, l0 = vc, l1 = vc, h0 = vc, h1 = vc;
    int idx = 0xDDDD;
    unsigned ad = lds0 + h * 512 + l31 * 16;
    const unsigned hw = w >> 2;
    unsigned wb = w < 4 ? TW_O : TW_1, ko[4] = {kq(0, 0), kq(0, 1), kq(0, 2), kq(0, 3)};
    auto wso = [&](int n) { return wb + ko[n >> 1] + (n & 1) * 4096; };
    unsigned wrm = (r < 16 ? TW_3 : TW_4) + ((xcd & 1) * 16 + (r & 15)) * 57344 + w * 1024;
    unsigned oo = WS_O + (qb * 8 + w) * 32768 + head * 1024, to = rq * 32768 + c4 * 8192 + w * 1024;
    const unsigned uf = WS_U + (rb * 8 + c8) * 28672 + w * 4096, sf = rq * 4096 + c4 * 1024;
    unsigned u0 = w < 7 ? uf : WS_STH + sf, u1 = w < 7 ? uf + 1024 : WS_STX + sf, ud = w < 7 ? 2048 : 0;
    u32x4 mk = {MARK, MARK, MARK, MARK};
    unsigned kv0 = kvt(2 * w), kv1 = kvt(16 + 2 * w), kv2 = kvt(32 + 2 * w), kv3 = kvt(48 + 2 * w), kl = lds0 + w * 4096;
    unsigned ho = FLAG_O + ((qb * 8 + w) * 4 + (head >> 3)) * 128 + (head & 7) * 4;
    const int nq = (int)rq - 1 + ((l - 32) >> 2);
    unsigned fo = l < 32 || (l < 44 && nq >= 0 && nq < 64) ? FLAG_F + 8 * (l < 32 ? (l >> 2) + 8 * ((rq >> 3) * 4 + (l & 3)) : (nq >> 4) * 2 + ((l >> 1) & 1) + 8 * ((nq & 15) * 2 + (l & 1))) : 0x7ff00000u;
    u32x2 fv;
    unsigned gro = gr + l * GRS, po = l < 3 ? gr + ((qb * 8 + (l == 0 ? w : 2 * w + l - 1)) & 63) * GRS : 0x7ff00000u, qo = (h * 2048 + qb * 256 + w * 32 + l31) * 16 + WS_Q + head * 65536;
    unsigned fl0 = w == 0 && l == 0 ? b * 8 : 0x7ff00000u, alo = w < 4 ? (w * 64 + l) * 8 : 0x7ff00000u;
    unsigned wlo = FLAG_W + (l31 * 8 + c4 * 2 + (wi >> 1)) * 8;
    unsigned al0 = w < 4 ? tag[0] : 0u, al1 = w < 4 ? tag[1] : 0u;
    auto flag = [&](unsigned base) { __builtin_amdgcn_raw_buffer_store_b64(tag, rws, fl0, base, 16); };
    const rsrc_t rx = mkrsrc(x, S * D * 2);
    unsigned xo = ((rq * 32 + l31) * D + c4 * 128 + wi * 32 + 4 * h) * 2;
    const bool big3 = b < 128, c3p = big3 && w < 7;
    const unsigned pp = c3p ? F * 2 : 1024;
    unsigned pso = w == 7 ? (b >> 3) * 16384 + (b & 7) * 128 : big3 ? (b >> 2) * 16 * (F * 2) + ((b & 3) * 7 + w) * 128 : (((b - 128) >> 3) * 7 + w) * 16384 + (b & 7) * 128, ps1 = pso + 8 * pp;
    unsigned pdo = w == 7 ? TW_2 + ((b & 7) * 64 + (b >> 3)) * 1024 : big3 ? TW_3 + (((b & 3) * 14 + 2 * w) * 32 + (b >> 2)) * 1024 : TW_4 + ((b & 7) * 224 + ((b - 128) >> 3) * 7 + w) * 1024;
    unsigned pd1 = pdo + (w == 7 || big3 ? 32768 : 114688), pco = WS_CS + ((pdo - TW_O) >> 15) * 4096 + ((pdo >> 10) & 31) * 128;
    unsigned plc = lds0 + 131072 + w * 2048;
    rsrc_t rp = mkrsrc(w == 7 ? w2 : big3 ? w3 : w4, w == 7 ? D * D * 2 : D * F * 2);
    unsigned pvo = (l >> 3) * pp + (((l & 7) ^ ((l >> 2) & 4)) << 4), pcv = c3p && l < 32 ? l * 4 : 0x7ff00000u;
    unsigned pta = plc + (4 * h + ((l >> 2) & 3)) * 128 + (((((l >> 3) & 2) + ((l >> 1) & 1)) ^ ((l >> 1) & 4)) << 4) + 8 * (l & 1), ptb = pta ^ 64;
    const unsigned so_ = ((0x10100101u >> l31) & 1u) * 0x3f803f80u;
    u32x4 se = {so_, so_, so_, so_};
    unsigned long long msk = 0x0000ffff0000ffffull;
    unsigned dum = 0;
    PINS(ko[0]); PINS(ko[1]); PINS(ko[2]); PINS(ko[3]); PINS(oo); PINS(to); PINS(u0); PINS(u1); PINS(ud);
    PINS(kv1); PINS(kv2); PINS(kv3); PINS(kl); PINS(al0); PINS(al1); PINS(pso); PINS(ps1); PINS(pdo); PINS(pd1); PINS(pco); PINS(rp); PINS(msk);
    TOUCH(vl); TOUCH(mk); TOUCH(fo); TOUCH(gro); TOUCH(po); TOUCH(qo); TOUCH(fl0); TOUCH(alo); TOUCH(wlo); TOUCH(xo); TOUCH(pvo); TOUCH(pcv); TOUCH(pta); TOUCH(ptb); TOUCH(se);
    asm volatile("" : "+{v[2:17]}"(oacc), "+{v[34:49]}"(s1), "+{v[62:65]}"(l0), "+{v[66:69]}"(l1), "+{v[70:73]}"(h0), "+{v[74:77]}"(h1), "+{v78}"(idx), "+{v79}"(ad), "+{v80}"(dum));
    polltag(rws, po, tag);
    STAMP(16);
    asm volatile("buffer_load_dwordx4 %0, %1, %2, 0 offen sc1" : "={v[50:53]}"(qf) : "v"(qo), "s"(rws) : "memory");
    dmaN<4>(rws, vl, kv0, kl);
    asm volatile("s_waitcnt vmcnt(4)" : "+{v[50:53]}"(qf) :: "memory");
    float qn = 0.f;
    { const u32x4 u = __builtin_bit_cast(u32x4, qf);
#pragma unroll
      for (int j = 0; j < 4; ++j) qn = dot2(u[j], u[j], qn); }
    qn = swap16max(max16(swapadd(qn))) * 1.05f;
    TOUCH(qn);
    STAMP(1);
    waitv<0>();
    BAR();
    STAMP(2); STAMPC(40); AP(0);
    u32x4 g4; u32x2 ga; unsigned cn; unsigned long long tm;
    u32x8 c1a, c1c;
    asm volatile("ds_read_b128 v[54:57], v79\n\tds_read_b128 v[58:61], v79 offset:2048\n\ts_mov_b64 exec, %[msk]\n\tds_read_b128 v[62:65], v79 offset:1024\n\ts_mov_b64 exec, -1\n\t" TAGLD
                 "s_waitcnt lgkmcnt(2)\n\tv_mfma_f32_32x32x16_bf16 v[18:33], v[54:57], v[50:53], 0"
                 : "=&{v[18:33]}"(s0), "=&{v[54:57]}"(k0), "=&{v[58:61]}"(k1), "+{v[62:65]}"(l0), "=&{v[92:95]}"(g4) : "{v79}"(ad), "{v[50:53]}"(qf), [msk] "s"(msk), [rs] "s"(rws), [po] "v"(gro), [ps] "s"(0) : "memory");
    UPP;
    flag(FLAG_W);
    asm volatile("s_waitcnt lgkmcnt(0)\n\ts_nop 7\n\ts_nop 1\n\t" TILE4(0, "", FS(0), "", "", "", FS(1), "", "") TAGCHK(3, TAGLD) : ATT_OPS, "+{v[92:95]}"(g4), TAG_OUT : ATT_IN, ATT_LD, ATT_FILL(oo, WS_H + to), TAG_IN(gro) : "memory", "vcc", "scc");
    AP(1);
    *(LAS unsigned*)(uintptr_t)(lds0 + L_SLOW + w * 4) = __all(qn * __uint_as_float((head & 1) ? g4[3] : g4[2]) <= 48.f * 48.f) ? 0u : 1u;
#define DM(i) "buffer_load_dwordx4 %[vl], %[rs], %[sm] offen offset:" #i "*1024 sc1 lds\n\t"
#define DN_(i) "buffer_load_dwordx4 %[vl], %[rs], %[sn] offen offset:" #i "*1024 sc1 lds\n\t"
#define MUP "s_add_u32 m0, m0, 0x8000\n\ts_nop 0\n\t"
#define WM_(B, i) "buffer_load_dwordx4 v[" #B "+4*" #i ":" #B "+4*" #i "+3], %[vl], %[rs], %[sz] offen offset:" #i "*1024 sc1\n\t"
    asm volatile("s_mov_b32 m0, %[ld]\n\t" TILE4(1, DL(0), DL(1), DL(2), DL(3), MUP DM(0), DM(1), DM(2), DM(3))
                 : ATT_OPS : ATT_IN, ATT_LD, ATT_DMA(kv1, kl + 32768), [sm] "s"(kv2) : "memory", "scc"); AP(2);
    asm volatile("s_mov_b32 m0, %[ld]\n\t" TILE4(2, DL(0), DL(1), DL(2), DL(3), FS(0), FS(1), "", PL) "\n\ts_waitcnt vmcnt(12)"
                 : ATT_OPS : ATT_IN, ATT_LD, ATT_DMA(kv3, kl + 98304), ATT_FILL(WS_G + to, WS_X1 + to),
                   [pl] "s"(plc), [pv] "v"(pvo), [pr] "s"(rp), [p0] "s"(pso), [p1] "s"(ps1) : "memory", "scc"); AP(3);
    BAR(); AP(4);
    UPP;
    {
      asm volatile(TILE4(3, "", "", "", "", "", "", "", "") : ATT_OPS : ATT_IN : "memory"); AP(5);
      asm volatile(TILE4(4, "", "", "", "", TAGLD2, "", "", "") TAGCHK(0, TAGLD2)
                   : ATT_OPS, "=&{v[92:93]}"(ga), TAG_OUT : ATT_IN, [rs] "s"(rws), TAG_IN(wlo) : "memory", "vcc", "scc"); AP(6);
      { u32x8 pf; u32x4 pc0, pc1;
        asm volatile(TILE4(5, PR, WL(144, 0), PS(96, 104, 0), WL(144, 1), PS(100, 108, 1), WL(144, 2), PC, WL(144, 3))
                     : ATT_OPS, "=&{v[144:159]}"(wt[1]), "=&{v[96:103]}"(pf), "=&{v[104:107]}"(pc0), "=&{v[108:111]}"(pc1)
                     : ATT_IN, ATT_LD, [sa] "s"(wso(1)), [ta] "v"(pta), [tb] "v"(ptb), [se] "v"(se), [d0] "s"(pdo), [d1] "s"(pd1),
                       [cv] "v"(pcv), [c0] "s"(pco), [c1] "s"(pco + 4096) : "memory"); }
      asm volatile(TILE4(6, WM_(128, 0), WL(160, 0), WM_(128, 1), WL(160, 1), WM_(128, 2), WL(160, 2), WM_(128, 3), WL(160, 3))
                   : ATT_OPS, "=&{v[160:175]}"(wt[2]), "=&{v[128:143]}"(wt[0]) : ATT_IN, ATT_LD, [sa] "s"(wso(2)), [sz] "s"(wso(0)) : "memory");
      AP(7); waitv<9>(); BAR(); AP(8);
      UPP;
      flag(FLAG_B);
      asm volatile(TILE4(7, FS(0), WL(176, 0), FS(2), WL(176, 1), FS(1), WL(176, 2), FS(3), WL(176, 3))
                   : ATT_OPS, "=&{v[176:191]}"(wt[3]) : ATT_IN, ATT_LD, [sa] "s"(wso(3)), ATT_FILL(u0 + ud, u1 + ud), [f2] "s"(u0), [f3] "s"(u1) : "memory"); AP(9);
      asm volatile(TILE4(8, WARM, WL(192, 0), CM(108, 0), WL(192, 1), WARM2, WL(192, 2), CM(108, 1), WL(192, 3))
                   : ATT_OPS, "+{v80}"(dum), "=&{v[192:207]}"(wt[4]), "=&{v[108:115]}"(c1a) : ATT_IN, ATT_LD, [sa] "s"(wso(4)), [sw] "s"(wrm), [sx] "s"(wrm + 8192), [sd] "s"(sc1) : "memory");
      asm volatile(TILE4(9, WARM, WL(208, 0), CM(108, 2), WL(208, 1), WARM2, WL(208, 2), CM(108, 3), WL(208, 3))
                   : ATT_OPS, "+{v80}"(dum), "=&{v[208:223]}"(wt[5]), "=&{v[116:123]}"(c1c) : ATT_IN, ATT_LD, [sa] "s"(wso(5)), [sw] "s"(wrm + 2 * 8192), [sx] "s"(wrm + 3 * 8192), [sd] "s"(sc1) : "memory"); AP(10);
      asm volatile(TILE4(10, TAGLD2, WL(224, 0), WARM, WL(224, 1), WARM2, WL(224, 2), "", WL(224, 3)) TAGCHK_(WN, TAGLD2)
                   : ATT_OPS, "+{v80}"(dum), "=&{v[224:239]}"(wt[6]), "=&{v[92:93]}"(ga), TAG_OUT
                   : ATT_IN, ATT_LD, [sa] "s"(wso(6)), [sw] "s"(wrm + 4 * 8192), [sx] "s"(wrm + 5 * 8192), TAG_LO(FLAG_B) : "memory", "vcc", "scc");
      asm volatile(TILE4(11, WARM, WL(240, 0), "", WL(240, 1), "", WL(240, 2), "", WL(240, 3))
                   : ATT_OPS, "+{v80}"(dum), "=&{v[240:255]}"(wt[7]) : ATT_IN, ATT_LD, [sa] "s"(wso(7)), [sw] "s"(wrm + 6 * 8192) : "memory");
      AP(11); BAR(); AP(12);
      UPP;
      flag(FLAG_F);
      asm volatile("s_mov_b32 m0, %[ld]\n\t" TILE4(12, CL(92, 0), DL(0), CL(92, 1), DL(1), CL(92, 2), DL(2), CL(92, 3), DL(3))
                   : ATT_OPS, "=&{v[92:107]}"(c3) : ATT_IN, ATT_LD, ATT_DMA(TW_2 + (hw ? ko[1] : ko[0]), r2a + hw * 8192), [sc] "s"(sc3) : "memory"); AP(13);
      asm volatile("s_mov_b32 m0, %[ld]\n\t" TILE4(13, XL(0), DL(0), XL(1), DL(1), XL(2), DL(2), XL(3), DL(3))
                   : ATT_OPS, "=&{v[82:83]}"(xr[0]), "=&{v[84:85]}"(xr[1]), "=&{v[86:87]}"(xr[2]), "=&{v[88:89]}"(xr[3])
                   : ATT_IN, ATT_LD, ATT_DMA(TW_2 + (hw ? ko[1] : ko[0]) + 4096, r2a + hw * 8192 + 4096), [rx] "s"(rx), [xo] "v"(xo) : "memory");
      asm volatile(TILE4(14, D2, DL(0), "", DL(1), "", DL(2), "buffer_load_dwordx2 v[90:91], %[fo], %[rs], 0 offen sc1\n\t", DL(3))
                   : ATT_OPS, "=&{v[90:91]}"(fv) : ATT_IN, ATT_LD, ATT_DMA(TW_2 + ko[2] + hw * 4096, r2b + hw * 4096), [fo] "v"(fo), [l2] "s"(r2c + hw * 1024), [s2] "s"(TW_2 + ko[3] + hw * 1024) : "memory"); AP(14);
      asm volatile(TILE4(15, "", "", "", "", "", "", "s_waitcnt vmcnt(0)\n\t", HINT)
                   : ATT_OPS, "+{v[82:83]}"(xr[0]), "+{v[84:85]}"(xr[1]), "+{v[86:87]}"(xr[2]), "+{v[88:89]}"(xr[3]), "+{v[90:91]}"(fv),
                     "+{v[128:143]}"(wt[0]), "+{v[144:159]}"(wt[1]), "+{v[160:175]}"(wt[2]), "+{v[176:191]}"(wt[3]), "+{v[192:207]}"(wt[4]),
                     "+{v[208:223]}"(wt[5]), "+{v[224:239]}"(wt[6]), "+{v[240:255]}"(wt[7]), "+{v80}"(dum)
                   : ATT_IN, [rs] "s"(rws), [ho] "s"(ho), [t0] "s"(tag[0]) : "memory");
      asm volatile("" : "+{v[92:107]}"(c3), "+{v[108:115]}"(c1a), "+{v[116:123]}"(c1c));
#pragma unroll
      for (int i = 0; i < 8; ++i) { c1[i] = c1a[i]; c1[8 + i] = c1c[i]; }
      asm volatile("s_nop 1\n\tv_smfmac_f32_32x32x32_bf16 v[2:17], v[74:77], v[42:49], v78\n\ts_nop 7\n\ts_nop 4" : "+{v[2:17]}"(oacc) : "{v[34:49]}"(s1), "{v[74:77]}"(h1), "{v78}"(idx));
    }
    const int l2 = lane();
    auto csum = [&](const u32x16& c) {
      float t[4];
#pragma unroll
      for (int j = 0; j < 4; ++j) {
        float a = __uint_as_float(c[j]) + __uint_as_float(c[4 + j]), d = __uint_as_float(c[8 + j]) + __uint_as_float(c[12 + j]);
        TOUCH(a); TOUCH(d);
        a += d; a += dppf<0x128>(a);
        t[j] = __uint_as_float(__builtin_amdgcn_ds_bpermute(l2 & 28, __float_as_uint(swapadd(swap16add(a)))));
      }
      return (l2 & 2) ? ((l2 & 1) ? t[3] : t[2]) : ((l2 & 1) ? t[1] : t[0]);
    };
    const u32x4 sf0 = *(const LAS u32x4*)(uintptr_t)(lds0 + L_SLOW), sf1 = *(const LAS u32x4*)(uintptr_t)(lds0 + L_SLOW + 16);
    if (rfl(sf0[0] | sf0[1] | sf0[2] | sf0[3] | sf1[0] | sf1[1] | sf1[2] | sf1[3]) != 0u) {
      BAR();
      sfor<0, 4>([&](auto I) { dmaN<4>(rws, vl, kv + w * 16384 + I.v * 4096, lds0 + w * 16384 + I.v * 4096); });
      waitv<0>(); BAR();
      const int n = lane();
      const unsigned cs_ = (n & 31) == 16 ? 0x3f803f80u : 0u;
      const bf16x8 vc = __builtin_bit_cast(bf16x8, (u32x4){cs_, cs_, cs_, cs_});
      oacc = f32x16{}; ad = lds0 + (n >> 5) * 512 + (n & 31) * 16;
      float mx = -3.0e38f;
      const f32x16 zero = {};
#pragma unroll 1
      for (int t = 0; t < 64; ++t) {
        const bf16x8 kf = lds128(ad); bf16x8 v0 = vc, v1 = vc;
        if ((n & 31) < 16) { v0 = lds128(ad + 1024); v1 = lds128(ad + 1280); }
        f32x16 s = MFMA32(kf, qf, zero);
        float tm = s[0];
#pragma unroll
        for (int i = 1; i < 16; ++i) tm = fmaxf(tm, s[i]);
        const float mn = fmaxf(mx, swapmax(tm)), al = __builtin_amdgcn_exp2f(mx - mn);
        mx = mn;
#pragma unroll
        for (int i = 0; i < 16; ++i) { s[i] = __builtin_amdgcn_exp2f(s[i] - mn); oacc[i] *= al; }
        oacc = __builtin_amdgcn_smfmac_f32_32x32x32_bf16(__builtin_bit_cast(bfv8_t, v0), __builtin_bit_cast(bf16_t, __builtin_shufflevector(s, s, 0, 1, 2, 3, 4, 5, 6, 7)), oacc, idx, 0, 0);
        oacc = __builtin_amdgcn_smfmac_f32_32x32x32_bf16(__builtin_bit_cast(bfv8_t, v1), __builtin_bit_cast(bf16_t, __builtin_shufflevector(s, s, 8, 9, 10, 11, 12, 13, 14, 15)), oacc, idx, 0, 0);
        ad += 2048;
      }
      BAR();
      tile_dma(rws, vl, kq(TW_2, hw), r2a + hw * 8192); dmaN<4>(rws, vl, kq(TW_2, 2) + hw * 4096, r2b + hw * 4096);
      waitv<0>();
    }
    STAMP(3); STAMPC(41); AP(15);
    const float inv = __builtin_amdgcn_rcpf(swapadd(l2 < 32 ? oacc[8] : 0.f));
#pragma unroll
    for (int i = 0; i < 8; ++i) oacc[i] *= inv;
    st128(rws, oo + vl, pack8(oacc, 0));
    if (w == 4) { if (!__all(fo >= 0x7ff00000u || (fv[0] == tag[0] && fv[1] == tag[1]))) polltag(rws, fo, tag); }
#pragma unroll
    for (int k = 0; k < 32; ++k) wf[k] = __builtin_bit_cast(bf16x8, (u32x4){wt[k >> 2][(k & 3) * 4], wt[k >> 2][(k & 3) * 4 + 1], wt[k >> 2][(k & 3) * 4 + 2], wt[k >> 2][(k & 3) * 4 + 3]});
    STAMP(4);
    BAR();
    *(LAS float*)(uintptr_t)(lds0 + L_CS + w * 128 + (l2 & 31) * 4) = csum(c3);
    cs1 = csum(c1); TOUCH(cs1);
  }
  unsigned wsel = w;
  asm volatile("" : "+s"(wsel));
  const unsigned so3 = TW_3 + (c8 * 7 + wv) * 32768;
  {
  const int l = lane();
  const unsigned vl = l * 16;
  const unsigned tileo = rq * 32768 + (c4 * 16 + wi * 4) * 512;
  const unsigned stato = rq * 4096 + c4 * 1024 + (wi >> 1) * 512 + (wi & 1) * 8;
  const unsigned hq = (rq * 4 + wi) * 128, hs = (rq * 4 + c4) * 128;
  auto rest = [&](unsigned o) { ldw<3072>(wf[27], rws, vl, o); sfor<0, 4>([&](auto K) { ldw<K.v * 1024>(wf[28 + K.v], rws, vl, o + 4096); }); };
  auto unstage = [&]() {
    const unsigned ua = r2a + vl, ub_ = r2b + vl, uc = r2c + vl;
    sfor<0, 16>([&](auto K) { ldsw<K.v * 1024>(wf[K.v], ua); });
    sfor<0, 8>([&](auto K) { ldsw<K.v * 1024>(wf[16 + K.v], ub_); });
    sfor<0, 3>([&](auto K) { ldsw<K.v * 1024>(wf[24 + K.v], uc); });
    asm volatile("s_waitcnt lgkmcnt(0)" ::: "memory");
  };
  unsigned qd[4];
#pragma unroll
  for (int j = 0; j < 4; ++j) qd[j] = lds0 + L_PB + vl + ((wi + j) & 3) * 8192;
  unsigned qa = lds0 + C_Q + ((wi + 1 + l) & 3) * 4, lm = lds0 + L_M, one = 1u;
  bf16x8 st;
  unsigned long long late;
  static_assert(C_S == L_M, "");
  if (w < 4) {
    __builtin_amdgcn_s_setprio(1);
    f32x16 hres;
#pragma unroll
    for (int g = 0; g < 4; ++g) { hres[4 * g] = bflo(xr[g][0]); hres[4 * g + 1] = bfhi(xr[g][0]); hres[4 * g + 2] = bflo(xr[g][1]); hres[4 * g + 3] = bfhi(xr[g][1]); }
    unsigned so = WS_O + rq * 32768 + wi * 8192, ho = FLAG_H + hs, vh = vl + WS_H + tileo;
    asm volatile("" : "+{v[16:31]}"(hres), "+v"(qd[0]), "+v"(qd[1]), "+v"(qd[2]), "+v"(qd[3]), "+v"(qa), "+v"(lm), "+v"(one), "+v"(ho), "+v"(vh)); PINS(so);
    spoll8(rws, FLAG_O + hq, tag[0]);
    STAMP(5);
    kstage<0>(hres, wf, rws, vl, so, qd, qa, lm, one, st, 0u, 0u, late, rws, ho);
    STAMP(7);
    st128(rws, vh, pack8(hres, 0)); st128(rws, vh + 1024, pack8(hres, 8));
    put_stats(rws, vl, WS_STH + stato, hres);
    ladd(lds0 + C_R);
    STAMP(8);
    __builtin_amdgcn_s_setprio(0);
    rest(kq(TW_2, 3));
    unstage();
#define TH3 6
    dmaN<4>(rws, vl, so3, r2a);
    lwait(lds0 + C_H, 4);
    sfor<4, 27>([&](auto K) {
      constexpr int k = K.v;
      dma16<0, true>(rws, vl, so3 + k * 1024, k < 16 ? r2a + k * 1024 : k < 24 ? r2b + (k - 16) * 1024 : r2c + (k - 24) * 1024);
      if constexpr (k < 26) waitv<TH3>();
    });
    STAMP(11);
    const int dl = (wi & 1) ? (int)(wi >> 1) + 1 : -(int)(wi >> 1) - 1;
    const int rn = (int)rq + ((wi & 1) ? 1 : -1);
    __builtin_amdgcn_s_setprio(1);
    const int src = (int)rq * 32 + (l & 31) + dl;
    unsigned vg = (src >= 0 && src < S) ? (unsigned)((src >> 5) * 32768 + (src & 31) * 16 + (l >> 5) * 512) : 0x7ff00000u;
    rsrc_t rg = mkrsrc(ws + WS_G, 2u << 20);
    f32x16 acc;
    lwait(lds0 + C_R, 8);
    { const f32x2 t = *(const LAS f32x2*)(uintptr_t)(lds0 + L_TH + wi * 256 + (l & 31) * 8);
      float rs = t[0], ms = t[1];
      TOUCH(rs); TOUCH(ms);
#pragma unroll
      for (int i = 0; i < 16; ++i) acc[i] = hres[i] * rs - ms; }
    unsigned hx = FLAG_X + hs, vx = vl + WS_X1 + tileo;
    asm volatile("" : "+{v[16:31]}"(acc), "+v"(vg), "+v"(hx), "+v"(vx)); PINS(rg);
    spoll(rws, FLAG_G + hq, FLAG_G + (rn >= 0 && rn < 64 ? (rn * 4 + wi) * 128 : hq), 4u);
    if (w == 3) { drain(); ladd(lds0 + C_S7); }
    STAMP(15);
    kstage<2>(acc, wf, rg, vg, wi * 8192, qd, qa, lm, one, st, 0u, 0u, late, rws, hx);
    STAMP(17);
    st128(rws, vx, pack8(acc, 0)); st128(rws, vx + 1024, pack8(acc, 8));
    put_stats(rws, vl, WS_STX + stato, acc);
    ladd(lds0 + C_R);
    STAMP(18);
    if (w < 3) {
      rest(so3 + 24576);
      unstage();
    }
  }
  if (wsel >= 4) {
    bf16x8 co = split_op(cs1, true, l < 32);
    unsigned spa = lds0 + L_SP + (l & 31) * 32 + wi * 8, spb = lds0 + L_SP + (l & 31) * 32 + (l >> 5) * 16, tha = lds0 + L_TH + wi * 256 + (l & 31) * 8;
    TOUCH(co); TOUCH(spa); TOUCH(spb); TOUCH(tha);
    __builtin_amdgcn_s_setprio(1);
    f32x16 acc = {};
    unsigned so = WS_H + rq * 32768 + wi * 8192, ss = WS_STH + rq * 4096 + wi * 1024;
    asm volatile("" : "+{v[16:31]}"(acc), "+v"(qd[0]), "+v"(qd[1]), "+v"(qd[2]), "+v"(qd[3]), "+v"(qa), "+v"(lm), "+v"(one)); PINS(so); PINS(ss);
    lwait(lds0 + C_R, 4);
    spoll(rws, FLAG_H + hq, FLAG_H + hq, 4u);
    STAMP(6);
    kstage<1>(acc, wf, rws, vl, so, qd, qa, lm, one, st, ss, spa, late, rws, so3);
    STAMP(50);
    if (late) {
      got1<0>(st);
#pragma unroll 1
      for (unsigned sp = 0; sp < SPIN && __any(mx4(0u, st) == MARK); ++sp) { ldw<0>(st, rws, vl, ss); got1<0>(st); }
      const f32x4 v = __builtin_bit_cast(f32x4, st);
      const float s = swapadd(v[0] + v[2]), q = swapadd(v[1] + v[3]);
      *(LAS f32x2*)(uintptr_t)(spa) = (f32x2){s, q};
      ladd(lds0 + C_S);
    }
    lwait(lds0 + C_S, 4);
    float cz;
    { const f32x4 v = lds128f(spb);
      const float mu = swapadd(v[0] + v[2]) * (1.f / D), var = fmaxf(swapadd(v[1] + v[3]) * (1.f / D) - mu * mu, 0.f);
      acc = MFMA32(co, split_op(-mu, false, true), acc);
      const float rs = __builtin_amdgcn_rsqf(var + EPS);
      cz = rs * __builtin_amdgcn_rsqf(var * rs * rs + EPS);
      *(LAS f32x2*)(uintptr_t)(tha) = (f32x2){rs, mu * rs}; }
    ladd(lds0 + C_R);
    { float k1 = GC1 * cz * cz * cz, k2 = GC2 * cz, ir = __builtin_amdgcn_rcpf(cz);
      asm volatile("s_nop 7\n\ts_nop 3" : "+v"(acc), "+v"(k1), "+v"(k2), "+v"(ir));
      const u32x4 g0 = gelu8(acc, 0, k1, k2, ir);
      hint(rws, FLAG_G + hs);
      asm volatile("s_waitcnt lgkmcnt(0)" ::: "memory");
      st128s(rws, vl, WS_G + tileo, g0);
      st128s(rws, vl + 1024, WS_G + tileo, gelu8(acc, 8, k1, k2, ir)); }
    STAMP(13);
    __builtin_amdgcn_s_setprio(0);
    unsigned ws3 = w;
    asm volatile("" : "+s"(ws3));
    if (w != 7) {
#define NE 24
#define PC3 9
      sfor<EW * 8, NE>([&](auto K) { ldw<(K.v & 3) * 1024>(wf[K.v], rws, vl, so3 + (K.v >> 2) * 4096); });
      lwait(lds0 + C_G, 4);
      STAMP(14);
      sfor<NE, 32>([&](auto K) { ldw<(K.v & 3) * 1024>(wf[K.v], rws, vl, so3 + (K.v >> 2) * 4096); waitv<PC3>(); });
    }
    if (ws3 == 7) {
      rest(so3 + 24576);
      lwait(lds0 + C_S7, 1);
      unstage();
      ladd(lds0 + C_U7);
      drain();
    }
    STAMP(20);
  }
  }
#define XQ(q) ((q) < 3 ? L_PB + (q) * 16384 : 49152)
#define STG(w) ((w) < 3 ? (w) * 16384 : (w) == 6 ? L_Z : 16384 + (w) * 12288)
#define SGO(j) (((j) & 1) * 6144 + (KSP((j) >> 1) - 6) * 1024)
#define UA(i) (L_PB + (i) * 16384)
#define UB(i) ((i) * 16384 + 10240)
#define PAO(w) ((w) == 3 ? 49152 : (w) == 7 ? 90112 : STG(w))
#define PBO(w) ((w) < 6 ? L_PB + (w) * 8192 : (w) == 6 ? 57344 : 8192)
#define TAB8(F) ((F(0) >> 10) | (F(1) >> 10) << 8 | (F(2) >> 10) << 16 | (unsigned long long)(F(3) >> 10) << 24 | (unsigned long long)(F(4) >> 10) << 32 | (unsigned long long)(F(5) >> 10) << 40 | \
                 (unsigned long long)(F(6) >> 10) << 48 | (unsigned long long)(F(7) >> 10) << 56)
#define LOOKUP(F, w) (((unsigned)(TAB8(F) >> ((w) * 8)) & 255u) << 10)
#define KSP(p) ((p) < 8 ? (((p) & 3) < 3 ? ((p) & 3) : 6) * 2 + ((p) >> 2) : (3 + ((p) - 8) % 3) * 2 + ((p) - 8) / 3)
#define W4OFF(j) ((KSP((j) >> 1) & 3) * 1024)
#define W4REL(j) (((j) & 1) * 114688 + (KSP((j) >> 1) >> 2) * 4096)
#define W4SO(j) (so4 + W4REL(j))
#define SY 28
#define SL 16
#define QA 2
#define QB 0
#define W3D 0
#define Q3 4
#define WSTR 128
#define WRM 0
#define PD_ 5
  {
    const int l = lane();
    const int h = l >> 5, l31 = l & 31;
    const unsigned vl = l * 16;
    const unsigned so4 = TW_4 + (c8 * 2 * 112 + w * 14) * 1024;
    const unsigned bb = lds0 + vl;
    const unsigned fw = FLAG_U + (rb * 8 + w) * 128, ub = WS_U + (rb * 8 + w) * 28672;
    UPart g;
    const unsigned fj = FLAG_U + (rb * 8 + (c8 & 3) * 2) * 128;
    unsigned dum = 0u;
    auto warm = [&](unsigned o, unsigned n) {
      const unsigned so = WS_U + (rb * 8 + (c8 & 3) * 2) * 28672 + o, z = vl * (WSTR / 16);
      const unsigned vo = z < n ? z : 0x7ff00000u;
      asm volatile("buffer_load_dword %0, %1, %2, %3 offen sc1\n\tbuffer_load_dword %0, %1, %2, %4 offen sc1" : "+v"(dum) : "v"(vo), "s"(rws), "s"(so), "s"(so + 28672) : "memory");
    };
    u32x16 wq[4];
    u32x2 rr[4];
    const unsigned xf = sbit<0x87>(w), xn = sbit<0x70>(w), turn = sbit<0x7f>(w), x3 = sbit<0x08>(w), xa = sbit<0x07>(w), xh = sbit<0xf0>(w), x7 = sbit<0x80>(w);
    unsigned ws2 = w;
    asm volatile("" : "+s"(ws2));
    const unsigned q1 = (w + 1) & 3, ys = WS_X1 + rb * 65536 + 32768 + q1 * 8192, yd = bb + XQ(q1) + 8192, fy = FLAG_X + (rb * 8 + 4 + q1) * 128;
    const unsigned vs = h * 2048 + l31 * 16, ss = WS_STX + rb * 8192;
    auto stats = [&](bf16x8 (&t)[4], auto RT, auto N) {
      constexpr int rt = RT.v;
      take4<N.v, 0, 512, 1024, 1536>(t, rws, vs, ss + rt * 4096, ss + rt * 4096);
      float mu, va;
      fin_stats(t, mu, va);
      const float ve = va + EPS, rs = __builtin_amdgcn_rsqf(ve);
      *(LAS f32x4*)(uintptr_t)(lds0 + L_TX + rt * 512 + l31 * 16) = (f32x4){-mu, GC1 * rs * rs * rs, GC2 * rs, ve * rs};
      ladd(lds0 + C_ST);
      STAMP(26 + rt);
    };
    unsigned f2 = 0u;
    auto ldwq = [&](auto F, auto T, unsigned on) { sfor<F.v, T.v>([&](auto K) { constexpr int j = K.v * 4; ldq<K.v, W4OFF(j), W4OFF(j + 2)>(wq[K.v], rws, vl, W4SO(j), W4SO(j + 1), W4SO(j + 2), W4SO(j + 3), on); }); };
    auto stage = [&](unsigned t) {
      const unsigned st = TW_4 + (c8 * 2 * 112 + t * 14 + 6) * 1024, d = lds0 + LOOKUP(PAO, t);
      dmaN<4>(rws, vl, st, d); dmaN<2>(rws, vl, st + 4096, d + 4096); dmaN<4>(rws, vl, st + 114688, d + 6144); dmaN<2>(rws, vl, st + 114688 + 4096, d + 10240);
    };
    undefq(wq);
#define ST0 12
#define STD 6
    if (xa) ladd(lds0 + C_X);
    const unsigned sts = so4 + 6144, std_ = lds0 + w * 16384;
    sfor<0, 4>([&](auto K) { ldwq(K, IC<K.v + 1>{}, (K.v < QA ? xf : x7) | (K.v < QB ? xn : 0u)); });
    const unsigned uo = WS_U + (rb * 8 + c8) * 28672 + (xf ? wi : w + 4) * 1024 + vl, uh = xf ? 4096 : 3072, fu = FLAG_U + (rb * 8 + c8) * 128 + xn * 4;
    unsigned b0 = bb + L_PB, b1 = bb + 49152, ba = bb + LOOKUP(PAO, w), ta = lds0 + L_TX + l31 * 16;
    bf16x8 co = split_op(*(const LAS float*)(uintptr_t)(lds0 + L_CS + w * 128 + l31 * 4), true, l < 32);
    const unsigned fr = w * 4 + (l >> 4), fc = l & 15;
    unsigned pr = lds0 + fc * 512 + (fr ^ fc) * 16, pb = (bb + LOOKUP(PBO, w)) ^ (h * 16), oo = ((rb * 64 + fr) * D + c8 * 64 + fc * 4) * 2;
    const unsigned cy = lds0 + C_Y + wi * 4;
    unsigned ucc = bb + UB(wi), laa = lds0 + C_D + xf * (C_Y - C_D + wi * 4), und = xf * turn * 4u + xn * 2u;
    unsigned sor = rb * 65536 + c8 * 4096 + w * 64 + WS_H;
    TOUCH(b0); TOUCH(b1); TOUCH(ba); TOUCH(ta); TOUCH(co); TOUCH(pr); TOUCH(pb); TOUCH(oo); TOUCH(ucc); PINS(laa); PINS(und); PINS(sor);
    auto second = [&](bf16x8 (&f0)[4], bf16x8 (&f1)[4], auto N) {
      take4<N.v + 4, 0, 1024, 2048, 3072>(f0, rws, vl, ys, ys);
      sfor<0, 4>([&](auto K) { *(LAS bf16x8*)(uintptr_t)(yd + K.v * 1024) = f0[K.v]; });
      take4<N.v, 0, 1024, 2048, 3072>(f1, rws, vl, ys + 4096, ys + 4096);
      sfor<0, 4>([&](auto K) { *(LAS bf16x8*)(uintptr_t)(yd + 4096 + K.v * 1024) = f1[K.v]; });
      ladd(lds0 + C_X + 4);
      STAMP(19);
    };
    STAMP(44);
    __builtin_amdgcn_s_setprio(2);
    if (wi == 3) {
      const unsigned xs = WS_X1 + rb * 65536 + 24576 + x7 * 4096, xd = bb + XQ(3) + x7 * 4096;
      bf16x8 f0[4], s0[4];
      const unsigned cr = lcnt(lds0 + C_R), c7 = lcnt(lds0 + C_U7);
      f2 = spoll1(rws, FLAG_X + (rb * 8 + 3) * 128, fy, 4u);
      STAMP(21);
      asm volatile("" : "=v"(s0[0]), "=v"(s0[1]), "=v"(s0[2]), "=v"(s0[3]));
      ld4s(s0, rws, vs, ss, x3);
      sfor<0, 4>([&](auto K) { ldn<K.v * 1024>(f0[K.v], rws, vl, xs); });
      if (rfl(cr) < 12u) lwait(lds0 + C_R, 12u);
      if (rfl(c7) < 1u) lwait(lds0 + C_U7, 1u);
      stage4<0>(f0, rws, vl, xs, xd);
      ladd(lds0 + C_X);
      keepq(wq);
      STAMP(31);
      if (x3) {
        bf16x8 s1[4], f1[4];
        if (f2 < 4u) { stats(s0, IC<0>{}, IC<0>{}); spoll(rws, fy, fy, 4u); }
        sfor<0, 4>([&](auto K) { ldn<K.v * 1024>(f0[K.v], rws, vl, ys); });
        sfor<0, 4>([&](auto K) { ldn<K.v * 1024>(f1[K.v], rws, vl, ys + 4096); });
        ld_stats(s1, rws, vs, ss + 4096);
        if (f2 >= 4u) stats(s0, IC<0>{}, IC<12>{});
        second(f0, f1, IC<4>{});
        stats(s1, IC<1>{}, IC<0>{});
        undefq(wq);
      }
    }
    if (xn) {
      const unsigned xs = WS_X1 + rb * 65536 + wi * 8192, xd = bb + L_PB + wi * 16384;
      bf16x8 f0[4], f1[4];
      const unsigned cr = lcnt(lds0 + C_R);
      f2 = spoll1(rws, FLAG_X + (rb * 8 + wi) * 128, fy, 4u);
      STAMP(21);
      sfor<0, 4>([&](auto K) { ldn<K.v * 1024>(f0[K.v], rws, vl, xs); });
      sfor<0, 4>([&](auto K) { ldn<K.v * 1024>(f1[K.v], rws, vl, xs + 4096); });
      if (rfl(cr) < 12u) lwait(lds0 + C_R, 12u);
      stage4<4>(f0, rws, vl, xs, xd);
      stage4<0>(f1, rws, vl, xs + 4096, xd + 4096);
      ladd(lds0 + C_X);
      STAMP(31);
      if (f2 < 4u) spoll(rws, fy, fy, 4u);
      sfor<0, 4>([&](auto K) { ldn<K.v * 1024>(f0[K.v], rws, vl, ys); });
      sfor<0, 4>([&](auto K) { ldn<K.v * 1024>(f1[K.v], rws, vl, ys + 4096); });
      if (w == 6) lwait(lds0 + C_U7, 1u);
      second(f0, f1, IC<0>{});
    }
#define QB2 4
    ldwq(IC<QB>{}, IC<QB2>{}, xn);
    auto ldres = [&]() {
      unsigned z = vl;
      TOUCH(z);
      ld4x2(rr, rws, (z & 0xc0) * 16 + (z & 0x10) * 32 + ((z & 0x20) >> 2) + ((z >> 8) << 4), sor, sor + (WS_X1 - WS_H), sor + 32768, sor + (WS_X1 - WS_H) + 32768);
    };
    u32x4 fl;
    unsigned on0 = 0u;
    unsigned ws4 = w;
    asm volatile("" : "+s"(ws4));
    if (ws4 != 3) {
      constexpr int PD = PD_;
      f32x16 a;
      bf16x8 f[PD + 1];
      const f32x16 zero = {};
      f32x4 tb;
      unsigned cn = 0;
      auto adr = [&](auto S_) { constexpr int s = S_.v, q = (s & 31) >> 3; return (q < 3 ? b0 + q * 16384 : b1) + (s & 7) * 1024 + (s >> 5) * 8192; };
      sfor<0, 2>([&](auto RT) {
        constexpr int rt = RT.v;
        lwait(lds0 + C_X + rt * 4, rt ? 4 : 8);
        if constexpr (rt == 1) { if (xn) lwait(lds0 + C_D, 1); }
        STAMP(32 + rt * 11);
        sfor<0, PD>([&](auto K) { f[K.v] = lds128(adr(IC<rt * 32 + K.v>{})); });
        if constexpr (rt == 0) { if (xn) lwait(cy, 1); } else lwait(cy, (2 + xn) * turn);
        STAMP(9 + rt);
        __builtin_amdgcn_s_setprio(3);
        sfor<0, 32>([&](auto S_) {
          constexpr int s = S_.v, n = s + PD;
          if constexpr (rt == 0 && s == ST0) dmaNif<4>(rws, vl, sts, std_, xa);
          if constexpr (rt == 0 && s == ST0 + STD) dmaNif<2>(rws, vl, sts + 4096, std_ + 4096, xa);
          if constexpr (rt == 0 && s == 25) waitv<4 * QA + 6>();
          if constexpr (s == (rt ? 2 : 24)) { if (rfl(cn) < rt + 1) lwait(lds0 + C_ST, rt + 1); tb = lds128f(ta + rt * 512); }
          SB();
          a = MFMA32(wf[s], f[s % (PD + 1)], s ? a : zero); TOUCH(a);
          if constexpr (n < 32) f[n % (PD + 1)] = lds128(adr(IC<rt * 32 + n>{}));
          if constexpr (s == SY) ladd(cy);
          if constexpr (s == (rt ? 0 : 21)) cn = lcnt(lds0 + C_ST);
          if constexpr (s == (rt ? 6 : 29)) { a = MFMA32(co, split_op(tb[0], false, true), a); TOUCH(a); }
          if constexpr (rt == 1 && !(s & 1) && s >= 2 && s <= 24) ldsw<SGO(15 + s / 2)>(wf[s / 2 - 1], ba);
          if constexpr (rt == 1 && s == SL) fl = sld4(rws, fw);
          SB();
        });
        STAMP(22 + rt * 11);
        __builtin_amdgcn_s_setprio(0);
        if constexpr (rt == 0) {
          dmaNif<4>(rws, vl, sts + 114688, std_ + 6144, xa); dmaNif<2>(rws, vl, sts + 114688 + 4096, std_ + 10240, xa);
          asm volatile("s_nop 7\n\ts_nop 4" : "+v"(a));
          st128(rws, uo, gelu_0<0>(a, tb));
          hint(rws, fu);
          st128(rws, uo + uh, gelu_0<1>(a, tb));
        } else {
          ld12if(wf, rws, vl, so4, x7);
          undefu(g);
          sgot4(fl);
          on0 = xf & sge<4>(fl[0]);
          PINS(on0);
          asm volatile("s_nop 7" : "+v"(a));
          st128(rws, uo + 14336, gelu_u(a, tb, g, rws, vl, ub, on0));
          hint(rws, fu + 8);
          sld4if(fl, rws, fw, xf);
          st128(rws, uo + 14336 + uh, gelu_0<1>(a, tb));
        }
        STAMP(23 + rt);
        if constexpr (rt == 0) {
          sfor<(QA < QB2 ? QA : QB2), 4>([&](auto K) { ldwq(K, IC<K.v + 1>{}, (K.v >= QA ? xa : 0u) | (K.v >= QB2 ? xn : 0u)); });
          waitdyn2<19 - 4 * QA, 63>(xn * 63u);
        }
      });
      ldres();
    }
    if (ws2 == 3) {
      __builtin_amdgcn_s_setprio(0);
      if (W3D) waity<1, 1>(lds0 + C_Y); else lwait(lds0 + C_X, 8);
      STAMP(33);
#pragma unroll 1
      for (unsigned i = 4; i < 7; ++i) stage(i);
      STAMP(22);
      unsigned one_ = 1u, q[4];
      PINS(one_);
      const unsigned fa = FLAG_U + (rb * 8 + 3) * 128, u0 = WS_U + (rb * 8 + 4) * 28672, t0 = c8 % 3, t1 = (c8 + 1) % 3, t2 = (c8 + 2) % 3;
      sq_ld(q, rws, fa);
      ldwq(IC<0>{}, IC<Q3>{}, one_);
      waitv<Q3 * 4>(); ladd(lds0 + C_D);
      STAMP(23);
      waity<2, 1>(lds0 + C_Y);
      STAMP(25);
      if (sq_min(q) < 4u) spollq(rws, fa, 4u);
      STAMP(46);
      undefu(g);
      tile_dma(rws, vl, u0 + t0 * 28672, lds0 + UA(t0)); tile_dma(rws, vl, u0 + t1 * 28672, lds0 + UA(t1)); tile_dma(rws, vl, u0 + t2 * 28672, lds0 + UA(t2));
      STAMP(47);
      sq_ld(q, rws, fa + 4);
      ldwq(IC<Q3>{}, IC<4>{}, one_);
      waity<3, 2>(lds0 + C_Y);
      if (sq_min(q) < 3u) spollq(rws, fa + 4, 3u);
      STAMP(49);
#pragma unroll 1
      for (unsigned i = 0; i < 3; ++i) { const unsigned t = (i + c8) % 3; dmaN<4>(rws, vl, u0 + t * 28672 + 8192, lds0 + UB(t)); dmaN<2>(rws, vl, u0 + t * 28672 + 12288, lds0 + UB(t) + 4096); }
      STAMP(45);
      ld12if(wf, rws, vl, so4, one_);
      ldres();
      waitv<16>(); ladd(lds0 + C_D);
      STAMP(48);
      __builtin_amdgcn_raw_buffer_store_b64(tag, rws, b == 0 && l == 0 ? WS_EP : 0x7ff00000u, 0, 16);
      ld8if(g, rws, vl, ub, one_);
      ld6if(g, rws, vl, ub + 8192, one_);
      fl = sld4(rws, fw);
    }
    __builtin_amdgcn_s_setprio(1);
    unsigned pr1, pr2;
    f32x16 c[2];
    auto res = [&](int rt) {
      const u32x2 a = rr[2 * rt], b = rr[2 * rt + 1];
      return (f32x4){bflo(a[0]) + bflo(b[0]), bfhi(a[0]) + bfhi(b[0]), bflo(a[1]) + bflo(b[1]), bfhi(a[1]) + bfhi(b[1])};
    };
    auto fin = [&](auto RT, const f32x4& t) {
      constexpr int rt = RT.v;
      f32x4 p[8];
#pragma unroll
      for (int k = 0; k < 8; ++k) { const unsigned o = rt ? PBO(k) : PAO(k); p[k] = lds128f(o < 65536 ? pr + o : o < L_PB ? pr1 + (o - 65536) : pr2 + (o - L_PB)); }
      const f32x4 v = (((p[0] + t) + p[1]) + (p[2] + p[3])) + ((p[4] + p[5]) + (p[6] + p[7]));
      return (u32x2){pk2(v[0], v[1]), pk2(v[2], v[3])};
    };
    rsrc_t ro = mkrsrc(out, S * D * 2);
    auto sto = [&](int rt, u32x2 v) { __builtin_amdgcn_raw_buffer_store_b64(v, ro, oo, rt * 32 * D * 2, 0); };
    STAMP(39);
    sgot4(fl);
    unsigned f3, m3, cpa = lds0 + C_PA;
    TOUCH(cpa);
    ba ^= (vl >> 5) & 16;
    uthree(c[0], c[1], g, wq, wf, rws, vl, ub, fw, xf + xn * 2u + (on0 ^ 1u) * 4u + sge<4>(fl[0]) * 8u + sge<3>(fl[1]) * 16u, laa, und, ucc + (UA(0) - UB(0)), ucc, ba, cpa, f3, m3);
    STAMP(38);
    const unsigned ca = lcnt(cpa);
    sgot(f3);
    if (f3 < 3u) spoll(rws, fw + 12, fw + 12, 3u);
    ld6if(g, rws, vl, ub + 22528, m3);
    keep4(rr);
    pr1 = pr + 65536; pr2 = pr + L_PB;
    f32x4 t0 = res(0), t1 = res(1);
    TOUCH(t0); TOUCH(t1); TOUCH(pr); TOUCH(pr1); TOUCH(pr2); TOUCH(pb); TOUCH(oo); PINS(ro);
    f32x16 d0, d1;
    unsigned did = sge<8>(rfl(ca)), dd = did;
    asm volatile("" : "+s"(dd));
    if (did) {
      sto(0, fin(IC<0>{}, t0));
      STAMP(30);
      ufin<1>(c[0], c[1], d0, d1, g, wf, rws, vl, ub + 22528, pb);
    }
    if (!dd) {
      ufin<0>(c[0], c[1], d0, d1, g, wf, rws, vl, ub + 22528, pb);
      STAMP(29);
      lwait(cpa, 8);
      uput(d0, d1, pb);
      sto(0, fin(IC<0>{}, t0));
    }
    STAMP(34);
    BAR();
    STAMP(35);
    sto(1, fin(IC<1>{}, t1));
  }
  STAMPC(42); STAMP(28);
  { const int l = lane(); (void)l; STDUMP(0); STDUMP(1); }
  asm volatile("kernel_end:");
}

extern "C" void kernel_launch(void* const* d_in, const int* in_sizes, int n_in, void* d_out, int out_size, void* d_ws, size_t ws_size, hipStream_t stream) {
  const u16* x = (const u16*)d_in[0]; const u16* wqkv = (const u16*)d_in[1]; const u16* wo = (const u16*)d_in[2];
  const u16* w1 = (const u16*)d_in[4]; const u16* w2 = (const u16*)d_in[5]; const u16* w3 = (const u16*)d_in[6]; const u16* w4 = (const u16*)d_in[7];
  (void)hipFuncSetAttribute((const void*)k_main, hipFuncAttributeMaxDynamicSharedMemorySize, LDS_BYTES);
  k_main<<<256, 512, LDS_BYTES, stream>>>(x, wqkv, wo, w1, w2, w3, w4, (u16*)d_out, (char*)d_ws);
}
```
